```python
import torch

def _softmax(x, axis=-1):
    return torch.softmax(x, dim=axis)
def _gelu(x):
    return torch.nn.functional.gelu(x, approximate="tanh")
def _silu(x):
    return torch.nn.functional.silu(x)
def _sigmoid(x):
    return torch.sigmoid(x)
def _layernorm(x, axis=-1, eps=1e-5):
    m = x.mean(dim=axis, keepdim=True)
    v = x.var(dim=axis, keepdim=True, unbiased=False)
    return (x - m) / torch.sqrt(v + eps)
def _scatter_add(tgt, idx, src, axis=0):
    return tgt.clone().index_add_(axis, idx, src)
def _segment_sum(src, seg, n):
    out = torch.zeros((n,) + src.shape[1:], dtype=src.dtype)
    return out.index_add_(0, seg, src)
def _pad(x, pw):
    flat = tuple(v for pair in reversed(pw) for v in pair)
    return torch.nn.functional.pad(x, flat)

kernel_name = 'parallel_sum__sliding_local_dilated__glu_gated_bilinear__prompt'

def setup_inputs():

    _w = (lambda a: (lambda t: t.to(torch.bfloat16) if t.is_floating_point() else t)(torch.as_tensor(a)))
    def _A_setup():
        B, S, D = 1, 2048, 1024
        return {
            "x": _w((lambda *s: torch.randn(*s, dtype=torch.bfloat16) * 0.05)(B, S, D)),
            "wqkv": _w((lambda *s: torch.randn(*s, dtype=torch.bfloat16) * 0.05)(D, 3 * D)),
            "wo": _w((lambda *s: torch.randn(*s, dtype=torch.bfloat16) * 0.05)(D, D)),
        }
    _A_in = _A_setup()

    def _B_setup():
        B, S, D, F = 1, 2048, 1024, 3584
        return {
            "x": _w((lambda *s: torch.randn(*s, dtype=torch.bfloat16) * 0.05)(B, S, D)),
            "w_gate": _w((lambda *s: torch.randn(*s, dtype=torch.bfloat16) * 0.8)(D, F) * (D ** -0.5)),
            "w_up": _w((lambda *s: torch.randn(*s, dtype=torch.bfloat16) * 0.8)(D, F) * (D ** -0.5)),
            "w_down": _w((lambda *s: torch.randn(*s, dtype=torch.bfloat16) * 0.8)(F, D) * (F ** -0.5)),
        }
    _B_in = _B_setup()

    return {
        "A_x": _A_in["x"],
        "A_wqkv": _A_in["wqkv"],
        "A_wo": _A_in["wo"],
        "B_x": _B_in["x"],
        "B_w_gate": _B_in["w_gate"],
        "B_w_up": _B_in["w_up"],
        "B_w_down": _B_in["w_down"],
    }

def reference(A_x, A_wqkv, A_wo, B_x, B_w_gate, B_w_up, B_w_down):
    def _A_ref(x, wqkv, wo):
        B, S, D = x.shape
        H = 32; Dh = D // H
        W, dil = 8, 2
        qkv = (lambda x, *d: x.permute(*d))((x @ wqkv).reshape(B, S, 3, H, Dh), 2, 0, 3, 1, 4)
        q, k, v = qkv[0], qkv[1], qkv[2]
        scale = Dh ** -0.5
        pos = torch.arange(S)
        off = torch.arange(W) * dil
        idx = pos[:, None] - off[None, :]
        mask = idx >= 0
        idx_c = torch.where(mask, idx, 0).reshape(-1)
        kg = k[:, :, idx_c, :].reshape(B, H, S, W, Dh)
        vg = v[:, :, idx_c, :].reshape(B, H, S, W, Dh)
        logits = torch.einsum("bhsd,bhswd->bhsw", q, kg) * scale
        logits = torch.where(mask[None, None, :, :], logits, -1e9)
        attn = _softmax(logits, axis=-1)
        o = torch.einsum("bhsw,bhswd->bhsd", attn, vg)
        return (lambda x, *d: x.permute(*d))(o, 0, 2, 1, 3).reshape(B, S, D) @ wo

    def _B_ref(x, w_gate, w_up, w_down):
        g = torch.einsum("bsd,df->bsf", x, w_gate)
        u = torch.einsum("bsd,df->bsf", x, w_up)
        h = g * u
        return torch.einsum("bsf,fd->bsd", h, w_down)

    x = A_x
    xn = _layernorm(x, axis=-1)
    return x + _A_ref(xn, A_wqkv, A_wo) + _B_ref(xn, B_w_gate, B_w_up, B_w_down)
```

```cpp
#include <hip/hip_runtime.h>
#include <hip/hip_bf16.h>
typedef unsigned short u16;
typedef short bf16x8 __attribute__((ext_vector_type(8)));
typedef float f32x4 __attribute__((ext_vector_type(4)));
__device__ __forceinline__ float bf2f(u16 v){ return __uint_as_float(((unsigned)v)<<16); }
__device__ __forceinline__ u16 f2bf(float f){ unsigned u=__float_as_uint(f); u += 0x7fff + ((u>>16)&1); return (u16)(u>>16); }
constexpr int S=2048, D=1024, F=3584, H=32, DH=32;

__global__ void ln_kernel(const u16* x, u16* xn){
  int row=blockIdx.x; int t=threadIdx.x;
  const u16* p=x+(size_t)row*D;
  float v[4]; float s=0;
  for(int i=0;i<4;i++){ v[i]=bf2f(p[t*4+i]); s+=v[i]; }
  __shared__ float red[8];
  for(int o=32;o>0;o>>=1) s+=__shfl_xor(s,o);
  if((t&63)==0) red[t>>6]=s; __syncthreads();
  float m=(red[0]+red[1]+red[2]+red[3])/D; __syncthreads();
  float q=0; for(int i=0;i<4;i++){ float d=v[i]-m; q+=d*d; }
  for(int o=32;o>0;o>>=1) q+=__shfl_xor(q,o);
  if((t&63)==0) red[t>>6]=q; __syncthreads();
  float var=(red[0]+red[1]+red[2]+red[3])/D;
  float r=1.0f/sqrtf(var+1e-5f);
  for(int i=0;i<4;i++) xn[(size_t)row*D+t*4+i]=f2bf((v[i]-m)*r);
}
template<int MODE>
__global__ void gemm_kernel(const u16* A,int lda,const u16* B,int ldb,int K, u16* C,int ldc, const u16* B2, const u16* resid, float* facc){
  __shared__ u16 As[64][40]; __shared__ u16 Bs[64][40]; __shared__ u16 Bs2[64][40];
  int t=threadIdx.x, w=t>>6, l=t&63; int wr=w>>1, wc=w&1;
  int m0=blockIdx.y*64, n0=blockIdx.x*64;
  f32x4 acc[2][2]={}; f32x4 acc2[2][2]={};
  for(int k0=0;k0<K;k0+=32){
    for(int i=t;i<64*32;i+=256){ int r=i>>5,c=i&31; As[r][c]=A[(size_t)(m0+r)*lda+k0+c]; }
    for(int i=t;i<64*32;i+=256){ int k=i>>6,n=i&63; Bs[n][k]=B[(size_t)(k0+k)*ldb+n0+n]; if(MODE==1) Bs2[n][k]=B2[(size_t)(k0+k)*ldb+n0+n]; }
    __syncthreads();
    for(int m=0;m<2;m++) for(int n=0;n<2;n++){
      bf16x8 a=*(const bf16x8*)&As[wr*32+m*16+(l&15)][(l>>4)*8];
      bf16x8 b=*(const bf16x8*)&Bs[wc*32+n*16+(l&15)][(l>>4)*8];
      acc[m][n]=__builtin_amdgcn_mfma_f32_16x16x32_bf16(a,b,acc[m][n],0,0,0);
      if(MODE==1){ bf16x8 b2=*(const bf16x8*)&Bs2[wc*32+n*16+(l&15)][(l>>4)*8]; acc2[m][n]=__builtin_amdgcn_mfma_f32_16x16x32_bf16(a,b2,acc2[m][n],0,0,0);} }
    __syncthreads();
  }
  for(int m=0;m<2;m++) for(int n=0;n<2;n++) for(int j=0;j<4;j++){
    int r=m0+wr*32+m*16+(l>>4)*4+j, c=n0+wc*32+n*16+(l&15);
    float v=acc[m][n][j];
    if(MODE==0) C[(size_t)r*ldc+c]=f2bf(v);
    if(MODE==1) C[(size_t)r*ldc+c]=f2bf(v*acc2[m][n][j]);
    if(MODE==2) C[(size_t)r*ldc+c]=f2bf(v+bf2f(resid[(size_t)r*ldc+c]));
  }
}
__global__ void attn_kernel(const u16* qkv, u16* o,int ldo){
  int idx=blockIdx.x*blockDim.x+threadIdx.x; int s=idx>>5, h=idx&31;
  const u16* q=qkv+(size_t)s*3072+h*32;
  float lg[8]; float mx=-1e30f;
  for(int w=0;w<8;w++){ int p=s-2*w; if(p<0){lg[w]=-1e9f;} else { const u16* k=qkv+(size_t)p*3072+1024+h*32; float a=0; for(int d=0;d<32;d++) a+=bf2f(q[d])*bf2f(k[d]); lg[w]=a*0.17677669529663687f;} mx=fmaxf(mx,lg[w]); }
  float sum=0; for(int w=0;w<8;w++){ lg[w]=__expf(lg[w]-mx); sum+=lg[w]; }
  float inv=1.0f/sum;
  for(int d=0;d<32;d++){ float a=0; for(int w=0;w<8;w++){ int p=s-2*w; if(p>=0) a+=lg[w]*bf2f(qkv[(size_t)p*3072+2048+h*32+d]); } o[(size_t)s*ldo+h*32+d]=f2bf(a*inv); }
}
__global__ void concat_w(const u16* wo,const u16* wd,u16* w2){ size_t i=(size_t)blockIdx.x*256+threadIdx.x; if(i<(size_t)1024*1024) w2[i]=wo[i]; else w2[i]=wd[i-(size_t)1024*1024]; }
extern "C" void kernel_launch(void* const* d_in, const int* in_sizes, int n_in, void* d_out, int out_size, void* d_ws, size_t ws_size, hipStream_t stream){
  const u16* x=(const u16*)d_in[0]; const u16* wqkv=(const u16*)d_in[1]; const u16* wo=(const u16*)d_in[2];
  const u16* wg=(const u16*)d_in[4]; const u16* wu=(const u16*)d_in[5]; const u16* wd=(const u16*)d_in[6];
  u16* ws=(u16*)d_ws; u16* xn=ws; u16* qkv=xn+S*D; u16* a2=qkv+(size_t)S*3072; u16* w2=a2+(size_t)S*4608;
  ln_kernel<<<S,256,0,stream>>>(x,xn);
  gemm_kernel<0><<<dim3(3072/64,S/64),256,0,stream>>>(xn,D,wqkv,3072,D,qkv,3072,nullptr,nullptr,nullptr);
  gemm_kernel<1><<<dim3(F/64,S/64),256,0,stream>>>(xn,D,wg,F,D,a2+1024,4608,wu,nullptr,nullptr);
  attn_kernel<<<S*H/256,256,0,stream>>>(qkv,a2,4608);
  concat_w<<<4608*1024/256,256,0,stream>>>(wo,wd,w2);
  gemm_kernel<2><<<dim3(D/64,S/64),256,0,stream>>>(a2,4608,w2,1024,4608,(u16*)d_out,1024,nullptr,x,nullptr);
}
```

```cpp
#include <hip/hip_runtime.h>

typedef unsigned u32x2v __attribute__((ext_vector_type(2)));
typedef unsigned u32x4v __attribute__((ext_vector_type(4)));
typedef unsigned u32x8v __attribute__((ext_vector_type(8)));
typedef unsigned u32x16v __attribute__((ext_vector_type(16)));
typedef unsigned u32x32v __attribute__((ext_vector_type(32)));
#define LO(x) ((unsigned)(unsigned long long)(x))
#define HI(x) ((unsigned)((unsigned long long)(x)>>32))
#define RF(x) ((unsigned)__builtin_amdgcn_readfirstlane((int)(x)))
#define SET64(V,i,x) do{ unsigned long long _p=(unsigned long long)(x); V[i]=RF(LO(_p)); V[i+1]=RF(HI(_p)); }while(0)
__global__ __launch_bounds__(256,1) void fused_kernel(const char* __restrict__ x, char* ws, char* __restrict__ out, const char* __restrict__ wqkv,
    const char* __restrict__ wg, const char* __restrict__ wu, const char* __restrict__ wo, const char* __restrict__ wd){
  const int tid=threadIdx.x, l=tid&63; const int w=__builtin_amdgcn_readfirstlane(tid>>6);
  const int wr=w>>1, wc=w&1;
  const int bid=blockIdx.x, xcd=bid&7, idx=bid>>3;
  const int typM=xcd&1, rg=4*((xcd>>1)&1)+(idx>>3), ti=8*(xcd>>2)+(idx&7);
  const int rb2=2*rg+(ti&1), ct=ti>>1;
  const int g=l>>4, q=(l&15)>>2, p=l&3, h=g>>1, r=4*(g&1)+q, f=r>>1, i=l&15;
  unsigned GV; u32x2v ST={0u,0u};
  char* xnF=ws; char* a2f=ws+(8<<20); char* part=ws+(32<<20); char* flags=ws+(48<<20);
  u32x16v P8;
  { const int b32=typM*16+ti, rowl=b32*8+wc*4;
    char* lnf=flags+20480;
    SET64(P8,0,x+(size_t)(rg*256+rowl)*2048);
    SET64(P8,2,xnF+(size_t)rg*524288+(rowl>>4)*1024+(rowl&15)*16);
    SET64(P8,4,lnf+rg*256); SET64(P8,6,lnf+(rg>0?rg-1:0)*256+240);
    SET64(P8,8,flags+24576); P8[10]=RF((b32*2+wc)*4); P8[11]=RF(bid==1?1:0);
    SET64(P8,12,lnf); P8[14]=0; P8[15]=0;
  }
  asm volatile(R"ASM(
v_mov_b32 v29, 0
global_load_dword v28, v29, s[24:25] sc1
s_cmp_lt_u32 %[w], 2
s_cbranch_scc0 Lnox_%=
v_mov_b32 v26, %[l]
v_and_b32 v27, 3, v26
v_lshlrev_b32 v24, 11, v27
v_lshlrev_b32 v25, 4, v27
v_bfe_u32 v27, v26, 2, 2
v_lshl_add_u32 v24, v27, 3, v24
v_lshl_add_u32 v25, v27, 8, v25
v_lshrrev_b32 v27, 4, v26
v_lshl_add_u32 v24, v27, 6, v24
v_lshl_add_u32 v25, v27, 14, v25
global_load_dwordx2 v[128:129], v24, s[16:17] offset:0
global_load_dwordx2 v[130:131], v24, s[16:17] offset:32
global_load_dwordx2 v[132:133], v24, s[16:17] offset:256
global_load_dwordx2 v[134:135], v24, s[16:17] offset:288
global_load_dwordx2 v[136:137], v24, s[16:17] offset:512
global_load_dwordx2 v[138:139], v24, s[16:17] offset:544
global_load_dwordx2 v[140:141], v24, s[16:17] offset:768
global_load_dwordx2 v[142:143], v24, s[16:17] offset:800
global_load_dwordx2 v[144:145], v24, s[16:17] offset:1024
global_load_dwordx2 v[146:147], v24, s[16:17] offset:1056
global_load_dwordx2 v[148:149], v24, s[16:17] offset:1280
global_load_dwordx2 v[150:151], v24, s[16:17] offset:1312
global_load_dwordx2 v[152:153], v24, s[16:17] offset:1536
global_load_dwordx2 v[154:155], v24, s[16:17] offset:1568
global_load_dwordx2 v[156:157], v24, s[16:17] offset:1792
global_load_dwordx2 v[158:159], v24, s[16:17] offset:1824
s_waitcnt vmcnt(0)
s_waitcnt vmcnt(16)
v_readfirstlane_b32 s98, v28
s_nop 3
s_add_u32 s98, s98, 1
v_lshlrev_b32 v64, 16, v128
v_and_b32 v65, 0xffff0000, v128
v_lshlrev_b32 v66, 16, v129
v_and_b32 v67, 0xffff0000, v129
v_lshlrev_b32 v68, 16, v130
v_and_b32 v69, 0xffff0000, v130
v_lshlrev_b32 v70, 16, v131
v_and_b32 v71, 0xffff0000, v131
v_lshlrev_b32 v72, 16, v132
v_and_b32 v73, 0xffff0000, v132
v_lshlrev_b32 v74, 16, v133
v_and_b32 v75, 0xffff0000, v133
v_lshlrev_b32 v76, 16, v134
v_and_b32 v77, 0xffff0000, v134
v_lshlrev_b32 v78, 16, v135
v_and_b32 v79, 0xffff0000, v135
v_lshlrev_b32 v80, 16, v136
v_and_b32 v81, 0xffff0000, v136
v_lshlrev_b32 v82, 16, v137
v_and_b32 v83, 0xffff0000, v137
v_lshlrev_b32 v84, 16, v138
v_and_b32 v85, 0xffff0000, v138
v_lshlrev_b32 v86, 16, v139
v_and_b32 v87, 0xffff0000, v139
v_lshlrev_b32 v88, 16, v140
v_and_b32 v89, 0xffff0000, v140
v_lshlrev_b32 v90, 16, v141
v_and_b32 v91, 0xffff0000, v141
v_lshlrev_b32 v92, 16, v142
v_and_b32 v93, 0xffff0000, v142
v_lshlrev_b32 v94, 16, v143
v_and_b32 v95, 0xffff0000, v143
v_lshlrev_b32 v96, 16, v144
v_and_b32 v97, 0xffff0000, v144
v_lshlrev_b32 v98, 16, v145
v_and_b32 v99, 0xffff0000, v145
v_lshlrev_b32 v100, 16, v146
v_and_b32 v101, 0xffff0000, v146
v_lshlrev_b32 v102, 16, v147
v_and_b32 v103, 0xffff0000, v147
v_lshlrev_b32 v104, 16, v148
v_and_b32 v105, 0xffff0000, v148
v_lshlrev_b32 v106, 16, v149
v_and_b32 v107, 0xffff0000, v149
v_lshlrev_b32 v108, 16, v150
v_and_b32 v109, 0xffff0000, v150
v_lshlrev_b32 v110, 16, v151
v_and_b32 v111, 0xffff0000, v151
v_lshlrev_b32 v112, 16, v152
v_and_b32 v113, 0xffff0000, v152
v_lshlrev_b32 v114, 16, v153
v_and_b32 v115, 0xffff0000, v153
v_lshlrev_b32 v116, 16, v154
v_and_b32 v117, 0xffff0000, v154
v_lshlrev_b32 v118, 16, v155
v_and_b32 v119, 0xffff0000, v155
v_lshlrev_b32 v120, 16, v156
v_and_b32 v121, 0xffff0000, v156
v_lshlrev_b32 v122, 16, v157
v_and_b32 v123, 0xffff0000, v157
v_lshlrev_b32 v124, 16, v158
v_and_b32 v125, 0xffff0000, v158
v_lshlrev_b32 v126, 16, v159
v_and_b32 v127, 0xffff0000, v159
v_pk_add_f32 v[40:41], v[64:65], v[72:73]
v_pk_add_f32 v[42:43], v[66:67], v[74:75]
v_pk_add_f32 v[44:45], v[68:69], v[76:77]
v_pk_add_f32 v[46:47], v[70:71], v[78:79]
v_pk_add_f32 v[40:41], v[40:41], v[80:81]
v_pk_add_f32 v[42:43], v[42:43], v[82:83]
v_pk_add_f32 v[44:45], v[44:45], v[84:85]
v_pk_add_f32 v[46:47], v[46:47], v[86:87]
v_pk_add_f32 v[40:41], v[40:41], v[88:89]
v_pk_add_f32 v[42:43], v[42:43], v[90:91]
v_pk_add_f32 v[44:45], v[44:45], v[92:93]
v_pk_add_f32 v[46:47], v[46:47], v[94:95]
v_pk_add_f32 v[40:41], v[40:41], v[96:97]
v_pk_add_f32 v[42:43], v[42:43], v[98:99]
v_pk_add_f32 v[44:45], v[44:45], v[100:101]
v_pk_add_f32 v[46:47], v[46:47], v[102:103]
v_pk_add_f32 v[40:41], v[40:41], v[104:105]
v_pk_add_f32 v[42:43], v[42:43], v[106:107]
v_pk_add_f32 v[44:45], v[44:45], v[108:109]
v_pk_add_f32 v[46:47], v[46:47], v[110:111]
v_pk_add_f32 v[40:41], v[40:41], v[112:113]
v_pk_add_f32 v[42:43], v[42:43], v[114:115]
v_pk_add_f32 v[44:45], v[44:45], v[116:117]
v_pk_add_f32 v[46:47], v[46:47], v[118:119]
v_pk_add_f32 v[40:41], v[40:41], v[120:121]
v_pk_add_f32 v[42:43], v[42:43], v[122:123]
v_pk_add_f32 v[44:45], v[44:45], v[124:125]
v_pk_add_f32 v[46:47], v[46:47], v[126:127]
v_pk_add_f32 v[40:41], v[40:41], v[42:43]
v_pk_add_f32 v[44:45], v[44:45], v[46:47]
v_pk_add_f32 v[40:41], v[40:41], v[44:45]
v_add_f32 v40, v40, v41
s_nop 1
v_add_f32_dpp v40, v40, v40 row_ror:8 row_mask:0xf bank_mask:0xf
s_nop 1
v_add_f32_dpp v40, v40, v40 row_ror:4 row_mask:0xf bank_mask:0xf
v_mov_b32 v41, v40
s_nop 1
v_permlane16_swap_b32 v40, v41
v_add_f32 v40, v40, v41
v_mov_b32 v41, v40
s_nop 1
v_permlane32_swap_b32 v40, v41
v_add_f32 v40, v40, v41
v_mul_f32 v48, 0xba800000, v40
v_mov_b32 v49, v48
v_pk_add_f32 v[64:65], v[64:65], v[48:49]
v_pk_add_f32 v[66:67], v[66:67], v[48:49]
v_pk_add_f32 v[68:69], v[68:69], v[48:49]
v_pk_add_f32 v[70:71], v[70:71], v[48:49]
v_pk_add_f32 v[72:73], v[72:73], v[48:49]
v_pk_add_f32 v[74:75], v[74:75], v[48:49]
v_pk_add_f32 v[76:77], v[76:77], v[48:49]
v_pk_add_f32 v[78:79], v[78:79], v[48:49]
v_pk_add_f32 v[80:81], v[80:81], v[48:49]
v_pk_add_f32 v[82:83], v[82:83], v[48:49]
v_pk_add_f32 v[84:85], v[84:85], v[48:49]
v_pk_add_f32 v[86:87], v[86:87], v[48:49]
v_pk_add_f32 v[88:89], v[88:89], v[48:49]
v_pk_add_f32 v[90:91], v[90:91], v[48:49]
v_pk_add_f32 v[92:93], v[92:93], v[48:49]
v_pk_add_f32 v[94:95], v[94:95], v[48:49]
v_pk_add_f32 v[96:97], v[96:97], v[48:49]
v_pk_add_f32 v[98:99], v[98:99], v[48:49]
v_pk_add_f32 v[100:101], v[100:101], v[48:49]
v_pk_add_f32 v[102:103], v[102:103], v[48:49]
v_pk_add_f32 v[104:105], v[104:105], v[48:49]
v_pk_add_f32 v[106:107], v[106:107], v[48:49]
v_pk_add_f32 v[108:109], v[108:109], v[48:49]
v_pk_add_f32 v[110:111], v[110:111], v[48:49]
v_pk_add_f32 v[112:113], v[112:113], v[48:49]
v_pk_add_f32 v[114:115], v[114:115], v[48:49]
v_pk_add_f32 v[116:117], v[116:117], v[48:49]
v_pk_add_f32 v[118:119], v[118:119], v[48:49]
v_pk_add_f32 v[120:121], v[120:121], v[48:49]
v_pk_add_f32 v[122:123], v[122:123], v[48:49]
v_pk_add_f32 v[124:125], v[124:125], v[48:49]
v_pk_add_f32 v[126:127], v[126:127], v[48:49]
v_pk_mul_f32 v[40:41], v[64:65], v[64:65]
v_pk_mul_f32 v[42:43], v[66:67], v[66:67]
v_pk_mul_f32 v[44:45], v[68:69], v[68:69]
v_pk_mul_f32 v[46:47], v[70:71], v[70:71]
v_pk_fma_f32 v[40:41], v[72:73], v[72:73], v[40:41]
v_pk_fma_f32 v[42:43], v[74:75], v[74:75], v[42:43]
v_pk_fma_f32 v[44:45], v[76:77], v[76:77], v[44:45]
v_pk_fma_f32 v[46:47], v[78:79], v[78:79], v[46:47]
v_pk_fma_f32 v[40:41], v[80:81], v[80:81], v[40:41]
v_pk_fma_f32 v[42:43], v[82:83], v[82:83], v[42:43]
v_pk_fma_f32 v[44:45], v[84:85], v[84:85], v[44:45]
v_pk_fma_f32 v[46:47], v[86:87], v[86:87], v[46:47]
v_pk_fma_f32 v[40:41], v[88:89], v[88:89], v[40:41]
v_pk_fma_f32 v[42:43], v[90:91], v[90:91], v[42:43]
v_pk_fma_f32 v[44:45], v[92:93], v[92:93], v[44:45]
v_pk_fma_f32 v[46:47], v[94:95], v[94:95], v[46:47]
v_pk_fma_f32 v[40:41], v[96:97], v[96:97], v[40:41]
v_pk_fma_f32 v[42:43], v[98:99], v[98:99], v[42:43]
v_pk_fma_f32 v[44:45], v[100:101], v[100:101], v[44:45]
v_pk_fma_f32 v[46:47], v[102:103], v[102:103], v[46:47]
v_pk_fma_f32 v[40:41], v[104:105], v[104:105], v[40:41]
v_pk_fma_f32 v[42:43], v[106:107], v[106:107], v[42:43]
v_pk_fma_f32 v[44:45], v[108:109], v[108:109], v[44:45]
v_pk_fma_f32 v[46:47], v[110:111], v[110:111], v[46:47]
v_pk_fma_f32 v[40:41], v[112:113], v[112:113], v[40:41]
v_pk_fma_f32 v[42:43], v[114:115], v[114:115], v[42:43]
v_pk_fma_f32 v[44:45], v[116:117], v[116:117], v[44:45]
v_pk_fma_f32 v[46:47], v[118:119], v[118:119], v[46:47]
v_pk_fma_f32 v[40:41], v[120:121], v[120:121], v[40:41]
v_pk_fma_f32 v[42:43], v[122:123], v[122:123], v[42:43]
v_pk_fma_f32 v[44:45], v[124:125], v[124:125], v[44:45]
v_pk_fma_f32 v[46:47], v[126:127], v[126:127], v[46:47]
v_pk_add_f32 v[40:41], v[40:41], v[42:43]
v_pk_add_f32 v[44:45], v[44:45], v[46:47]
v_pk_add_f32 v[40:41], v[40:41], v[44:45]
v_add_f32 v40, v40, v41
s_nop 1
v_add_f32_dpp v40, v40, v40 row_ror:8 row_mask:0xf bank_mask:0xf
s_nop 1
v_add_f32_dpp v40, v40, v40 row_ror:4 row_mask:0xf bank_mask:0xf
v_mov_b32 v41, v40
s_nop 1
v_permlane16_swap_b32 v40, v41
v_add_f32 v40, v40, v41
v_mov_b32 v41, v40
s_nop 1
v_permlane32_swap_b32 v40, v41
v_add_f32 v40, v40, v41
v_mov_b32 v49, 0x3727c5ac
v_fmac_f32 v49, 0x3a800000, v40
v_rsq_f32 v48, v49
s_nop 0
v_mov_b32 v49, v48
v_pk_mul_f32 v[64:65], v[64:65], v[48:49]
v_pk_mul_f32 v[66:67], v[66:67], v[48:49]
v_pk_mul_f32 v[68:69], v[68:69], v[48:49]
v_pk_mul_f32 v[70:71], v[70:71], v[48:49]
v_cvt_pk_bf16_f32 v128, v64, v65
v_cvt_pk_bf16_f32 v129, v66, v67
v_cvt_pk_bf16_f32 v130, v68, v69
v_cvt_pk_bf16_f32 v131, v70, v71
global_store_dwordx4 v25, v[128:131], s[18:19] sc1
s_add_u32 s18, s18, 65536
s_addc_u32 s19, s19, 0
v_pk_mul_f32 v[72:73], v[72:73], v[48:49]
v_pk_mul_f32 v[74:75], v[74:75], v[48:49]
v_pk_mul_f32 v[76:77], v[76:77], v[48:49]
v_pk_mul_f32 v[78:79], v[78:79], v[48:49]
v_cvt_pk_bf16_f32 v132, v72, v73
v_cvt_pk_bf16_f32 v133, v74, v75
v_cvt_pk_bf16_f32 v134, v76, v77
v_cvt_pk_bf16_f32 v135, v78, v79
global_store_dwordx4 v25, v[132:135], s[18:19] sc1
s_add_u32 s18, s18, 65536
s_addc_u32 s19, s19, 0
v_pk_mul_f32 v[80:81], v[80:81], v[48:49]
v_pk_mul_f32 v[82:83], v[82:83], v[48:49]
v_pk_mul_f32 v[84:85], v[84:85], v[48:49]
v_pk_mul_f32 v[86:87], v[86:87], v[48:49]
v_cvt_pk_bf16_f32 v136, v80, v81
v_cvt_pk_bf16_f32 v137, v82, v83
v_cvt_pk_bf16_f32 v138, v84, v85
v_cvt_pk_bf16_f32 v139, v86, v87
global_store_dwordx4 v25, v[136:139], s[18:19] sc1
s_add_u32 s18, s18, 65536
s_addc_u32 s19, s19, 0
v_pk_mul_f32 v[88:89], v[88:89], v[48:49]
v_pk_mul_f32 v[90:91], v[90:91], v[48:49]
v_pk_mul_f32 v[92:93], v[92:93], v[48:49]
v_pk_mul_f32 v[94:95], v[94:95], v[48:49]
v_cvt_pk_bf16_f32 v140, v88, v89
v_cvt_pk_bf16_f32 v141, v90, v91
v_cvt_pk_bf16_f32 v142, v92, v93
v_cvt_pk_bf16_f32 v143, v94, v95
global_store_dwordx4 v25, v[140:143], s[18:19] sc1
s_add_u32 s18, s18, 65536
s_addc_u32 s19, s19, 0
v_pk_mul_f32 v[96:97], v[96:97], v[48:49]
v_pk_mul_f32 v[98:99], v[98:99], v[48:49]
v_pk_mul_f32 v[100:101], v[100:101], v[48:49]
v_pk_mul_f32 v[102:103], v[102:103], v[48:49]
v_cvt_pk_bf16_f32 v144, v96, v97
v_cvt_pk_bf16_f32 v145, v98, v99
v_cvt_pk_bf16_f32 v146, v100, v101
v_cvt_pk_bf16_f32 v147, v102, v103
global_store_dwordx4 v25, v[144:147], s[18:19] sc1
s_add_u32 s18, s18, 65536
s_addc_u32 s19, s19, 0
v_pk_mul_f32 v[104:105], v[104:105], v[48:49]
v_pk_mul_f32 v[106:107], v[106:107], v[48:49]
v_pk_mul_f32 v[108:109], v[108:109], v[48:49]
v_pk_mul_f32 v[110:111], v[110:111], v[48:49]
v_cvt_pk_bf16_f32 v148, v104, v105
v_cvt_pk_bf16_f32 v149, v106, v107
v_cvt_pk_bf16_f32 v150, v108, v109
v_cvt_pk_bf16_f32 v151, v110, v111
global_store_dwordx4 v25, v[148:151], s[18:19] sc1
s_add_u32 s18, s18, 65536
s_addc_u32 s19, s19, 0
v_pk_mul_f32 v[112:113], v[112:113], v[48:49]
v_pk_mul_f32 v[114:115], v[114:115], v[48:49]
v_pk_mul_f32 v[116:117], v[116:117], v[48:49]
v_pk_mul_f32 v[118:119], v[118:119], v[48:49]
v_cvt_pk_bf16_f32 v152, v112, v113
v_cvt_pk_bf16_f32 v153, v114, v115
v_cvt_pk_bf16_f32 v154, v116, v117
v_cvt_pk_bf16_f32 v155, v118, v119
global_store_dwordx4 v25, v[152:155], s[18:19] sc1
s_add_u32 s18, s18, 65536
s_addc_u32 s19, s19, 0
v_pk_mul_f32 v[120:121], v[120:121], v[48:49]
v_pk_mul_f32 v[122:123], v[122:123], v[48:49]
v_pk_mul_f32 v[124:125], v[124:125], v[48:49]
v_pk_mul_f32 v[126:127], v[126:127], v[48:49]
v_cvt_pk_bf16_f32 v156, v120, v121
v_cvt_pk_bf16_f32 v157, v122, v123
v_cvt_pk_bf16_f32 v158, v124, v125
v_cvt_pk_bf16_f32 v159, v126, v127
global_store_dwordx4 v25, v[156:159], s[18:19] sc1
v_mov_b32 v26, s26
v_mov_b32 v27, s98
s_waitcnt vmcnt(0)
s_mov_b64 exec, 1
global_store_dword v26, v27, s[20:21] sc1
s_mov_b64 exec, -1
Lnox_%=:
)ASM"
    : "={v28}"(GV), "+{s[16:31]}"(P8), "+{v[6:7]}"(ST)
    : [l]"v"(l), [w]"s"(w), "s"(out), "s"(wqkv), "s"(wg), "s"(wu), "s"(wo), "s"(wd)
    : "memory","scc","vcc","s64","s65","s66","s67","s98","v24","v25","v26","v27","v29","v40","v41","v42","v43","v44","v45","v46","v47","v48","v49","v64","v65","v66","v67","v68","v69","v70","v71","v72","v73","v74","v75","v76","v77","v78","v79","v80","v81","v82","v83","v84","v85","v86","v87","v88","v89","v90","v91","v92","v93","v94","v95","v96","v97","v98","v99","v100","v101","v102","v103","v104","v105","v106","v107","v108","v109","v110","v111","v112","v113","v114","v115","v116","v117","v118","v119","v120","v121","v122","v123","v124","v125","v126","v127","v128","v129","v130","v131","v132","v133","v134","v135","v136","v137","v138","v139","v140","v141","v142","v143","v144","v145","v146","v147","v148","v149","v150","v151","v152","v153","v154","v155","v156","v157","v158","v159");
  u32x16v PV, P1, P3; u32x8v P2, P7; u32x4v P4, P6, PFV;
  const unsigned swz=((((l&7)>>1)^(l>>4))*32)+(l&1)*16;
  const size_t rb0=(size_t)(rg*16+wr*8)*147456;
  PV[0]=wr*8192+l*16;
  PV[1]=65536+h*3072+r*128+8*p+32*((2*wc)^f);
  PV[2]=typM?(65536+2048+(4*g+q)*64+32*(g&1)+8*p):((wr?7*1024:65536+12288)+l*16);
  PV[3]=((l>>3)+8*w)*7168+swz;
  PV[4]=w*4096+l*16; PV[5]=15*1024+w*256+l*4;
  PV[6]=((l>>3)+8*w)*(typM?7168:6144)+swz;
  { const int row16=l>>2; PV[7]=(row16+16*(w&1))*7168+((((l&3)>>1)^((row16>>2)&1))*32)+(l&1)*16; }
  PV[8]=l*16; PV[9]=l*16+147456;
  {
    const unsigned full2=h*4096+r*128+8*p;
    const unsigned vgb=(8*w+(l>>3))*2048+swz+4096;
    PV[10]=vgb; PV[11]=vgb+128-1024; PV[12]=vgb+32*2048-2048; PV[13]=vgb+32*2048+128-3072;
    PV[14]=full2+32*f;
    PV[15]=i*2048+(typM?8*g:((g&1)*16+(g>>1)*8)*2);
  }
  SET64(P1,0,xnF+(size_t)rg*524288); SET64(P1,2,xnF+(size_t)(rg>0?rg-1:0)*524288);
  {
    const int h0=1024+96*ti, odd=ti&1; const int f0=h0+(odd?32:0), hh=h0+(odd?0:64);
    SET64(P1,4,typM?wg+f0*2:wqkv+ti*128);
    SET64(P1,6,(typM?wu+f0*2:wqkv+ti*128+2048)-1024);
    SET64(P1,8,(typM?(((w>>1)?wu:wg)+hh*2):wqkv+ti*128+4096)-2048);
    { const int ps=16*(typM?7168:6144);
      SET64(P7,0,wr?(typM?(wc?wu:wg):wd):xnF+(size_t)rg*524288+8192);
      SET64(P7,2,wr?wo:(typM?wg+f0*2:wqkv+ti*128)+ps);
      SET64(P7,4,wr?wqkv:(typM?wu+f0*2:wqkv+ti*128+2048)-1024+ps);
      SET64(P7,6,wr?wu:(typM?(wu+hh*2):wqkv+ti*128+4096+ps)-2048); }
    const int c2=(typM?2560:0)+64*ti;
    SET64(P1,10,wg+c2*2); SET64(P1,12,wu+c2*2-1024);
    P1[14]=RF(w*4096); P1[15]=RF(65536+w*3072);
    P2[0]=RF(65536+12288+w*256); P2[1]=RF((typM?0:64)+ti*4);
    const int kbf=32+f0/32+wc, kbh=32+hh/32;
    SET64(P2,2,a2f+rb0+(typM?(kbf*1024-2048+4*wc*147456):(2*ti+wc)*1024));
    SET64(P2,4,a2f+rb0+(size_t)(32+c2/32+wc)*1024);
    P2[6]=0; P2[7]=0;
    P6[2]=RF((rg==0&&wr==0)?0u:1u);
    P6[3]=RF((unsigned)((kbh-kbf)*1024+2048));
  }
  {
    const char* pa=a2f+(size_t)(rb2*8+2*w)*147456;
    const char* wdp=wd+ct*256-4096; const char* wop=wo+ct*256-4096;
    const size_t tile=(size_t)(rb2*128+32*w)*2048+ct*256;
    SET64(P3,0,pa+(typM?96:64)*1024); SET64(P3,2,wdp+(size_t)((typM?96:64)-32)*65536);
    SET64(P3,4,typM?wop:wdp+(size_t)96*65536); SET64(P3,6,pa+(typM?0:128*1024));
    SET64(P3,8,typM?wdp+(size_t)80*65536:wdp); SET64(P3,10,pa+(typM?112:32)*1024);
    SET64(P3,12,(typM?x:(const char*)out)+tile);
    SET64(P3,14,part+(size_t)(rb2*8+ct)*65536+w*16384);
    SET64(P6,0,flags+(rb2*8+ct)*128);
    SET64(P4,0,flags+16384+rg*128); P4[2]=0; P4[3]=RF(w*4096);
  }
  {
    const int bt=idx*4+(xcd>>1); const int n=bt*64+l, c=n&31;
    const unsigned xm=(n>>5)*7168+((c&16)?5120:0)+(c&15)*128, xq=((bt*2+(w&1))*64+l)*128, mk=0u-(unsigned)typM;
    PFV[0]=(xm&mk)|(xq&~mk); PFV[1]=(n>>5)*7168+2048+c*128; PFV[2]=((bt*7168+2048)&mk)|((bt*6144+(w&1)*3072)&~mk); PFV[2]+=(l<23?l:23)*128; PFV[3]=(idx*64+l)*128;
  }
  asm volatile(R"ASM(
.macro M d,a,b,c
v_mfma_f32_16x16x32_bf16 \d,\a,\b,\c
.endm
.macro T d,a,o
ds_read_b64_tr_b16 \d,\a offset:\o
.endm
.macro X d,a,o
ds_read_b128 \d,\a offset:\o
.endm
Lcode0_%=:
s_cmp_lt_u32 s57, 64
s_cbranch_scc1 LtypM_%=
s_cmp_lt_u32 s99, 8192
s_cbranch_scc1 LrolLQ_%=
s_nop 1
v_xor_b32 v32, 32, v9
s_sleep 40
global_load_dword v5, v38, s[12:13]
s_add_u32 s66, s12, 0
s_addc_u32 s67, s13, 0
v_writelane_b32 v38, s66, 0
v_writelane_b32 v38, s67, 16
s_add_u32 s66, s12, 2097152
s_addc_u32 s67, s13, 0
v_writelane_b32 v38, s66, 1
v_writelane_b32 v38, s67, 17
s_add_u32 s66, s12, 4194304
s_addc_u32 s67, s13, 0
v_writelane_b32 v38, s66, 2
v_writelane_b32 v38, s67, 18
s_add_u32 s66, s8, 0
s_addc_u32 s67, s9, 0
v_writelane_b32 v38, s66, 3
v_writelane_b32 v38, s67, 19
s_add_u32 s66, s8, 2097152
s_addc_u32 s67, s9, 0
v_writelane_b32 v38, s66, 4
v_writelane_b32 v38, s67, 20
s_add_u32 s66, s8, 4194304
s_addc_u32 s67, s9, 0
v_writelane_b32 v38, s66, 5
v_writelane_b32 v38, s67, 21
s_add_u32 s66, s8, 5242880
s_addc_u32 s67, s9, 0
v_writelane_b32 v38, s66, 6
v_writelane_b32 v38, s67, 22
s_add_u32 s66, s10, 0
s_addc_u32 s67, s11, 0
v_writelane_b32 v38, s66, 7
v_writelane_b32 v38, s67, 23
s_cmp_eq_u32 s99, 12288
s_cbranch_scc1 LpolQ_%=
s_waitcnt vmcnt(0)
v_readfirstlane_b32 s98, v28
s_nop 3
s_add_u32 s98, s98, 1
s_getpc_b64 s[66:67]
LpcQ_%=:
s_sub_u32 s66, s66, LpcQ_%=-Lcode0_%=
s_subb_u32 s67, s67, 0
v_min_u32 v39, Lcode1_%=-Lcode0_%=-128, v39
global_load_dword v5, v39, s[66:67]
v_add_u32 v24, 8192, v36
s_branch LpjQ_%=
LpolQ_%=:
s_waitcnt vmcnt(0)
v_readfirstlane_b32 s98, v28
s_nop 3
s_add_u32 s98, s98, 1
v_lshrrev_b32 v24, 2, v16
v_and_b32 v25, 12, v24
s_mov_b32 s65, 0x100000
LplQ_%=:
global_load_dword v26, v24, s[20:21] sc1
global_load_dword v27, v25, s[22:23] sc1
s_waitcnt vmcnt(0)
v_xor_b32 v26, s98, v26
v_xor_b32 v27, s98, v27
v_or_b32 v26, v26, v27
v_cmp_ne_u32 vcc, 0, v26
s_cbranch_vccz LplokQ_%=
s_sub_u32 s65, s65, 1
s_cmp_lg_u32 s65, 0
s_cbranch_scc1 LplQ_%=
LplokQ_%=:
LpjQ_%=:
v_add_u32 v13, 0x4000, v13
v_add_u32 v12, 0x4000, v12
v_add_u32 v14, 196608, v14
v_add_u32 v13, 0x4000, v13
v_add_u32 v12, 0x4000, v12
v_add_u32 v14, 196608, v14
v_add_u32 v13, 0x4000, v13
v_add_u32 v12, 0x4000, v12
v_add_u32 v14, 196608, v14
v_add_u32 v13, 0x4000, v13
v_add_u32 v12, 0x4000, v12
v_add_u32 v14, 196608, v14
s_barrier
s_nop 1
v_lshrrev_b32 v34, 2, v16
v_and_b32 v34, 124, v34
v_add_u32 v39, 32768, v23
s_barrier
X a[64:67],v10,0
X a[0:3],v8,0
X a[4:7],v8,1024
X a[8:11],v8,2048
X a[12:15],v8,3072
X a[16:19],v8,4096
X a[20:23],v8,5120
X a[24:27],v8,6144
X a[28:31],v8,7168
T a[72:73],v9,1024
T a[74:75],v9,7168
T a[76:77],v32,1024
T a[78:79],v32,7168
T a[80:81],v9,2048
T a[82:83],v9,8192
T a[84:85],v32,2048
T a[86:87],v32,8192
T a[88:89],v9,0
T a[90:91],v9,6144
s_waitcnt lgkmcnt(8)
M v[128:131],a[72:75],a[0:3],0
T a[92:93],v32,0
M v[132:135],a[72:75],a[4:7],0
T a[94:95],v32,6144
M v[136:139],a[72:75],a[8:11],0
M v[140:143],a[72:75],a[12:15],0
M v[144:147],a[72:75],a[16:19],0
M v[148:151],a[72:75],a[20:23],0
M v[152:155],a[72:75],a[24:27],0
M v[156:159],a[72:75],a[28:31],0
M v[48:51],a[72:75],a[64:67],0
s_waitcnt lgkmcnt(0)
s_barrier
M v[160:163],a[76:79],a[0:3],0
T a[72:73],v9,17408
M v[164:167],a[76:79],a[4:7],0
T a[74:75],v9,23552
M v[168:171],a[76:79],a[8:11],0
X a[68:71],v10,16384
M v[172:175],a[76:79],a[12:15],0
v_add_u32 v13, 0x4000, v13
v_readlane_b32 s66, v38, 0
M v[176:179],a[76:79],a[16:19],0
X a[32:35],v8,16384
M v[180:183],a[76:79],a[20:23],0
v_add_u32 v12, 0x4000, v12
v_readlane_b32 s67, v38, 16
M v[184:187],a[76:79],a[24:27],0
v_add_u32 v14, 196608, v14
M v[188:191],a[76:79],a[28:31],0
s_nop 4
global_load_dword v5, v36, s[66:67]
M v[52:55],a[76:79],a[64:67],0
X a[36:39],v8,17408
M v[192:195],a[0:3],a[80:83],0
T a[76:77],v32,17408
M v[196:199],a[4:7],a[80:83],0
T a[78:79],v32,23552
M v[200:203],a[8:11],a[80:83],0
M v[204:207],a[12:15],a[80:83],0
X a[40:43],v8,18432
M v[208:211],a[16:19],a[80:83],0
M v[212:215],a[20:23],a[80:83],0
M v[216:219],a[24:27],a[80:83],0
M v[220:223],a[28:31],a[80:83],0
X a[44:47],v8,19456
M v[56:59],a[64:67],a[80:83],0
M v[224:227],a[0:3],a[84:87],0
T a[80:81],v9,18432
M v[228:231],a[4:7],a[84:87],0
T a[82:83],v9,24576
M v[232:235],a[8:11],a[84:87],0
X a[48:51],v8,20480
M v[236:239],a[12:15],a[84:87],0
M v[240:243],a[16:19],a[84:87],0
M v[244:247],a[20:23],a[84:87],0
M v[248:251],a[24:27],a[84:87],0
X a[52:55],v8,21504
M v[252:255],a[28:31],a[84:87],0
M v[60:63],a[64:67],a[84:87],0
M v[64:67],a[88:91],a[0:3],0
T a[84:85],v32,18432
M v[68:71],a[88:91],a[4:7],0
T a[86:87],v32,24576
M v[72:75],a[88:91],a[8:11],0
X a[56:59],v8,22528
M v[76:79],a[88:91],a[12:15],0
M v[80:83],a[88:91],a[16:19],0
M v[84:87],a[88:91],a[20:23],0
M v[88:91],a[88:91],a[24:27],0
X a[60:63],v8,23552
M v[92:95],a[88:91],a[28:31],0
M v[96:99],a[92:95],a[0:3],0
T a[88:89],v9,16384
M v[100:103],a[92:95],a[4:7],0
T a[90:91],v9,22528
M v[104:107],a[92:95],a[8:11],0
M v[108:111],a[92:95],a[12:15],0
M v[112:115],a[92:95],a[16:19],0
M v[116:119],a[92:95],a[20:23],0
M v[120:123],a[92:95],a[24:27],0
M v[124:127],a[92:95],a[28:31],0
s_waitcnt lgkmcnt(2)
M v[128:131],a[72:75],a[32:35],v[128:131]
T a[92:93],v32,16384
M v[132:135],a[72:75],a[36:39],v[132:135]
T a[94:95],v32,22528
M v[136:139],a[72:75],a[40:43],v[136:139]
M v[140:143],a[72:75],a[44:47],v[140:143]
M v[144:147],a[72:75],a[48:51],v[144:147]
M v[148:151],a[72:75],a[52:55],v[148:151]
M v[152:155],a[72:75],a[56:59],v[152:155]
M v[156:159],a[72:75],a[60:63],v[156:159]
M v[48:51],a[72:75],a[68:71],v[48:51]
s_waitcnt lgkmcnt(0)
s_barrier
M v[160:163],a[76:79],a[32:35],v[160:163]
T a[72:73],v9,33792
M v[164:167],a[76:79],a[36:39],v[164:167]
T a[74:75],v9,39936
M v[168:171],a[76:79],a[40:43],v[168:171]
X a[64:67],v10,32768
M v[172:175],a[76:79],a[44:47],v[172:175]
v_add_u32 v13, 0x4000, v13
M v[176:179],a[76:79],a[48:51],v[176:179]
X a[0:3],v8,32768
M v[180:183],a[76:79],a[52:55],v[180:183]
v_add_u32 v12, 0x4000, v12
M v[184:187],a[76:79],a[56:59],v[184:187]
v_add_u32 v14, 196608, v14
M v[188:191],a[76:79],a[60:63],v[188:191]
M v[52:55],a[76:79],a[68:71],v[52:55]
X a[4:7],v8,33792
M v[192:195],a[32:35],a[80:83],v[192:195]
T a[76:77],v32,33792
M v[196:199],a[36:39],a[80:83],v[196:199]
T a[78:79],v32,39936
M v[200:203],a[40:43],a[80:83],v[200:203]
M v[204:207],a[44:47],a[80:83],v[204:207]
X a[8:11],v8,34816
M v[208:211],a[48:51],a[80:83],v[208:211]
M v[212:215],a[52:55],a[80:83],v[212:215]
M v[216:219],a[56:59],a[80:83],v[216:219]
M v[220:223],a[60:63],a[80:83],v[220:223]
X a[12:15],v8,35840
M v[56:59],a[68:71],a[80:83],v[56:59]
M v[224:227],a[32:35],a[84:87],v[224:227]
T a[80:81],v9,34816
M v[228:231],a[36:39],a[84:87],v[228:231]
T a[82:83],v9,40960
M v[232:235],a[40:43],a[84:87],v[232:235]
X a[16:19],v8,36864
M v[236:239],a[44:47],a[84:87],v[236:239]
M v[240:243],a[48:51],a[84:87],v[240:243]
M v[244:247],a[52:55],a[84:87],v[244:247]
M v[248:251],a[56:59],a[84:87],v[248:251]
X a[20:23],v8,37888
M v[252:255],a[60:63],a[84:87],v[252:255]
M v[60:63],a[68:71],a[84:87],v[60:63]
M v[64:67],a[88:91],a[32:35],v[64:67]
T a[84:85],v32,34816
M v[68:71],a[88:91],a[36:39],v[68:71]
T a[86:87],v32,40960
M v[72:75],a[88:91],a[40:43],v[72:75]
X a[24:27],v8,38912
M v[76:79],a[88:91],a[44:47],v[76:79]
M v[80:83],a[88:91],a[48:51],v[80:83]
M v[84:87],a[88:91],a[52:55],v[84:87]
M v[88:91],a[88:91],a[56:59],v[88:91]
X a[28:31],v8,39936
M v[92:95],a[88:91],a[60:63],v[92:95]
M v[96:99],a[92:95],a[32:35],v[96:99]
T a[88:89],v9,32768
M v[100:103],a[92:95],a[36:39],v[100:103]
T a[90:91],v9,38912
M v[104:107],a[92:95],a[40:43],v[104:107]
M v[108:111],a[92:95],a[44:47],v[108:111]
M v[112:115],a[92:95],a[48:51],v[112:115]
M v[116:119],a[92:95],a[52:55],v[116:119]
M v[120:123],a[92:95],a[56:59],v[120:123]
M v[124:127],a[92:95],a[60:63],v[124:127]
s_waitcnt lgkmcnt(2)
M v[128:131],a[72:75],a[0:3],v[128:131]
T a[92:93],v32,32768
M v[132:135],a[72:75],a[4:7],v[132:135]
T a[94:95],v32,38912
M v[136:139],a[72:75],a[8:11],v[136:139]
M v[140:143],a[72:75],a[12:15],v[140:143]
M v[144:147],a[72:75],a[16:19],v[144:147]
M v[148:151],a[72:75],a[20:23],v[148:151]
M v[152:155],a[72:75],a[24:27],v[152:155]
M v[156:159],a[72:75],a[28:31],v[156:159]
M v[48:51],a[72:75],a[64:67],v[48:51]
s_waitcnt lgkmcnt(0)
s_barrier
M v[160:163],a[76:79],a[0:3],v[160:163]
T a[72:73],v9,50176
M v[164:167],a[76:79],a[4:7],v[164:167]
T a[74:75],v9,56320
M v[168:171],a[76:79],a[8:11],v[168:171]
X a[68:71],v10,49152
M v[172:175],a[76:79],a[12:15],v[172:175]
v_add_u32 v13, 0x4000, v13
v_readlane_b32 s66, v38, 1
M v[176:179],a[76:79],a[16:19],v[176:179]
X a[32:35],v8,49152
M v[180:183],a[76:79],a[20:23],v[180:183]
v_add_u32 v12, 0x4000, v12
v_readlane_b32 s67, v38, 17
M v[184:187],a[76:79],a[24:27],v[184:187]
v_add_u32 v14, 196608, v14
M v[188:191],a[76:79],a[28:31],v[188:191]
s_nop 4
global_load_dword v5, v36, s[66:67]
M v[52:55],a[76:79],a[64:67],v[52:55]
X a[36:39],v8,50176
M v[192:195],a[0:3],a[80:83],v[192:195]
T a[76:77],v32,50176
M v[196:199],a[4:7],a[80:83],v[196:199]
T a[78:79],v32,56320
M v[200:203],a[8:11],a[80:83],v[200:203]
M v[204:207],a[12:15],a[80:83],v[204:207]
X a[40:43],v8,51200
M v[208:211],a[16:19],a[80:83],v[208:211]
M v[212:215],a[20:23],a[80:83],v[212:215]
M v[216:219],a[24:27],a[80:83],v[216:219]
M v[220:223],a[28:31],a[80:83],v[220:223]
X a[44:47],v8,52224
M v[56:59],a[64:67],a[80:83],v[56:59]
M v[224:227],a[0:3],a[84:87],v[224:227]
T a[80:81],v9,51200
M v[228:231],a[4:7],a[84:87],v[228:231]
T a[82:83],v9,57344
M v[232:235],a[8:11],a[84:87],v[232:235]
X a[48:51],v8,53248
M v[236:239],a[12:15],a[84:87],v[236:239]
M v[240:243],a[16:19],a[84:87],v[240:243]
M v[244:247],a[20:23],a[84:87],v[244:247]
M v[248:251],a[24:27],a[84:87],v[248:251]
X a[52:55],v8,54272
M v[252:255],a[28:31],a[84:87],v[252:255]
M v[60:63],a[64:67],a[84:87],v[60:63]
M v[64:67],a[88:91],a[0:3],v[64:67]
T a[84:85],v32,51200
M v[68:71],a[88:91],a[4:7],v[68:71]
T a[86:87],v32,57344
M v[72:75],a[88:91],a[8:11],v[72:75]
X a[56:59],v8,55296
M v[76:79],a[88:91],a[12:15],v[76:79]
M v[80:83],a[88:91],a[16:19],v[80:83]
M v[84:87],a[88:91],a[20:23],v[84:87]
M v[88:91],a[88:91],a[24:27],v[88:91]
X a[60:63],v8,56320
M v[92:95],a[88:91],a[28:31],v[92:95]
M v[96:99],a[92:95],a[0:3],v[96:99]
T a[88:89],v9,49152
M v[100:103],a[92:95],a[4:7],v[100:103]
T a[90:91],v9,55296
M v[104:107],a[92:95],a[8:11],v[104:107]
M v[108:111],a[92:95],a[12:15],v[108:111]
M v[112:115],a[92:95],a[16:19],v[112:115]
M v[116:119],a[92:95],a[20:23],v[116:119]
M v[120:123],a[92:95],a[24:27],v[120:123]
M v[124:127],a[92:95],a[28:31],v[124:127]
s_waitcnt lgkmcnt(2)
M v[128:131],a[72:75],a[32:35],v[128:131]
T a[92:93],v32,49152
M v[132:135],a[72:75],a[36:39],v[132:135]
T a[94:95],v32,55296
M v[136:139],a[72:75],a[40:43],v[136:139]
M v[140:143],a[72:75],a[44:47],v[140:143]
M v[144:147],a[72:75],a[48:51],v[144:147]
M v[148:151],a[72:75],a[52:55],v[148:151]
M v[152:155],a[72:75],a[56:59],v[152:155]
M v[156:159],a[72:75],a[60:63],v[156:159]
M v[48:51],a[72:75],a[68:71],v[48:51]
s_waitcnt lgkmcnt(0)
s_barrier
M v[160:163],a[76:79],a[32:35],v[160:163]
T a[72:73],v9,1024
M v[164:167],a[76:79],a[36:39],v[164:167]
T a[74:75],v9,7168
M v[168:171],a[76:79],a[40:43],v[168:171]
X a[64:67],v10,0
M v[172:175],a[76:79],a[44:47],v[172:175]
v_add_u32 v13, 0x4000, v13
M v[176:179],a[76:79],a[48:51],v[176:179]
X a[0:3],v8,0
M v[180:183],a[76:79],a[52:55],v[180:183]
v_add_u32 v12, 0x4000, v12
M v[184:187],a[76:79],a[56:59],v[184:187]
v_add_u32 v14, 196608, v14
M v[188:191],a[76:79],a[60:63],v[188:191]
M v[52:55],a[76:79],a[68:71],v[52:55]
X a[4:7],v8,1024
M v[192:195],a[32:35],a[80:83],v[192:195]
T a[76:77],v32,1024
M v[196:199],a[36:39],a[80:83],v[196:199]
T a[78:79],v32,7168
M v[200:203],a[40:43],a[80:83],v[200:203]
M v[204:207],a[44:47],a[80:83],v[204:207]
X a[8:11],v8,2048
M v[208:211],a[48:51],a[80:83],v[208:211]
M v[212:215],a[52:55],a[80:83],v[212:215]
M v[216:219],a[56:59],a[80:83],v[216:219]
M v[220:223],a[60:63],a[80:83],v[220:223]
X a[12:15],v8,3072
M v[56:59],a[68:71],a[80:83],v[56:59]
M v[224:227],a[32:35],a[84:87],v[224:227]
T a[80:81],v9,2048
M v[228:231],a[36:39],a[84:87],v[228:231]
T a[82:83],v9,8192
M v[232:235],a[40:43],a[84:87],v[232:235]
X a[16:19],v8,4096
M v[236:239],a[44:47],a[84:87],v[236:239]
M v[240:243],a[48:51],a[84:87],v[240:243]
M v[244:247],a[52:55],a[84:87],v[244:247]
M v[248:251],a[56:59],a[84:87],v[248:251]
X a[20:23],v8,5120
M v[252:255],a[60:63],a[84:87],v[252:255]
M v[60:63],a[68:71],a[84:87],v[60:63]
M v[64:67],a[88:91],a[32:35],v[64:67]
T a[84:85],v32,2048
M v[68:71],a[88:91],a[36:39],v[68:71]
T a[86:87],v32,8192
M v[72:75],a[88:91],a[40:43],v[72:75]
X a[24:27],v8,6144
M v[76:79],a[88:91],a[44:47],v[76:79]
M v[80:83],a[88:91],a[48:51],v[80:83]
M v[84:87],a[88:91],a[52:55],v[84:87]
M v[88:91],a[88:91],a[56:59],v[88:91]
X a[28:31],v8,7168
M v[92:95],a[88:91],a[60:63],v[92:95]
M v[96:99],a[92:95],a[32:35],v[96:99]
T a[88:89],v9,0
M v[100:103],a[92:95],a[36:39],v[100:103]
T a[90:91],v9,6144
M v[104:107],a[92:95],a[40:43],v[104:107]
M v[108:111],a[92:95],a[44:47],v[108:111]
M v[112:115],a[92:95],a[48:51],v[112:115]
M v[116:119],a[92:95],a[52:55],v[116:119]
M v[120:123],a[92:95],a[56:59],v[120:123]
M v[124:127],a[92:95],a[60:63],v[124:127]
s_mov_b32 s64, 6
Lp1QP_%=:
s_waitcnt lgkmcnt(2)
M v[128:131],a[72:75],a[0:3],v[128:131]
T a[92:93],v32,0
M v[132:135],a[72:75],a[4:7],v[132:135]
T a[94:95],v32,6144
M v[136:139],a[72:75],a[8:11],v[136:139]
M v[140:143],a[72:75],a[12:15],v[140:143]
M v[144:147],a[72:75],a[16:19],v[144:147]
M v[148:151],a[72:75],a[20:23],v[148:151]
M v[152:155],a[72:75],a[24:27],v[152:155]
M v[156:159],a[72:75],a[28:31],v[156:159]
M v[48:51],a[72:75],a[64:67],v[48:51]
s_waitcnt lgkmcnt(0)
s_barrier
M v[160:163],a[76:79],a[0:3],v[160:163]
T a[72:73],v9,17408
M v[164:167],a[76:79],a[4:7],v[164:167]
T a[74:75],v9,23552
M v[168:171],a[76:79],a[8:11],v[168:171]
X a[68:71],v10,16384
M v[172:175],a[76:79],a[12:15],v[172:175]
v_add_u32 v13, 0x4000, v13
M v[176:179],a[76:79],a[16:19],v[176:179]
X a[32:35],v8,16384
M v[180:183],a[76:79],a[20:23],v[180:183]
v_add_u32 v12, 0x4000, v12
M v[184:187],a[76:79],a[24:27],v[184:187]
v_add_u32 v14, 196608, v14
M v[188:191],a[76:79],a[28:31],v[188:191]
M v[52:55],a[76:79],a[64:67],v[52:55]
X a[36:39],v8,17408
M v[192:195],a[0:3],a[80:83],v[192:195]
T a[76:77],v32,17408
M v[196:199],a[4:7],a[80:83],v[196:199]
T a[78:79],v32,23552
M v[200:203],a[8:11],a[80:83],v[200:203]
M v[204:207],a[12:15],a[80:83],v[204:207]
X a[40:43],v8,18432
M v[208:211],a[16:19],a[80:83],v[208:211]
M v[212:215],a[20:23],a[80:83],v[212:215]
M v[216:219],a[24:27],a[80:83],v[216:219]
M v[220:223],a[28:31],a[80:83],v[220:223]
X a[44:47],v8,19456
M v[56:59],a[64:67],a[80:83],v[56:59]
M v[224:227],a[0:3],a[84:87],v[224:227]
T a[80:81],v9,18432
M v[228:231],a[4:7],a[84:87],v[228:231]
T a[82:83],v9,24576
M v[232:235],a[8:11],a[84:87],v[232:235]
X a[48:51],v8,20480
M v[236:239],a[12:15],a[84:87],v[236:239]
M v[240:243],a[16:19],a[84:87],v[240:243]
M v[244:247],a[20:23],a[84:87],v[244:247]
M v[248:251],a[24:27],a[84:87],v[248:251]
X a[52:55],v8,21504
M v[252:255],a[28:31],a[84:87],v[252:255]
M v[60:63],a[64:67],a[84:87],v[60:63]
M v[64:67],a[88:91],a[0:3],v[64:67]
T a[84:85],v32,18432
M v[68:71],a[88:91],a[4:7],v[68:71]
T a[86:87],v32,24576
M v[72:75],a[88:91],a[8:11],v[72:75]
X a[56:59],v8,22528
M v[76:79],a[88:91],a[12:15],v[76:79]
M v[80:83],a[88:91],a[16:19],v[80:83]
M v[84:87],a[88:91],a[20:23],v[84:87]
M v[88:91],a[88:91],a[24:27],v[88:91]
X a[60:63],v8,23552
M v[92:95],a[88:91],a[28:31],v[92:95]
M v[96:99],a[92:95],a[0:3],v[96:99]
T a[88:89],v9,16384
M v[100:103],a[92:95],a[4:7],v[100:103]
T a[90:91],v9,22528
M v[104:107],a[92:95],a[8:11],v[104:107]
M v[108:111],a[92:95],a[12:15],v[108:111]
M v[112:115],a[92:95],a[16:19],v[112:115]
M v[116:119],a[92:95],a[20:23],v[116:119]
M v[120:123],a[92:95],a[24:27],v[120:123]
M v[124:127],a[92:95],a[28:31],v[124:127]
s_waitcnt lgkmcnt(2)
M v[128:131],a[72:75],a[32:35],v[128:131]
T a[92:93],v32,16384
M v[132:135],a[72:75],a[36:39],v[132:135]
T a[94:95],v32,22528
M v[136:139],a[72:75],a[40:43],v[136:139]
M v[140:143],a[72:75],a[44:47],v[140:143]
M v[144:147],a[72:75],a[48:51],v[144:147]
M v[148:151],a[72:75],a[52:55],v[148:151]
M v[152:155],a[72:75],a[56:59],v[152:155]
M v[156:159],a[72:75],a[60:63],v[156:159]
M v[48:51],a[72:75],a[68:71],v[48:51]
s_waitcnt lgkmcnt(0)
s_barrier
M v[160:163],a[76:79],a[32:35],v[160:163]
T a[72:73],v9,33792
M v[164:167],a[76:79],a[36:39],v[164:167]
T a[74:75],v9,39936
M v[168:171],a[76:79],a[40:43],v[168:171]
X a[64:67],v10,32768
M v[172:175],a[76:79],a[44:47],v[172:175]
v_add_u32 v13, 0x4000, v13
M v[176:179],a[76:79],a[48:51],v[176:179]
X a[0:3],v8,32768
M v[180:183],a[76:79],a[52:55],v[180:183]
v_add_u32 v12, 0x4000, v12
M v[184:187],a[76:79],a[56:59],v[184:187]
v_add_u32 v14, 196608, v14
M v[188:191],a[76:79],a[60:63],v[188:191]
M v[52:55],a[76:79],a[68:71],v[52:55]
X a[4:7],v8,33792
M v[192:195],a[32:35],a[80:83],v[192:195]
T a[76:77],v32,33792
M v[196:199],a[36:39],a[80:83],v[196:199]
T a[78:79],v32,39936
M v[200:203],a[40:43],a[80:83],v[200:203]
M v[204:207],a[44:47],a[80:83],v[204:207]
X a[8:11],v8,34816
M v[208:211],a[48:51],a[80:83],v[208:211]
M v[212:215],a[52:55],a[80:83],v[212:215]
M v[216:219],a[56:59],a[80:83],v[216:219]
M v[220:223],a[60:63],a[80:83],v[220:223]
X a[12:15],v8,35840
M v[56:59],a[68:71],a[80:83],v[56:59]
M v[224:227],a[32:35],a[84:87],v[224:227]
T a[80:81],v9,34816
M v[228:231],a[36:39],a[84:87],v[228:231]
T a[82:83],v9,40960
M v[232:235],a[40:43],a[84:87],v[232:235]
X a[16:19],v8,36864
M v[236:239],a[44:47],a[84:87],v[236:239]
M v[240:243],a[48:51],a[84:87],v[240:243]
M v[244:247],a[52:55],a[84:87],v[244:247]
M v[248:251],a[56:59],a[84:87],v[248:251]
X a[20:23],v8,37888
M v[252:255],a[60:63],a[84:87],v[252:255]
M v[60:63],a[68:71],a[84:87],v[60:63]
M v[64:67],a[88:91],a[32:35],v[64:67]
T a[84:85],v32,34816
M v[68:71],a[88:91],a[36:39],v[68:71]
T a[86:87],v32,40960
M v[72:75],a[88:91],a[40:43],v[72:75]
X a[24:27],v8,38912
M v[76:79],a[88:91],a[44:47],v[76:79]
M v[80:83],a[88:91],a[48:51],v[80:83]
M v[84:87],a[88:91],a[52:55],v[84:87]
M v[88:91],a[88:91],a[56:59],v[88:91]
X a[28:31],v8,39936
M v[92:95],a[88:91],a[60:63],v[92:95]
M v[96:99],a[92:95],a[32:35],v[96:99]
T a[88:89],v9,32768
M v[100:103],a[92:95],a[36:39],v[100:103]
T a[90:91],v9,38912
M v[104:107],a[92:95],a[40:43],v[104:107]
M v[108:111],a[92:95],a[44:47],v[108:111]
M v[112:115],a[92:95],a[48:51],v[112:115]
M v[116:119],a[92:95],a[52:55],v[116:119]
M v[120:123],a[92:95],a[56:59],v[120:123]
M v[124:127],a[92:95],a[60:63],v[124:127]
s_waitcnt lgkmcnt(2)
M v[128:131],a[72:75],a[0:3],v[128:131]
T a[92:93],v32,32768
M v[132:135],a[72:75],a[4:7],v[132:135]
T a[94:95],v32,38912
M v[136:139],a[72:75],a[8:11],v[136:139]
M v[140:143],a[72:75],a[12:15],v[140:143]
M v[144:147],a[72:75],a[16:19],v[144:147]
M v[148:151],a[72:75],a[20:23],v[148:151]
M v[152:155],a[72:75],a[24:27],v[152:155]
M v[156:159],a[72:75],a[28:31],v[156:159]
M v[48:51],a[72:75],a[64:67],v[48:51]
s_waitcnt lgkmcnt(0)
s_barrier
M v[160:163],a[76:79],a[0:3],v[160:163]
T a[72:73],v9,50176
M v[164:167],a[76:79],a[4:7],v[164:167]
T a[74:75],v9,56320
M v[168:171],a[76:79],a[8:11],v[168:171]
X a[68:71],v10,49152
M v[172:175],a[76:79],a[12:15],v[172:175]
v_add_u32 v13, 0x4000, v13
s_sub_u32 s65, 8, s64
M v[176:179],a[76:79],a[16:19],v[176:179]
X a[32:35],v8,49152
M v[180:183],a[76:79],a[20:23],v[180:183]
v_add_u32 v12, 0x4000, v12
v_readlane_b32 s66, v38, s65
M v[184:187],a[76:79],a[24:27],v[184:187]
v_add_u32 v14, 196608, v14
M v[188:191],a[76:79],a[28:31],v[188:191]
s_add_u32 s65, s65, 16
M v[52:55],a[76:79],a[64:67],v[52:55]
X a[36:39],v8,50176
M v[192:195],a[0:3],a[80:83],v[192:195]
T a[76:77],v32,50176
v_readlane_b32 s67, v38, s65
M v[196:199],a[4:7],a[80:83],v[196:199]
T a[78:79],v32,56320
M v[200:203],a[8:11],a[80:83],v[200:203]
s_nop 4
global_load_dword v5, v36, s[66:67]
M v[204:207],a[12:15],a[80:83],v[204:207]
X a[40:43],v8,51200
M v[208:211],a[16:19],a[80:83],v[208:211]
M v[212:215],a[20:23],a[80:83],v[212:215]
M v[216:219],a[24:27],a[80:83],v[216:219]
M v[220:223],a[28:31],a[80:83],v[220:223]
X a[44:47],v8,52224
M v[56:59],a[64:67],a[80:83],v[56:59]
M v[224:227],a[0:3],a[84:87],v[224:227]
T a[80:81],v9,51200
M v[228:231],a[4:7],a[84:87],v[228:231]
T a[82:83],v9,57344
M v[232:235],a[8:11],a[84:87],v[232:235]
X a[48:51],v8,53248
M v[236:239],a[12:15],a[84:87],v[236:239]
M v[240:243],a[16:19],a[84:87],v[240:243]
M v[244:247],a[20:23],a[84:87],v[244:247]
M v[248:251],a[24:27],a[84:87],v[248:251]
X a[52:55],v8,54272
M v[252:255],a[28:31],a[84:87],v[252:255]
M v[60:63],a[64:67],a[84:87],v[60:63]
M v[64:67],a[88:91],a[0:3],v[64:67]
T a[84:85],v32,51200
M v[68:71],a[88:91],a[4:7],v[68:71]
T a[86:87],v32,57344
M v[72:75],a[88:91],a[8:11],v[72:75]
X a[56:59],v8,55296
M v[76:79],a[88:91],a[12:15],v[76:79]
M v[80:83],a[88:91],a[16:19],v[80:83]
M v[84:87],a[88:91],a[20:23],v[84:87]
M v[88:91],a[88:91],a[24:27],v[88:91]
X a[60:63],v8,56320
M v[92:95],a[88:91],a[28:31],v[92:95]
M v[96:99],a[92:95],a[0:3],v[96:99]
T a[88:89],v9,49152
M v[100:103],a[92:95],a[4:7],v[100:103]
T a[90:91],v9,55296
M v[104:107],a[92:95],a[8:11],v[104:107]
M v[108:111],a[92:95],a[12:15],v[108:111]
M v[112:115],a[92:95],a[16:19],v[112:115]
M v[116:119],a[92:95],a[20:23],v[116:119]
M v[120:123],a[92:95],a[24:27],v[120:123]
M v[124:127],a[92:95],a[28:31],v[124:127]
s_waitcnt lgkmcnt(2)
M v[128:131],a[72:75],a[32:35],v[128:131]
T a[92:93],v32,49152
M v[132:135],a[72:75],a[36:39],v[132:135]
T a[94:95],v32,55296
M v[136:139],a[72:75],a[40:43],v[136:139]
M v[140:143],a[72:75],a[44:47],v[140:143]
M v[144:147],a[72:75],a[48:51],v[144:147]
M v[148:151],a[72:75],a[52:55],v[148:151]
M v[152:155],a[72:75],a[56:59],v[152:155]
M v[156:159],a[72:75],a[60:63],v[156:159]
M v[48:51],a[72:75],a[68:71],v[48:51]
s_waitcnt lgkmcnt(0)
s_barrier
M v[160:163],a[76:79],a[32:35],v[160:163]
T a[72:73],v9,1024
M v[164:167],a[76:79],a[36:39],v[164:167]
T a[74:75],v9,7168
M v[168:171],a[76:79],a[40:43],v[168:171]
X a[64:67],v10,0
M v[172:175],a[76:79],a[44:47],v[172:175]
v_add_u32 v13, 0x4000, v13
M v[176:179],a[76:79],a[48:51],v[176:179]
X a[0:3],v8,0
M v[180:183],a[76:79],a[52:55],v[180:183]
v_add_u32 v12, 0x4000, v12
M v[184:187],a[76:79],a[56:59],v[184:187]
v_add_u32 v14, 196608, v14
M v[188:191],a[76:79],a[60:63],v[188:191]
M v[52:55],a[76:79],a[68:71],v[52:55]
X a[4:7],v8,1024
M v[192:195],a[32:35],a[80:83],v[192:195]
T a[76:77],v32,1024
M v[196:199],a[36:39],a[80:83],v[196:199]
T a[78:79],v32,7168
M v[200:203],a[40:43],a[80:83],v[200:203]
M v[204:207],a[44:47],a[80:83],v[204:207]
X a[8:11],v8,2048
M v[208:211],a[48:51],a[80:83],v[208:211]
M v[212:215],a[52:55],a[80:83],v[212:215]
M v[216:219],a[56:59],a[80:83],v[216:219]
M v[220:223],a[60:63],a[80:83],v[220:223]
X a[12:15],v8,3072
M v[56:59],a[68:71],a[80:83],v[56:59]
M v[224:227],a[32:35],a[84:87],v[224:227]
T a[80:81],v9,2048
M v[228:231],a[36:39],a[84:87],v[228:231]
T a[82:83],v9,8192
M v[232:235],a[40:43],a[84:87],v[232:235]
X a[16:19],v8,4096
M v[236:239],a[44:47],a[84:87],v[236:239]
M v[240:243],a[48:51],a[84:87],v[240:243]
M v[244:247],a[52:55],a[84:87],v[244:247]
M v[248:251],a[56:59],a[84:87],v[248:251]
X a[20:23],v8,5120
M v[252:255],a[60:63],a[84:87],v[252:255]
M v[60:63],a[68:71],a[84:87],v[60:63]
M v[64:67],a[88:91],a[32:35],v[64:67]
T a[84:85],v32,2048
M v[68:71],a[88:91],a[36:39],v[68:71]
T a[86:87],v32,8192
M v[72:75],a[88:91],a[40:43],v[72:75]
X a[24:27],v8,6144
M v[76:79],a[88:91],a[44:47],v[76:79]
M v[80:83],a[88:91],a[48:51],v[80:83]
M v[84:87],a[88:91],a[52:55],v[84:87]
M v[88:91],a[88:91],a[56:59],v[88:91]
X a[28:31],v8,7168
M v[92:95],a[88:91],a[60:63],v[92:95]
M v[96:99],a[92:95],a[32:35],v[96:99]
T a[88:89],v9,0
M v[100:103],a[92:95],a[36:39],v[100:103]
T a[90:91],v9,6144
M v[104:107],a[92:95],a[40:43],v[104:107]
M v[108:111],a[92:95],a[44:47],v[108:111]
M v[112:115],a[92:95],a[48:51],v[112:115]
M v[116:119],a[92:95],a[52:55],v[116:119]
s_sub_u32 s64, s64, 1
M v[120:123],a[92:95],a[56:59],v[120:123]
s_cmp_lg_u32 s64, 0
M v[124:127],a[92:95],a[60:63],v[124:127]
s_cbranch_scc1 Lp1QP_%=
s_branch LmrgQ_%=
LrolLQ_%=:
s_nop 1
v_xor_b32 v32, 32, v9
v_readfirstlane_b32 s98, v28
s_nop 3
s_add_u32 s98, s98, 1
s_add_u32 m0, s55, 0
s_nop 0
global_load_lds_dwordx4 v14, s[44:45] offset:0
global_load_lds_dwordx4 v14, s[46:47] offset:1024
global_load_lds_dwordx4 v14, s[48:49] offset:2048
s_add_u32 m0, s55, 6144
s_nop 0
global_load_lds_dwordx4 v14, s[10:11] offset:0
global_load_lds_dwordx4 v14, s[12:13] offset:1024
global_load_lds_dwordx4 v14, s[14:15] offset:2048
v_add_u32 v14, 196608, v14
s_add_u32 m0, s55, 16384
s_nop 0
global_load_lds_dwordx4 v14, s[44:45] offset:0
global_load_lds_dwordx4 v14, s[46:47] offset:1024
global_load_lds_dwordx4 v14, s[48:49] offset:2048
s_add_u32 m0, s55, 22528
s_nop 0
global_load_lds_dwordx4 v14, s[10:11] offset:0
global_load_lds_dwordx4 v14, s[12:13] offset:1024
global_load_lds_dwordx4 v14, s[14:15] offset:2048
v_add_u32 v14, 196608, v14
s_add_u32 m0, s55, 32768
s_nop 0
global_load_lds_dwordx4 v14, s[44:45] offset:0
global_load_lds_dwordx4 v14, s[46:47] offset:1024
global_load_lds_dwordx4 v14, s[48:49] offset:2048
s_add_u32 m0, s55, 38912
s_nop 0
global_load_lds_dwordx4 v14, s[10:11] offset:0
global_load_lds_dwordx4 v14, s[12:13] offset:1024
global_load_lds_dwordx4 v14, s[14:15] offset:2048
v_add_u32 v14, 196608, v14
s_add_u32 m0, s55, 49152
s_nop 0
global_load_lds_dwordx4 v14, s[44:45] offset:0
global_load_lds_dwordx4 v14, s[46:47] offset:1024
global_load_lds_dwordx4 v14, s[48:49] offset:2048
s_add_u32 m0, s55, 55296
s_nop 0
global_load_lds_dwordx4 v14, s[10:11] offset:0
global_load_lds_dwordx4 v14, s[12:13] offset:1024
global_load_lds_dwordx4 v14, s[14:15] offset:2048
v_add_u32 v14, 196608, v14
s_barrier
s_add_u32 m0, s56, 0
s_nop 0
global_load_lds_dword v13, s[42:43] sc1
global_load_lds_dword v13, s[42:43] offset:512 sc1
s_add_u32 m0, s54, 0
s_nop 0
global_load_lds_dwordx4 v12, s[40:41] offset:0 sc1
global_load_lds_dwordx4 v12, s[40:41] offset:1024 sc1
global_load_lds_dwordx4 v12, s[40:41] offset:2048 sc1
global_load_lds_dwordx4 v12, s[40:41] offset:3072 sc1
s_add_u32 m0, s54, 8192
s_nop 0
global_load_lds_dwordx4 v12, s[8:9] offset:0 sc1
global_load_lds_dwordx4 v12, s[8:9] offset:1024 sc1
global_load_lds_dwordx4 v12, s[8:9] offset:2048 sc1
global_load_lds_dwordx4 v12, s[8:9] offset:3072 sc1
v_add_u32 v13, 0x4000, v13
v_add_u32 v12, 0x4000, v12
s_add_u32 m0, s56, 16384
s_nop 0
global_load_lds_dword v13, s[42:43] sc1
global_load_lds_dword v13, s[42:43] offset:512 sc1
s_add_u32 m0, s54, 16384
s_nop 0
global_load_lds_dwordx4 v12, s[40:41] offset:0 sc1
global_load_lds_dwordx4 v12, s[40:41] offset:1024 sc1
global_load_lds_dwordx4 v12, s[40:41] offset:2048 sc1
global_load_lds_dwordx4 v12, s[40:41] offset:3072 sc1
s_add_u32 m0, s54, 24576
s_nop 0
global_load_lds_dwordx4 v12, s[8:9] offset:0 sc1
global_load_lds_dwordx4 v12, s[8:9] offset:1024 sc1
global_load_lds_dwordx4 v12, s[8:9] offset:2048 sc1
global_load_lds_dwordx4 v12, s[8:9] offset:3072 sc1
v_add_u32 v13, 0x4000, v13
v_add_u32 v12, 0x4000, v12
s_add_u32 m0, s56, 32768
s_nop 0
global_load_lds_dword v13, s[42:43] sc1
global_load_lds_dword v13, s[42:43] offset:512 sc1
s_add_u32 m0, s54, 32768
s_nop 0
global_load_lds_dwordx4 v12, s[40:41] offset:0 sc1
global_load_lds_dwordx4 v12, s[40:41] offset:1024 sc1
global_load_lds_dwordx4 v12, s[40:41] offset:2048 sc1
global_load_lds_dwordx4 v12, s[40:41] offset:3072 sc1
s_add_u32 m0, s54, 40960
s_nop 0
global_load_lds_dwordx4 v12, s[8:9] offset:0 sc1
global_load_lds_dwordx4 v12, s[8:9] offset:1024 sc1
global_load_lds_dwordx4 v12, s[8:9] offset:2048 sc1
global_load_lds_dwordx4 v12, s[8:9] offset:3072 sc1
v_add_u32 v13, 0x4000, v13
v_add_u32 v12, 0x4000, v12
s_add_u32 m0, s56, 49152
s_nop 0
global_load_lds_dword v13, s[42:43] sc1
global_load_lds_dword v13, s[42:43] offset:512 sc1
s_add_u32 m0, s54, 49152
s_nop 0
global_load_lds_dwordx4 v12, s[40:41] offset:0 sc1
global_load_lds_dwordx4 v12, s[40:41] offset:1024 sc1
global_load_lds_dwordx4 v12, s[40:41] offset:2048 sc1
global_load_lds_dwordx4 v12, s[40:41] offset:3072 sc1
s_add_u32 m0, s54, 57344
s_nop 0
global_load_lds_dwordx4 v12, s[8:9] offset:0 sc1
global_load_lds_dwordx4 v12, s[8:9] offset:1024 sc1
global_load_lds_dwordx4 v12, s[8:9] offset:2048 sc1
global_load_lds_dwordx4 v12, s[8:9] offset:3072 sc1
v_add_u32 v13, 0x4000, v13
v_add_u32 v12, 0x4000, v12
s_nop 1
v_lshrrev_b32 v34, 2, v16
v_and_b32 v34, 124, v34
v_add_u32 v39, 32768, v23
s_waitcnt vmcnt(30)
s_barrier
X a[64:67],v10,0
X a[0:3],v8,0
X a[4:7],v8,1024
X a[8:11],v8,2048
X a[12:15],v8,3072
X a[16:19],v8,4096
X a[20:23],v8,5120
X a[24:27],v8,6144
X a[28:31],v8,7168
T a[72:73],v9,1024
T a[74:75],v9,7168
T a[76:77],v32,1024
T a[78:79],v32,7168
T a[80:81],v9,2048
T a[82:83],v9,8192
T a[84:85],v32,2048
T a[86:87],v32,8192
T a[88:89],v9,0
T a[90:91],v9,6144
s_waitcnt lgkmcnt(8)
M v[128:131],a[72:75],a[0:3],0
T a[92:93],v32,0
M v[132:135],a[72:75],a[4:7],0
T a[94:95],v32,6144
M v[136:139],a[72:75],a[8:11],0
M v[140:143],a[72:75],a[12:15],0
M v[144:147],a[72:75],a[16:19],0
M v[148:151],a[72:75],a[20:23],0
M v[152:155],a[72:75],a[24:27],0
M v[156:159],a[72:75],a[28:31],0
M v[48:51],a[72:75],a[64:67],0
s_waitcnt vmcnt(20) lgkmcnt(0)
s_barrier
M v[160:163],a[76:79],a[0:3],0
T a[72:73],v9,17408
s_add_u32 m0, s56, 0
M v[164:167],a[76:79],a[4:7],0
T a[74:75],v9,23552
global_load_lds_dword v13, s[42:43] sc1
M v[168:171],a[76:79],a[8:11],0
X a[68:71],v10,16384
M v[172:175],a[76:79],a[12:15],0
global_load_lds_dword v13, s[42:43] offset:512 sc1
M v[176:179],a[76:79],a[16:19],0
X a[32:35],v8,16384
M v[180:183],a[76:79],a[20:23],0
s_add_u32 m0, s54, 0
M v[184:187],a[76:79],a[24:27],0
global_load_lds_dwordx4 v12, s[40:41] offset:0 sc1
M v[188:191],a[76:79],a[28:31],0
M v[52:55],a[76:79],a[64:67],0
X a[36:39],v8,17408
M v[192:195],a[0:3],a[80:83],0
T a[76:77],v32,17408
M v[196:199],a[4:7],a[80:83],0
T a[78:79],v32,23552
M v[200:203],a[8:11],a[80:83],0
global_load_lds_dwordx4 v12, s[40:41] offset:1024 sc1
M v[204:207],a[12:15],a[80:83],0
X a[40:43],v8,18432
M v[208:211],a[16:19],a[80:83],0
global_load_lds_dwordx4 v12, s[40:41] offset:2048 sc1
M v[212:215],a[20:23],a[80:83],0
global_load_lds_dwordx4 v12, s[40:41] offset:3072 sc1
M v[216:219],a[24:27],a[80:83],0
s_add_u32 m0, s54, 8192
M v[220:223],a[28:31],a[80:83],0
X a[44:47],v8,19456
M v[56:59],a[64:67],a[80:83],0
global_load_lds_dwordx4 v12, s[8:9] offset:0 sc1
M v[224:227],a[0:3],a[84:87],0
T a[80:81],v9,18432
M v[228:231],a[4:7],a[84:87],0
T a[82:83],v9,24576
M v[232:235],a[8:11],a[84:87],0
X a[48:51],v8,20480
M v[236:239],a[12:15],a[84:87],0
global_load_lds_dwordx4 v12, s[8:9] offset:1024 sc1
M v[240:243],a[16:19],a[84:87],0
global_load_lds_dwordx4 v12, s[8:9] offset:2048 sc1
M v[244:247],a[20:23],a[84:87],0
global_load_lds_dwordx4 v12, s[8:9] offset:3072 sc1
M v[248:251],a[24:27],a[84:87],0
X a[52:55],v8,21504
M v[252:255],a[28:31],a[84:87],0
v_add_u32 v13, 0x4000, v13
M v[60:63],a[64:67],a[84:87],0
v_add_u32 v12, 0x4000, v12
M v[64:67],a[88:91],a[0:3],0
T a[84:85],v32,18432
M v[68:71],a[88:91],a[4:7],0
T a[86:87],v32,24576
M v[72:75],a[88:91],a[8:11],0
X a[56:59],v8,22528
M v[76:79],a[88:91],a[12:15],0
s_add_u32 m0, s55, 0
M v[80:83],a[88:91],a[16:19],0
global_load_lds_dwordx4 v14, s[44:45] offset:0
M v[84:87],a[88:91],a[20:23],0
global_load_lds_dwordx4 v14, s[46:47] offset:1024
M v[88:91],a[88:91],a[24:27],0
X a[60:63],v8,23552
M v[92:95],a[88:91],a[28:31],0
global_load_lds_dwordx4 v14, s[48:49] offset:2048
M v[96:99],a[92:95],a[0:3],0
T a[88:89],v9,16384
M v[100:103],a[92:95],a[4:7],0
T a[90:91],v9,22528
M v[104:107],a[92:95],a[8:11],0
s_add_u32 m0, s55, 6144
M v[108:111],a[92:95],a[12:15],0
global_load_lds_dwordx4 v14, s[10:11] offset:0
M v[112:115],a[92:95],a[16:19],0
global_load_lds_dwordx4 v14, s[12:13] offset:1024
M v[116:119],a[92:95],a[20:23],0
global_load_lds_dwordx4 v14, s[14:15] offset:2048
M v[120:123],a[92:95],a[24:27],0
v_add_u32 v14, 196608, v14
M v[124:127],a[92:95],a[28:31],0
s_waitcnt lgkmcnt(2)
M v[128:131],a[72:75],a[32:35],v[128:131]
T a[92:93],v32,16384
M v[132:135],a[72:75],a[36:39],v[132:135]
T a[94:95],v32,22528
M v[136:139],a[72:75],a[40:43],v[136:139]
M v[140:143],a[72:75],a[44:47],v[140:143]
M v[144:147],a[72:75],a[48:51],v[144:147]
M v[148:151],a[72:75],a[52:55],v[148:151]
M v[152:155],a[72:75],a[56:59],v[152:155]
M v[156:159],a[72:75],a[60:63],v[156:159]
M v[48:51],a[72:75],a[68:71],v[48:51]
s_waitcnt vmcnt(26) lgkmcnt(0)
s_barrier
M v[160:163],a[76:79],a[32:35],v[160:163]
T a[72:73],v9,33792
s_add_u32 m0, s56, 16384
M v[164:167],a[76:79],a[36:39],v[164:167]
T a[74:75],v9,39936
global_load_lds_dword v13, s[42:43] sc1
M v[168:171],a[76:79],a[40:43],v[168:171]
X a[64:67],v10,32768
M v[172:175],a[76:79],a[44:47],v[172:175]
global_load_lds_dword v13, s[42:43] offset:512 sc1
M v[176:179],a[76:79],a[48:51],v[176:179]
X a[0:3],v8,32768
M v[180:183],a[76:79],a[52:55],v[180:183]
s_add_u32 m0, s54, 16384
M v[184:187],a[76:79],a[56:59],v[184:187]
global_load_lds_dwordx4 v12, s[40:41] offset:0 sc1
M v[188:191],a[76:79],a[60:63],v[188:191]
M v[52:55],a[76:79],a[68:71],v[52:55]
X a[4:7],v8,33792
M v[192:195],a[32:35],a[80:83],v[192:195]
T a[76:77],v32,33792
M v[196:199],a[36:39],a[80:83],v[196:199]
T a[78:79],v32,39936
M v[200:203],a[40:43],a[80:83],v[200:203]
global_load_lds_dwordx4 v12, s[40:41] offset:1024 sc1
M v[204:207],a[44:47],a[80:83],v[204:207]
X a[8:11],v8,34816
M v[208:211],a[48:51],a[80:83],v[208:211]
global_load_lds_dwordx4 v12, s[40:41] offset:2048 sc1
M v[212:215],a[52:55],a[80:83],v[212:215]
global_load_lds_dwordx4 v12, s[40:41] offset:3072 sc1
M v[216:219],a[56:59],a[80:83],v[216:219]
s_add_u32 m0, s54, 24576
M v[220:223],a[60:63],a[80:83],v[220:223]
X a[12:15],v8,35840
M v[56:59],a[68:71],a[80:83],v[56:59]
global_load_lds_dwordx4 v12, s[8:9] offset:0 sc1
M v[224:227],a[32:35],a[84:87],v[224:227]
T a[80:81],v9,34816
M v[228:231],a[36:39],a[84:87],v[228:231]
T a[82:83],v9,40960
M v[232:235],a[40:43],a[84:87],v[232:235]
X a[16:19],v8,36864
M v[236:239],a[44:47],a[84:87],v[236:239]
global_load_lds_dwordx4 v12, s[8:9] offset:1024 sc1
M v[240:243],a[48:51],a[84:87],v[240:243]
global_load_lds_dwordx4 v12, s[8:9] offset:2048 sc1
M v[244:247],a[52:55],a[84:87],v[244:247]
global_load_lds_dwordx4 v12, s[8:9] offset:3072 sc1
M v[248:251],a[56:59],a[84:87],v[248:251]
X a[20:23],v8,37888
M v[252:255],a[60:63],a[84:87],v[252:255]
v_add_u32 v13, 0x4000, v13
M v[60:63],a[68:71],a[84:87],v[60:63]
v_add_u32 v12, 0x4000, v12
M v[64:67],a[88:91],a[32:35],v[64:67]
T a[84:85],v32,34816
M v[68:71],a[88:91],a[36:39],v[68:71]
T a[86:87],v32,40960
M v[72:75],a[88:91],a[40:43],v[72:75]
X a[24:27],v8,38912
M v[76:79],a[88:91],a[44:47],v[76:79]
s_add_u32 m0, s55, 16384
M v[80:83],a[88:91],a[48:51],v[80:83]
global_load_lds_dwordx4 v14, s[44:45] offset:0
M v[84:87],a[88:91],a[52:55],v[84:87]
global_load_lds_dwordx4 v14, s[46:47] offset:1024
M v[88:91],a[88:91],a[56:59],v[88:91]
X a[28:31],v8,39936
M v[92:95],a[88:91],a[60:63],v[92:95]
global_load_lds_dwordx4 v14, s[48:49] offset:2048
M v[96:99],a[92:95],a[32:35],v[96:99]
T a[88:89],v9,32768
M v[100:103],a[92:95],a[36:39],v[100:103]
T a[90:91],v9,38912
M v[104:107],a[92:95],a[40:43],v[104:107]
s_add_u32 m0, s55, 22528
M v[108:111],a[92:95],a[44:47],v[108:111]
global_load_lds_dwordx4 v14, s[10:11] offset:0
M v[112:115],a[92:95],a[48:51],v[112:115]
global_load_lds_dwordx4 v14, s[12:13] offset:1024
M v[116:119],a[92:95],a[52:55],v[116:119]
global_load_lds_dwordx4 v14, s[14:15] offset:2048
M v[120:123],a[92:95],a[56:59],v[120:123]
v_add_u32 v14, 196608, v14
M v[124:127],a[92:95],a[60:63],v[124:127]
s_waitcnt lgkmcnt(2)
M v[128:131],a[72:75],a[0:3],v[128:131]
T a[92:93],v32,32768
M v[132:135],a[72:75],a[4:7],v[132:135]
T a[94:95],v32,38912
M v[136:139],a[72:75],a[8:11],v[136:139]
M v[140:143],a[72:75],a[12:15],v[140:143]
M v[144:147],a[72:75],a[16:19],v[144:147]
M v[148:151],a[72:75],a[20:23],v[148:151]
M v[152:155],a[72:75],a[24:27],v[152:155]
M v[156:159],a[72:75],a[28:31],v[156:159]
M v[48:51],a[72:75],a[64:67],v[48:51]
s_waitcnt vmcnt(32) lgkmcnt(0)
s_barrier
M v[160:163],a[76:79],a[0:3],v[160:163]
T a[72:73],v9,50176
s_add_u32 m0, s56, 32768
M v[164:167],a[76:79],a[4:7],v[164:167]
T a[74:75],v9,56320
global_load_lds_dword v13, s[42:43] sc1
M v[168:171],a[76:79],a[8:11],v[168:171]
X a[68:71],v10,49152
M v[172:175],a[76:79],a[12:15],v[172:175]
global_load_lds_dword v13, s[42:43] offset:512 sc1
M v[176:179],a[76:79],a[16:19],v[176:179]
X a[32:35],v8,49152
M v[180:183],a[76:79],a[20:23],v[180:183]
s_add_u32 m0, s54, 32768
M v[184:187],a[76:79],a[24:27],v[184:187]
global_load_lds_dwordx4 v12, s[40:41] offset:0 sc1
M v[188:191],a[76:79],a[28:31],v[188:191]
M v[52:55],a[76:79],a[64:67],v[52:55]
X a[36:39],v8,50176
M v[192:195],a[0:3],a[80:83],v[192:195]
T a[76:77],v32,50176
M v[196:199],a[4:7],a[80:83],v[196:199]
T a[78:79],v32,56320
M v[200:203],a[8:11],a[80:83],v[200:203]
global_load_lds_dwordx4 v12, s[40:41] offset:1024 sc1
M v[204:207],a[12:15],a[80:83],v[204:207]
X a[40:43],v8,51200
M v[208:211],a[16:19],a[80:83],v[208:211]
global_load_lds_dwordx4 v12, s[40:41] offset:2048 sc1
M v[212:215],a[20:23],a[80:83],v[212:215]
global_load_lds_dwordx4 v12, s[40:41] offset:3072 sc1
M v[216:219],a[24:27],a[80:83],v[216:219]
s_add_u32 m0, s54, 40960
M v[220:223],a[28:31],a[80:83],v[220:223]
X a[44:47],v8,52224
M v[56:59],a[64:67],a[80:83],v[56:59]
global_load_lds_dwordx4 v12, s[8:9] offset:0 sc1
M v[224:227],a[0:3],a[84:87],v[224:227]
T a[80:81],v9,51200
M v[228:231],a[4:7],a[84:87],v[228:231]
T a[82:83],v9,57344
M v[232:235],a[8:11],a[84:87],v[232:235]
X a[48:51],v8,53248
M v[236:239],a[12:15],a[84:87],v[236:239]
global_load_lds_dwordx4 v12, s[8:9] offset:1024 sc1
M v[240:243],a[16:19],a[84:87],v[240:243]
global_load_lds_dwordx4 v12, s[8:9] offset:2048 sc1
M v[244:247],a[20:23],a[84:87],v[244:247]
global_load_lds_dwordx4 v12, s[8:9] offset:3072 sc1
M v[248:251],a[24:27],a[84:87],v[248:251]
X a[52:55],v8,54272
M v[252:255],a[28:31],a[84:87],v[252:255]
v_add_u32 v13, 0x4000, v13
M v[60:63],a[64:67],a[84:87],v[60:63]
v_add_u32 v12, 0x4000, v12
M v[64:67],a[88:91],a[0:3],v[64:67]
T a[84:85],v32,51200
M v[68:71],a[88:91],a[4:7],v[68:71]
T a[86:87],v32,57344
M v[72:75],a[88:91],a[8:11],v[72:75]
X a[56:59],v8,55296
M v[76:79],a[88:91],a[12:15],v[76:79]
s_add_u32 m0, s55, 32768
M v[80:83],a[88:91],a[16:19],v[80:83]
global_load_lds_dwordx4 v14, s[44:45] offset:0
M v[84:87],a[88:91],a[20:23],v[84:87]
global_load_lds_dwordx4 v14, s[46:47] offset:1024
M v[88:91],a[88:91],a[24:27],v[88:91]
X a[60:63],v8,56320
M v[92:95],a[88:91],a[28:31],v[92:95]
global_load_lds_dwordx4 v14, s[48:49] offset:2048
M v[96:99],a[92:95],a[0:3],v[96:99]
T a[88:89],v9,49152
M v[100:103],a[92:95],a[4:7],v[100:103]
T a[90:91],v9,55296
M v[104:107],a[92:95],a[8:11],v[104:107]
s_add_u32 m0, s55, 38912
M v[108:111],a[92:95],a[12:15],v[108:111]
global_load_lds_dwordx4 v14, s[10:11] offset:0
M v[112:115],a[92:95],a[16:19],v[112:115]
global_load_lds_dwordx4 v14, s[12:13] offset:1024
M v[116:119],a[92:95],a[20:23],v[116:119]
global_load_lds_dwordx4 v14, s[14:15] offset:2048
M v[120:123],a[92:95],a[24:27],v[120:123]
v_add_u32 v14, 196608, v14
M v[124:127],a[92:95],a[28:31],v[124:127]
s_waitcnt lgkmcnt(2)
M v[128:131],a[72:75],a[32:35],v[128:131]
T a[92:93],v32,49152
M v[132:135],a[72:75],a[36:39],v[132:135]
T a[94:95],v32,55296
M v[136:139],a[72:75],a[40:43],v[136:139]
M v[140:143],a[72:75],a[44:47],v[140:143]
M v[144:147],a[72:75],a[48:51],v[144:147]
M v[148:151],a[72:75],a[52:55],v[148:151]
M v[152:155],a[72:75],a[56:59],v[152:155]
M v[156:159],a[72:75],a[60:63],v[156:159]
M v[48:51],a[72:75],a[68:71],v[48:51]
s_waitcnt vmcnt(32) lgkmcnt(0)
s_barrier
M v[160:163],a[76:79],a[32:35],v[160:163]
T a[72:73],v9,1024
s_add_u32 m0, s56, 49152
M v[164:167],a[76:79],a[36:39],v[164:167]
T a[74:75],v9,7168
global_load_lds_dword v13, s[42:43] sc1
M v[168:171],a[76:79],a[40:43],v[168:171]
X a[64:67],v10,0
M v[172:175],a[76:79],a[44:47],v[172:175]
global_load_lds_dword v13, s[42:43] offset:512 sc1
M v[176:179],a[76:79],a[48:51],v[176:179]
X a[0:3],v8,0
M v[180:183],a[76:79],a[52:55],v[180:183]
s_add_u32 m0, s54, 49152
M v[184:187],a[76:79],a[56:59],v[184:187]
global_load_lds_dwordx4 v12, s[40:41] offset:0 sc1
M v[188:191],a[76:79],a[60:63],v[188:191]
M v[52:55],a[76:79],a[68:71],v[52:55]
X a[4:7],v8,1024
M v[192:195],a[32:35],a[80:83],v[192:195]
T a[76:77],v32,1024
M v[196:199],a[36:39],a[80:83],v[196:199]
T a[78:79],v32,7168
M v[200:203],a[40:43],a[80:83],v[200:203]
global_load_lds_dwordx4 v12, s[40:41] offset:1024 sc1
M v[204:207],a[44:47],a[80:83],v[204:207]
X a[8:11],v8,2048
M v[208:211],a[48:51],a[80:83],v[208:211]
global_load_lds_dwordx4 v12, s[40:41] offset:2048 sc1
M v[212:215],a[52:55],a[80:83],v[212:215]
global_load_lds_dwordx4 v12, s[40:41] offset:3072 sc1
M v[216:219],a[56:59],a[80:83],v[216:219]
s_add_u32 m0, s54, 57344
M v[220:223],a[60:63],a[80:83],v[220:223]
X a[12:15],v8,3072
M v[56:59],a[68:71],a[80:83],v[56:59]
global_load_lds_dwordx4 v12, s[8:9] offset:0 sc1
M v[224:227],a[32:35],a[84:87],v[224:227]
T a[80:81],v9,2048
M v[228:231],a[36:39],a[84:87],v[228:231]
T a[82:83],v9,8192
M v[232:235],a[40:43],a[84:87],v[232:235]
X a[16:19],v8,4096
M v[236:239],a[44:47],a[84:87],v[236:239]
global_load_lds_dwordx4 v12, s[8:9] offset:1024 sc1
M v[240:243],a[48:51],a[84:87],v[240:243]
global_load_lds_dwordx4 v12, s[8:9] offset:2048 sc1
M v[244:247],a[52:55],a[84:87],v[244:247]
global_load_lds_dwordx4 v12, s[8:9] offset:3072 sc1
M v[248:251],a[56:59],a[84:87],v[248:251]
X a[20:23],v8,5120
M v[252:255],a[60:63],a[84:87],v[252:255]
v_add_u32 v13, 0x4000, v13
M v[60:63],a[68:71],a[84:87],v[60:63]
v_add_u32 v12, 0x4000, v12
M v[64:67],a[88:91],a[32:35],v[64:67]
T a[84:85],v32,2048
M v[68:71],a[88:91],a[36:39],v[68:71]
T a[86:87],v32,8192
M v[72:75],a[88:91],a[40:43],v[72:75]
X a[24:27],v8,6144
M v[76:79],a[88:91],a[44:47],v[76:79]
s_add_u32 m0, s55, 49152
M v[80:83],a[88:91],a[48:51],v[80:83]
global_load_lds_dwordx4 v14, s[44:45] offset:0
M v[84:87],a[88:91],a[52:55],v[84:87]
global_load_lds_dwordx4 v14, s[46:47] offset:1024
M v[88:91],a[88:91],a[56:59],v[88:91]
X a[28:31],v8,7168
M v[92:95],a[88:91],a[60:63],v[92:95]
global_load_lds_dwordx4 v14, s[48:49] offset:2048
M v[96:99],a[92:95],a[32:35],v[96:99]
T a[88:89],v9,0
M v[100:103],a[92:95],a[36:39],v[100:103]
T a[90:91],v9,6144
M v[104:107],a[92:95],a[40:43],v[104:107]
s_add_u32 m0, s55, 55296
M v[108:111],a[92:95],a[44:47],v[108:111]
global_load_lds_dwordx4 v14, s[10:11] offset:0
M v[112:115],a[92:95],a[48:51],v[112:115]
global_load_lds_dwordx4 v14, s[12:13] offset:1024
M v[116:119],a[92:95],a[52:55],v[116:119]
global_load_lds_dwordx4 v14, s[14:15] offset:2048
M v[120:123],a[92:95],a[56:59],v[120:123]
v_add_u32 v14, 196608, v14
M v[124:127],a[92:95],a[60:63],v[124:127]
s_mov_b32 s64, 6
Lp1QL_%=:
s_waitcnt lgkmcnt(2)
M v[128:131],a[72:75],a[0:3],v[128:131]
T a[92:93],v32,0
M v[132:135],a[72:75],a[4:7],v[132:135]
T a[94:95],v32,6144
M v[136:139],a[72:75],a[8:11],v[136:139]
M v[140:143],a[72:75],a[12:15],v[140:143]
M v[144:147],a[72:75],a[16:19],v[144:147]
M v[148:151],a[72:75],a[20:23],v[148:151]
M v[152:155],a[72:75],a[24:27],v[152:155]
M v[156:159],a[72:75],a[28:31],v[156:159]
M v[48:51],a[72:75],a[64:67],v[48:51]
s_waitcnt vmcnt(32) lgkmcnt(0)
s_barrier
M v[160:163],a[76:79],a[0:3],v[160:163]
T a[72:73],v9,17408
s_add_u32 m0, s56, 0
M v[164:167],a[76:79],a[4:7],v[164:167]
T a[74:75],v9,23552
global_load_lds_dword v13, s[42:43] sc1
M v[168:171],a[76:79],a[8:11],v[168:171]
X a[68:71],v10,16384
M v[172:175],a[76:79],a[12:15],v[172:175]
global_load_lds_dword v13, s[42:43] offset:512 sc1
M v[176:179],a[76:79],a[16:19],v[176:179]
X a[32:35],v8,16384
M v[180:183],a[76:79],a[20:23],v[180:183]
s_add_u32 m0, s54, 0
M v[184:187],a[76:79],a[24:27],v[184:187]
global_load_lds_dwordx4 v12, s[40:41] offset:0 sc1
M v[188:191],a[76:79],a[28:31],v[188:191]
M v[52:55],a[76:79],a[64:67],v[52:55]
X a[36:39],v8,17408
M v[192:195],a[0:3],a[80:83],v[192:195]
T a[76:77],v32,17408
M v[196:199],a[4:7],a[80:83],v[196:199]
T a[78:79],v32,23552
M v[200:203],a[8:11],a[80:83],v[200:203]
global_load_lds_dwordx4 v12, s[40:41] offset:1024 sc1
M v[204:207],a[12:15],a[80:83],v[204:207]
X a[40:43],v8,18432
M v[208:211],a[16:19],a[80:83],v[208:211]
global_load_lds_dwordx4 v12, s[40:41] offset:2048 sc1
M v[212:215],a[20:23],a[80:83],v[212:215]
global_load_lds_dwordx4 v12, s[40:41] offset:3072 sc1
M v[216:219],a[24:27],a[80:83],v[216:219]
s_add_u32 m0, s54, 8192
M v[220:223],a[28:31],a[80:83],v[220:223]
X a[44:47],v8,19456
M v[56:59],a[64:67],a[80:83],v[56:59]
global_load_lds_dwordx4 v12, s[8:9] offset:0 sc1
M v[224:227],a[0:3],a[84:87],v[224:227]
T a[80:81],v9,18432
M v[228:231],a[4:7],a[84:87],v[228:231]
T a[82:83],v9,24576
M v[232:235],a[8:11],a[84:87],v[232:235]
X a[48:51],v8,20480
M v[236:239],a[12:15],a[84:87],v[236:239]
global_load_lds_dwordx4 v12, s[8:9] offset:1024 sc1
M v[240:243],a[16:19],a[84:87],v[240:243]
global_load_lds_dwordx4 v12, s[8:9] offset:2048 sc1
M v[244:247],a[20:23],a[84:87],v[244:247]
global_load_lds_dwordx4 v12, s[8:9] offset:3072 sc1
M v[248:251],a[24:27],a[84:87],v[248:251]
X a[52:55],v8,21504
M v[252:255],a[28:31],a[84:87],v[252:255]
v_add_u32 v13, 0x4000, v13
M v[60:63],a[64:67],a[84:87],v[60:63]
v_add_u32 v12, 0x4000, v12
M v[64:67],a[88:91],a[0:3],v[64:67]
T a[84:85],v32,18432
M v[68:71],a[88:91],a[4:7],v[68:71]
T a[86:87],v32,24576
M v[72:75],a[88:91],a[8:11],v[72:75]
X a[56:59],v8,22528
M v[76:79],a[88:91],a[12:15],v[76:79]
s_add_u32 m0, s55, 0
M v[80:83],a[88:91],a[16:19],v[80:83]
global_load_lds_dwordx4 v14, s[44:45] offset:0
M v[84:87],a[88:91],a[20:23],v[84:87]
global_load_lds_dwordx4 v14, s[46:47] offset:1024
M v[88:91],a[88:91],a[24:27],v[88:91]
X a[60:63],v8,23552
M v[92:95],a[88:91],a[28:31],v[92:95]
global_load_lds_dwordx4 v14, s[48:49] offset:2048
M v[96:99],a[92:95],a[0:3],v[96:99]
T a[88:89],v9,16384
M v[100:103],a[92:95],a[4:7],v[100:103]
T a[90:91],v9,22528
M v[104:107],a[92:95],a[8:11],v[104:107]
s_add_u32 m0, s55, 6144
M v[108:111],a[92:95],a[12:15],v[108:111]
global_load_lds_dwordx4 v14, s[10:11] offset:0
M v[112:115],a[92:95],a[16:19],v[112:115]
global_load_lds_dwordx4 v14, s[12:13] offset:1024
M v[116:119],a[92:95],a[20:23],v[116:119]
global_load_lds_dwordx4 v14, s[14:15] offset:2048
M v[120:123],a[92:95],a[24:27],v[120:123]
v_add_u32 v14, 196608, v14
M v[124:127],a[92:95],a[28:31],v[124:127]
s_waitcnt lgkmcnt(2)
M v[128:131],a[72:75],a[32:35],v[128:131]
T a[92:93],v32,16384
M v[132:135],a[72:75],a[36:39],v[132:135]
T a[94:95],v32,22528
M v[136:139],a[72:75],a[40:43],v[136:139]
M v[140:143],a[72:75],a[44:47],v[140:143]
M v[144:147],a[72:75],a[48:51],v[144:147]
M v[148:151],a[72:75],a[52:55],v[148:151]
M v[152:155],a[72:75],a[56:59],v[152:155]
M v[156:159],a[72:75],a[60:63],v[156:159]
M v[48:51],a[72:75],a[68:71],v[48:51]
s_waitcnt vmcnt(32) lgkmcnt(0)
s_barrier
M v[160:163],a[76:79],a[32:35],v[160:163]
T a[72:73],v9,33792
s_add_u32 m0, s56, 16384
M v[164:167],a[76:79],a[36:39],v[164:167]
T a[74:75],v9,39936
global_load_lds_dword v13, s[42:43] sc1
M v[168:171],a[76:79],a[40:43],v[168:171]
X a[64:67],v10,32768
M v[172:175],a[76:79],a[44:47],v[172:175]
global_load_lds_dword v13, s[42:43] offset:512 sc1
M v[176:179],a[76:79],a[48:51],v[176:179]
X a[0:3],v8,32768
M v[180:183],a[76:79],a[52:55],v[180:183]
s_add_u32 m0, s54, 16384
M v[184:187],a[76:79],a[56:59],v[184:187]
global_load_lds_dwordx4 v12, s[40:41] offset:0 sc1
M v[188:191],a[76:79],a[60:63],v[188:191]
M v[52:55],a[76:79],a[68:71],v[52:55]
X a[4:7],v8,33792
M v[192:195],a[32:35],a[80:83],v[192:195]
T a[76:77],v32,33792
M v[196:199],a[36:39],a[80:83],v[196:199]
T a[78:79],v32,39936
M v[200:203],a[40:43],a[80:83],v[200:203]
global_load_lds_dwordx4 v12, s[40:41] offset:1024 sc1
M v[204:207],a[44:47],a[80:83],v[204:207]
X a[8:11],v8,34816
M v[208:211],a[48:51],a[80:83],v[208:211]
global_load_lds_dwordx4 v12, s[40:41] offset:2048 sc1
M v[212:215],a[52:55],a[80:83],v[212:215]
global_load_lds_dwordx4 v12, s[40:41] offset:3072 sc1
M v[216:219],a[56:59],a[80:83],v[216:219]
s_add_u32 m0, s54, 24576
M v[220:223],a[60:63],a[80:83],v[220:223]
X a[12:15],v8,35840
M v[56:59],a[68:71],a[80:83],v[56:59]
global_load_lds_dwordx4 v12, s[8:9] offset:0 sc1
M v[224:227],a[32:35],a[84:87],v[224:227]
T a[80:81],v9,34816
M v[228:231],a[36:39],a[84:87],v[228:231]
T a[82:83],v9,40960
M v[232:235],a[40:43],a[84:87],v[232:235]
X a[16:19],v8,36864
M v[236:239],a[44:47],a[84:87],v[236:239]
global_load_lds_dwordx4 v12, s[8:9] offset:1024 sc1
M v[240:243],a[48:51],a[84:87],v[240:243]
global_load_lds_dwordx4 v12, s[8:9] offset:2048 sc1
M v[244:247],a[52:55],a[84:87],v[244:247]
global_load_lds_dwordx4 v12, s[8:9] offset:3072 sc1
M v[248:251],a[56:59],a[84:87],v[248:251]
X a[20:23],v8,37888
M v[252:255],a[60:63],a[84:87],v[252:255]
v_add_u32 v13, 0x4000, v13
M v[60:63],a[68:71],a[84:87],v[60:63]
v_add_u32 v12, 0x4000, v12
M v[64:67],a[88:91],a[32:35],v[64:67]
T a[84:85],v32,34816
M v[68:71],a[88:91],a[36:39],v[68:71]
T a[86:87],v32,40960
M v[72:75],a[88:91],a[40:43],v[72:75]
X a[24:27],v8,38912
M v[76:79],a[88:91],a[44:47],v[76:79]
s_add_u32 m0, s55, 16384
M v[80:83],a[88:91],a[48:51],v[80:83]
global_load_lds_dwordx4 v14, s[44:45] offset:0
M v[84:87],a[88:91],a[52:55],v[84:87]
global_load_lds_dwordx4 v14, s[46:47] offset:1024
M v[88:91],a[88:91],a[56:59],v[88:91]
X a[28:31],v8,39936
M v[92:95],a[88:91],a[60:63],v[92:95]
global_load_lds_dwordx4 v14, s[48:49] offset:2048
M v[96:99],a[92:95],a[32:35],v[96:99]
T a[88:89],v9,32768
M v[100:103],a[92:95],a[36:39],v[100:103]
T a[90:91],v9,38912
M v[104:107],a[92:95],a[40:43],v[104:107]
s_add_u32 m0, s55, 22528
M v[108:111],a[92:95],a[44:47],v[108:111]
global_load_lds_dwordx4 v14, s[10:11] offset:0
M v[112:115],a[92:95],a[48:51],v[112:115]
global_load_lds_dwordx4 v14, s[12:13] offset:1024
M v[116:119],a[92:95],a[52:55],v[116:119]
global_load_lds_dwordx4 v14, s[14:15] offset:2048
M v[120:123],a[92:95],a[56:59],v[120:123]
v_add_u32 v14, 196608, v14
M v[124:127],a[92:95],a[60:63],v[124:127]
s_waitcnt lgkmcnt(2)
M v[128:131],a[72:75],a[0:3],v[128:131]
T a[92:93],v32,32768
M v[132:135],a[72:75],a[4:7],v[132:135]
T a[94:95],v32,38912
M v[136:139],a[72:75],a[8:11],v[136:139]
M v[140:143],a[72:75],a[12:15],v[140:143]
M v[144:147],a[72:75],a[16:19],v[144:147]
M v[148:151],a[72:75],a[20:23],v[148:151]
M v[152:155],a[72:75],a[24:27],v[152:155]
M v[156:159],a[72:75],a[28:31],v[156:159]
M v[48:51],a[72:75],a[64:67],v[48:51]
s_waitcnt vmcnt(32) lgkmcnt(0)
s_barrier
M v[160:163],a[76:79],a[0:3],v[160:163]
T a[72:73],v9,50176
s_add_u32 m0, s56, 32768
M v[164:167],a[76:79],a[4:7],v[164:167]
T a[74:75],v9,56320
global_load_lds_dword v13, s[42:43] sc1
M v[168:171],a[76:79],a[8:11],v[168:171]
X a[68:71],v10,49152
M v[172:175],a[76:79],a[12:15],v[172:175]
global_load_lds_dword v13, s[42:43] offset:512 sc1
M v[176:179],a[76:79],a[16:19],v[176:179]
X a[32:35],v8,49152
M v[180:183],a[76:79],a[20:23],v[180:183]
s_add_u32 m0, s54, 32768
M v[184:187],a[76:79],a[24:27],v[184:187]
global_load_lds_dwordx4 v12, s[40:41] offset:0 sc1
M v[188:191],a[76:79],a[28:31],v[188:191]
M v[52:55],a[76:79],a[64:67],v[52:55]
X a[36:39],v8,50176
M v[192:195],a[0:3],a[80:83],v[192:195]
T a[76:77],v32,50176
M v[196:199],a[4:7],a[80:83],v[196:199]
T a[78:79],v32,56320
M v[200:203],a[8:11],a[80:83],v[200:203]
global_load_lds_dwordx4 v12, s[40:41] offset:1024 sc1
M v[204:207],a[12:15],a[80:83],v[204:207]
X a[40:43],v8,51200
M v[208:211],a[16:19],a[80:83],v[208:211]
global_load_lds_dwordx4 v12, s[40:41] offset:2048 sc1
M v[212:215],a[20:23],a[80:83],v[212:215]
global_load_lds_dwordx4 v12, s[40:41] offset:3072 sc1
M v[216:219],a[24:27],a[80:83],v[216:219]
s_add_u32 m0, s54, 40960
M v[220:223],a[28:31],a[80:83],v[220:223]
X a[44:47],v8,52224
M v[56:59],a[64:67],a[80:83],v[56:59]
global_load_lds_dwordx4 v12, s[8:9] offset:0 sc1
M v[224:227],a[0:3],a[84:87],v[224:227]
T a[80:81],v9,51200
M v[228:231],a[4:7],a[84:87],v[228:231]
T a[82:83],v9,57344
M v[232:235],a[8:11],a[84:87],v[232:235]
X a[48:51],v8,53248
M v[236:239],a[12:15],a[84:87],v[236:239]
global_load_lds_dwordx4 v12, s[8:9] offset:1024 sc1
M v[240:243],a[16:19],a[84:87],v[240:243]
global_load_lds_dwordx4 v12, s[8:9] offset:2048 sc1
M v[244:247],a[20:23],a[84:87],v[244:247]
global_load_lds_dwordx4 v12, s[8:9] offset:3072 sc1
M v[248:251],a[24:27],a[84:87],v[248:251]
X a[52:55],v8,54272
M v[252:255],a[28:31],a[84:87],v[252:255]
v_add_u32 v13, 0x4000, v13
M v[60:63],a[64:67],a[84:87],v[60:63]
v_add_u32 v12, 0x4000, v12
M v[64:67],a[88:91],a[0:3],v[64:67]
T a[84:85],v32,51200
M v[68:71],a[88:91],a[4:7],v[68:71]
T a[86:87],v32,57344
M v[72:75],a[88:91],a[8:11],v[72:75]
X a[56:59],v8,55296
M v[76:79],a[88:91],a[12:15],v[76:79]
s_add_u32 m0, s55, 32768
M v[80:83],a[88:91],a[16:19],v[80:83]
global_load_lds_dwordx4 v14, s[44:45] offset:0
M v[84:87],a[88:91],a[20:23],v[84:87]
global_load_lds_dwordx4 v14, s[46:47] offset:1024
M v[88:91],a[88:91],a[24:27],v[88:91]
X a[60:63],v8,56320
M v[92:95],a[88:91],a[28:31],v[92:95]
global_load_lds_dwordx4 v14, s[48:49] offset:2048
M v[96:99],a[92:95],a[0:3],v[96:99]
T a[88:89],v9,49152
M v[100:103],a[92:95],a[4:7],v[100:103]
T a[90:91],v9,55296
M v[104:107],a[92:95],a[8:11],v[104:107]
s_add_u32 m0, s55, 38912
M v[108:111],a[92:95],a[12:15],v[108:111]
global_load_lds_dwordx4 v14, s[10:11] offset:0
M v[112:115],a[92:95],a[16:19],v[112:115]
global_load_lds_dwordx4 v14, s[12:13] offset:1024
M v[116:119],a[92:95],a[20:23],v[116:119]
global_load_lds_dwordx4 v14, s[14:15] offset:2048
M v[120:123],a[92:95],a[24:27],v[120:123]
v_add_u32 v14, 196608, v14
M v[124:127],a[92:95],a[28:31],v[124:127]
s_waitcnt lgkmcnt(2)
M v[128:131],a[72:75],a[32:35],v[128:131]
T a[92:93],v32,49152
M v[132:135],a[72:75],a[36:39],v[132:135]
T a[94:95],v32,55296
M v[136:139],a[72:75],a[40:43],v[136:139]
M v[140:143],a[72:75],a[44:47],v[140:143]
M v[144:147],a[72:75],a[48:51],v[144:147]
M v[148:151],a[72:75],a[52:55],v[148:151]
M v[152:155],a[72:75],a[56:59],v[152:155]
M v[156:159],a[72:75],a[60:63],v[156:159]
M v[48:51],a[72:75],a[68:71],v[48:51]
s_waitcnt vmcnt(32) lgkmcnt(0)
s_barrier
M v[160:163],a[76:79],a[32:35],v[160:163]
T a[72:73],v9,1024
s_add_u32 m0, s56, 49152
M v[164:167],a[76:79],a[36:39],v[164:167]
T a[74:75],v9,7168
global_load_lds_dword v13, s[42:43] sc1
M v[168:171],a[76:79],a[40:43],v[168:171]
X a[64:67],v10,0
M v[172:175],a[76:79],a[44:47],v[172:175]
global_load_lds_dword v13, s[42:43] offset:512 sc1
M v[176:179],a[76:79],a[48:51],v[176:179]
X a[0:3],v8,0
M v[180:183],a[76:79],a[52:55],v[180:183]
s_add_u32 m0, s54, 49152
M v[184:187],a[76:79],a[56:59],v[184:187]
global_load_lds_dwordx4 v12, s[40:41] offset:0 sc1
M v[188:191],a[76:79],a[60:63],v[188:191]
M v[52:55],a[76:79],a[68:71],v[52:55]
X a[4:7],v8,1024
M v[192:195],a[32:35],a[80:83],v[192:195]
T a[76:77],v32,1024
M v[196:199],a[36:39],a[80:83],v[196:199]
T a[78:79],v32,7168
M v[200:203],a[40:43],a[80:83],v[200:203]
global_load_lds_dwordx4 v12, s[40:41] offset:1024 sc1
M v[204:207],a[44:47],a[80:83],v[204:207]
X a[8:11],v8,2048
M v[208:211],a[48:51],a[80:83],v[208:211]
global_load_lds_dwordx4 v12, s[40:41] offset:2048 sc1
M v[212:215],a[52:55],a[80:83],v[212:215]
global_load_lds_dwordx4 v12, s[40:41] offset:3072 sc1
M v[216:219],a[56:59],a[80:83],v[216:219]
s_add_u32 m0, s54, 57344
M v[220:223],a[60:63],a[80:83],v[220:223]
X a[12:15],v8,3072
M v[56:59],a[68:71],a[80:83],v[56:59]
global_load_lds_dwordx4 v12, s[8:9] offset:0 sc1
M v[224:227],a[32:35],a[84:87],v[224:227]
T a[80:81],v9,2048
M v[228:231],a[36:39],a[84:87],v[228:231]
T a[82:83],v9,8192
M v[232:235],a[40:43],a[84:87],v[232:235]
X a[16:19],v8,4096
M v[236:239],a[44:47],a[84:87],v[236:239]
global_load_lds_dwordx4 v12, s[8:9] offset:1024 sc1
M v[240:243],a[48:51],a[84:87],v[240:243]
global_load_lds_dwordx4 v12, s[8:9] offset:2048 sc1
M v[244:247],a[52:55],a[84:87],v[244:247]
global_load_lds_dwordx4 v12, s[8:9] offset:3072 sc1
M v[248:251],a[56:59],a[84:87],v[248:251]
X a[20:23],v8,5120
M v[252:255],a[60:63],a[84:87],v[252:255]
v_add_u32 v13, 0x4000, v13
M v[60:63],a[68:71],a[84:87],v[60:63]
v_add_u32 v12, 0x4000, v12
M v[64:67],a[88:91],a[32:35],v[64:67]
T a[84:85],v32,2048
M v[68:71],a[88:91],a[36:39],v[68:71]
T a[86:87],v32,8192
M v[72:75],a[88:91],a[40:43],v[72:75]
X a[24:27],v8,6144
M v[76:79],a[88:91],a[44:47],v[76:79]
s_add_u32 m0, s55, 49152
M v[80:83],a[88:91],a[48:51],v[80:83]
global_load_lds_dwordx4 v14, s[44:45] offset:0
M v[84:87],a[88:91],a[52:55],v[84:87]
global_load_lds_dwordx4 v14, s[46:47] offset:1024
M v[88:91],a[88:91],a[56:59],v[88:91]
X a[28:31],v8,7168
M v[92:95],a[88:91],a[60:63],v[92:95]
global_load_lds_dwordx4 v14, s[48:49] offset:2048
M v[96:99],a[92:95],a[32:35],v[96:99]
T a[88:89],v9,0
M v[100:103],a[92:95],a[36:39],v[100:103]
T a[90:91],v9,6144
M v[104:107],a[92:95],a[40:43],v[104:107]
s_add_u32 m0, s55, 55296
M v[108:111],a[92:95],a[44:47],v[108:111]
global_load_lds_dwordx4 v14, s[10:11] offset:0
M v[112:115],a[92:95],a[48:51],v[112:115]
global_load_lds_dwordx4 v14, s[12:13] offset:1024
M v[116:119],a[92:95],a[52:55],v[116:119]
global_load_lds_dwordx4 v14, s[14:15] offset:2048
s_sub_u32 s64, s64, 1
M v[120:123],a[92:95],a[56:59],v[120:123]
v_add_u32 v14, 196608, v14
s_cmp_lg_u32 s64, 0
M v[124:127],a[92:95],a[60:63],v[124:127]
s_cbranch_scc1 Lp1QL_%=
LmrgQ_%=:
v_mov_b32 v36, s57
v_mov_b32 v38, 0
s_waitcnt lgkmcnt(2)
v_mov_b32 v37, 0xff800000
M v[128:131],a[72:75],a[0:3],v[128:131]
T a[92:93],v32,0
v_subrev_u32 v12, 524288, v12
M v[132:135],a[72:75],a[4:7],v[132:135]
T a[94:95],v32,6144
s_cmp_eq_u32 s78, 0
s_cselect_b32 s72, 0xcfa8965f, 0
M v[136:139],a[72:75],a[8:11],v[136:139]
s_lshr_b32 s65, s99, 2
v_mov_b32 v35, s72
M v[140:143],a[72:75],a[12:15],v[140:143]
s_sub_u32 s55, s55, s65
s_mov_b32 s72, 0x50000
M v[144:147],a[72:75],a[16:19],v[144:147]
s_mov_b32 s73, 0x5550055
v_cndmask_b32 v24, v37, 0, s[72:73]
M v[148:151],a[72:75],a[20:23],v[148:151]
v_cndmask_b32 v44, v37, v35, s[72:73]
s_mov_b32 s72, 0xa0000
M v[152:155],a[72:75],a[24:27],v[152:155]
s_mov_b32 s73, 0xaaa00aa
v_cndmask_b32 v25, v37, 0, s[72:73]
M v[156:159],a[72:75],a[28:31],v[156:159]
v_cndmask_b32 v45, v37, v35, s[72:73]
s_mov_b32 s72, 0x150001
M v[48:51],a[72:75],a[64:67],v[48:51]
s_waitcnt vmcnt(32) lgkmcnt(0)
s_barrier
M v[160:163],a[76:79],a[0:3],v[160:163]
T a[72:73],v9,17408
s_add_u32 m0, s54, 131072
M v[164:167],a[76:79],a[4:7],v[164:167]
T a[74:75],v9,23552
global_load_lds_dwordx4 v12, s[40:41] offset:0 sc1
M v[168:171],a[76:79],a[8:11],v[168:171]
X a[68:71],v10,16384
s_mov_b32 s73, 0x15550155
M v[172:175],a[76:79],a[12:15],v[172:175]
v_cndmask_b32 v26, v37, 0, s[72:73]
v_cndmask_b32 v46, v37, v35, s[72:73]
M v[176:179],a[76:79],a[16:19],v[176:179]
X a[32:35],v8,16384
s_mov_b32 s72, 0x2a0002
M v[180:183],a[76:79],a[20:23],v[180:183]
global_load_lds_dwordx4 v12, s[40:41] offset:1024 sc1
s_mov_b32 s73, 0x2aaa02aa
M v[184:187],a[76:79],a[24:27],v[184:187]
v_cndmask_b32 v27, v37, 0, s[72:73]
v_cndmask_b32 v47, v37, v35, s[72:73]
M v[188:191],a[76:79],a[28:31],v[188:191]
global_load_lds_dwordx4 v12, s[40:41] offset:2048 sc1
s_mov_b32 s72, 0x55505555
M v[52:55],a[76:79],a[64:67],v[52:55]
X a[36:39],v8,17408
s_mov_b32 s73, 0x50005500
M v[192:195],a[0:3],a[80:83],v[192:195]
T a[76:77],v32,17408
v_cndmask_b32 v28, v37, 0, s[72:73]
M v[196:199],a[4:7],a[80:83],v[196:199]
T a[78:79],v32,23552
s_mov_b32 s72, 0xaaa0aaaa
M v[200:203],a[8:11],a[80:83],v[200:203]
global_load_lds_dwordx4 v12, s[40:41] offset:3072 sc1
v_add_u32 v12, 0x4000, v12
M v[204:207],a[12:15],a[80:83],v[204:207]
X a[40:43],v8,18432
s_mov_b32 s73, 0xa000aa00
M v[208:211],a[16:19],a[80:83],v[208:211]
s_add_u32 m0, s55, 0
v_cndmask_b32 v29, v37, 0, s[72:73]
M v[212:215],a[20:23],a[80:83],v[212:215]
global_load_lds_dwordx4 v11, s[50:51] offset:0
s_mov_b32 s72, 0x55405554
M v[216:219],a[24:27],a[80:83],v[216:219]
s_mov_b32 s73, 0x40005400
v_cndmask_b32 v30, v37, 0, s[72:73]
M v[220:223],a[28:31],a[80:83],v[220:223]
X a[44:47],v8,19456
s_mov_b32 s72, 0xaa80aaa8
M v[56:59],a[64:67],a[80:83],v[56:59]
global_load_lds_dwordx4 v11, s[52:53] offset:1024
v_add_u32 v11, 229376, v11
M v[224:227],a[0:3],a[84:87],v[224:227]
T a[80:81],v9,18432
s_mov_b32 s73, 0x8000a800
M v[228:231],a[4:7],a[84:87],v[228:231]
T a[82:83],v9,24576
v_cndmask_b32 v31, v37, 0, s[72:73]
M v[232:235],a[8:11],a[84:87],v[232:235]
X a[48:51],v8,20480
v_mov_b32 v40, 0x3f803f80
M v[236:239],a[12:15],a[84:87],v[236:239]
s_add_u32 m0, s55, 8192
v_mov_b32 v41, 0x3f803f80
M v[240:243],a[16:19],a[84:87],v[240:243]
global_load_lds_dwordx4 v11, s[50:51] offset:0
v_mov_b32 v42, 0x3f803f80
M v[244:247],a[20:23],a[84:87],v[244:247]
global_load_lds_dwordx4 v11, s[52:53] offset:1024
v_add_u32 v11, 229376, v11
M v[248:251],a[24:27],a[84:87],v[248:251]
X a[52:55],v8,21504
v_mov_b32 v43, 0x3f803f80
M v[252:255],a[28:31],a[84:87],v[252:255]
s_add_u32 m0, s54, 147456
s_mov_b32 s74, 0x3e8293ee
M v[60:63],a[64:67],a[84:87],v[60:63]
global_load_lds_dwordx4 v12, s[40:41] offset:0 sc1
s_add_u32 s68, s58, 147456
s_addc_u32 s69, s59, 0
M v[64:67],a[88:91],a[0:3],v[64:67]
T a[84:85],v32,18432
M v[68:71],a[88:91],a[4:7],v[68:71]
T a[86:87],v32,24576
M v[72:75],a[88:91],a[8:11],v[72:75]
X a[56:59],v8,22528
M v[76:79],a[88:91],a[12:15],v[76:79]
global_load_lds_dwordx4 v12, s[40:41] offset:1024 sc1
M v[80:83],a[88:91],a[16:19],v[80:83]
global_load_lds_dwordx4 v12, s[40:41] offset:2048 sc1
M v[84:87],a[88:91],a[20:23],v[84:87]
M v[88:91],a[88:91],a[24:27],v[88:91]
X a[60:63],v8,23552
M v[92:95],a[88:91],a[28:31],v[92:95]
global_load_lds_dwordx4 v12, s[40:41] offset:3072 sc1
M v[96:99],a[92:95],a[0:3],v[96:99]
T a[88:89],v9,16384
M v[100:103],a[92:95],a[4:7],v[100:103]
T a[90:91],v9,22528
M v[104:107],a[92:95],a[8:11],v[104:107]
v_add_u32 v12, 0x4000, v12
M v[108:111],a[92:95],a[12:15],v[108:111]
M v[112:115],a[92:95],a[16:19],v[112:115]
M v[116:119],a[92:95],a[20:23],v[116:119]
M v[120:123],a[92:95],a[24:27],v[120:123]
M v[124:127],a[92:95],a[28:31],v[124:127]
s_waitcnt lgkmcnt(2)
M v[128:131],a[72:75],a[32:35],v[128:131]
T a[92:93],v32,16384
M v[132:135],a[72:75],a[36:39],v[132:135]
T a[94:95],v32,22528
M v[136:139],a[72:75],a[40:43],v[136:139]
M v[140:143],a[72:75],a[44:47],v[140:143]
M v[144:147],a[72:75],a[48:51],v[144:147]
M v[148:151],a[72:75],a[52:55],v[148:151]
M v[152:155],a[72:75],a[56:59],v[152:155]
M v[156:159],a[72:75],a[60:63],v[156:159]
M v[48:51],a[72:75],a[68:71],v[48:51]
s_waitcnt vmcnt(28) lgkmcnt(0)
s_barrier
M v[160:163],a[76:79],a[32:35],v[160:163]
T a[72:73],v9,33792
s_add_u32 m0, s54, 0
M v[164:167],a[76:79],a[36:39],v[164:167]
T a[74:75],v9,39936
global_load_lds_dwordx4 v12, s[40:41] offset:0 sc1
M v[168:171],a[76:79],a[40:43],v[168:171]
X a[64:67],v10,32768
M v[172:175],a[76:79],a[44:47],v[172:175]
M v[176:179],a[76:79],a[48:51],v[176:179]
X a[0:3],v8,32768
M v[180:183],a[76:79],a[52:55],v[180:183]
global_load_lds_dwordx4 v12, s[40:41] offset:1024 sc1
M v[184:187],a[76:79],a[56:59],v[184:187]
M v[188:191],a[76:79],a[60:63],v[188:191]
global_load_lds_dwordx4 v12, s[40:41] offset:2048 sc1
M v[52:55],a[76:79],a[68:71],v[52:55]
X a[4:7],v8,33792
M v[192:195],a[32:35],a[80:83],v[192:195]
T a[76:77],v32,33792
M v[196:199],a[36:39],a[80:83],v[196:199]
T a[78:79],v32,39936
M v[200:203],a[40:43],a[80:83],v[200:203]
global_load_lds_dwordx4 v12, s[40:41] offset:3072 sc1
v_add_u32 v12, 0x4000, v12
M v[204:207],a[44:47],a[80:83],v[204:207]
X a[8:11],v8,34816
M v[208:211],a[48:51],a[80:83],v[208:211]
s_add_u32 m0, s55, 16384
M v[212:215],a[52:55],a[80:83],v[212:215]
global_load_lds_dwordx4 v11, s[50:51] offset:0
M v[216:219],a[56:59],a[80:83],v[216:219]
M v[220:223],a[60:63],a[80:83],v[220:223]
X a[12:15],v8,35840
M v[56:59],a[68:71],a[80:83],v[56:59]
global_load_lds_dwordx4 v11, s[52:53] offset:1024
v_add_u32 v11, 229376, v11
M v[224:227],a[32:35],a[84:87],v[224:227]
T a[80:81],v9,34816
M v[228:231],a[36:39],a[84:87],v[228:231]
T a[82:83],v9,40960
M v[232:235],a[40:43],a[84:87],v[232:235]
X a[16:19],v8,36864
M v[236:239],a[44:47],a[84:87],v[236:239]
s_add_u32 m0, s55, 24576
M v[240:243],a[48:51],a[84:87],v[240:243]
global_load_lds_dwordx4 v11, s[50:51] offset:0
M v[244:247],a[52:55],a[84:87],v[244:247]
global_load_lds_dwordx4 v11, s[52:53] offset:1024
v_add_u32 v11, 229376, v11
M v[248:251],a[56:59],a[84:87],v[248:251]
X a[20:23],v8,37888
M v[252:255],a[60:63],a[84:87],v[252:255]
s_add_u32 m0, s54, 16384
M v[60:63],a[68:71],a[84:87],v[60:63]
global_load_lds_dwordx4 v12, s[40:41] offset:0 sc1
M v[64:67],a[88:91],a[32:35],v[64:67]
T a[84:85],v32,34816
M v[68:71],a[88:91],a[36:39],v[68:71]
T a[86:87],v32,40960
M v[72:75],a[88:91],a[40:43],v[72:75]
X a[24:27],v8,38912
M v[76:79],a[88:91],a[44:47],v[76:79]
global_load_lds_dwordx4 v12, s[40:41] offset:1024 sc1
M v[80:83],a[88:91],a[48:51],v[80:83]
global_load_lds_dwordx4 v12, s[40:41] offset:2048 sc1
M v[84:87],a[88:91],a[52:55],v[84:87]
M v[88:91],a[88:91],a[56:59],v[88:91]
X a[28:31],v8,39936
M v[92:95],a[88:91],a[60:63],v[92:95]
global_load_lds_dwordx4 v12, s[40:41] offset:3072 sc1
M v[96:99],a[92:95],a[32:35],v[96:99]
T a[88:89],v9,32768
M v[100:103],a[92:95],a[36:39],v[100:103]
T a[90:91],v9,38912
M v[104:107],a[92:95],a[40:43],v[104:107]
v_add_u32 v12, 0x4000, v12
M v[108:111],a[92:95],a[44:47],v[108:111]
M v[112:115],a[92:95],a[48:51],v[112:115]
M v[116:119],a[92:95],a[52:55],v[116:119]
M v[120:123],a[92:95],a[56:59],v[120:123]
M v[124:127],a[92:95],a[60:63],v[124:127]
s_waitcnt lgkmcnt(2)
M v[128:131],a[72:75],a[0:3],v[128:131]
T a[92:93],v32,32768
M v[132:135],a[72:75],a[4:7],v[132:135]
T a[94:95],v32,38912
M v[136:139],a[72:75],a[8:11],v[136:139]
M v[140:143],a[72:75],a[12:15],v[140:143]
M v[144:147],a[72:75],a[16:19],v[144:147]
M v[148:151],a[72:75],a[20:23],v[148:151]
M v[152:155],a[72:75],a[24:27],v[152:155]
M v[156:159],a[72:75],a[28:31],v[156:159]
M v[48:51],a[72:75],a[64:67],v[48:51]
s_waitcnt vmcnt(24) lgkmcnt(0)
s_barrier
M v[160:163],a[76:79],a[0:3],v[160:163]
T a[72:73],v9,50176
s_add_u32 m0, s54, 32768
M v[164:167],a[76:79],a[4:7],v[164:167]
T a[74:75],v9,56320
global_load_lds_dwordx4 v12, s[40:41] offset:0 sc1
M v[168:171],a[76:79],a[8:11],v[168:171]
X a[68:71],v10,49152
M v[172:175],a[76:79],a[12:15],v[172:175]
M v[176:179],a[76:79],a[16:19],v[176:179]
X a[32:35],v8,49152
M v[180:183],a[76:79],a[20:23],v[180:183]
M v[184:187],a[76:79],a[24:27],v[184:187]
global_load_lds_dwordx4 v12, s[40:41] offset:1024 sc1
M v[188:191],a[76:79],a[28:31],v[188:191]
M v[52:55],a[76:79],a[64:67],v[52:55]
X a[36:39],v8,50176
M v[192:195],a[0:3],a[80:83],v[192:195]
T a[76:77],v32,50176
M v[196:199],a[4:7],a[80:83],v[196:199]
T a[78:79],v32,56320
M v[200:203],a[8:11],a[80:83],v[200:203]
global_load_lds_dwordx4 v12, s[40:41] offset:2048 sc1
M v[204:207],a[12:15],a[80:83],v[204:207]
X a[40:43],v8,51200
M v[208:211],a[16:19],a[80:83],v[208:211]
M v[212:215],a[20:23],a[80:83],v[212:215]
M v[216:219],a[24:27],a[80:83],v[216:219]
M v[220:223],a[28:31],a[80:83],v[220:223]
X a[44:47],v8,52224
M v[56:59],a[64:67],a[80:83],v[56:59]
global_load_lds_dwordx4 v12, s[40:41] offset:3072 sc1
v_add_u32 v12, 0x4000, v12
M v[224:227],a[0:3],a[84:87],v[224:227]
T a[80:81],v9,51200
M v[228:231],a[4:7],a[84:87],v[228:231]
T a[82:83],v9,57344
M v[232:235],a[8:11],a[84:87],v[232:235]
X a[48:51],v8,53248
M v[236:239],a[12:15],a[84:87],v[236:239]
s_add_u32 m0, s55, 32768
M v[240:243],a[16:19],a[84:87],v[240:243]
global_load_lds_dwordx4 v11, s[50:51] offset:0
M v[244:247],a[20:23],a[84:87],v[244:247]
M v[248:251],a[24:27],a[84:87],v[248:251]
X a[52:55],v8,54272
M v[252:255],a[28:31],a[84:87],v[252:255]
M v[60:63],a[64:67],a[84:87],v[60:63]
global_load_lds_dwordx4 v11, s[52:53] offset:1024
v_add_u32 v11, 229376, v11
M v[64:67],a[88:91],a[0:3],v[64:67]
T a[84:85],v32,51200
M v[68:71],a[88:91],a[4:7],v[68:71]
T a[86:87],v32,57344
M v[72:75],a[88:91],a[8:11],v[72:75]
X a[56:59],v8,55296
M v[76:79],a[88:91],a[12:15],v[76:79]
s_add_u32 m0, s55, 40960
M v[80:83],a[88:91],a[16:19],v[80:83]
global_load_lds_dwordx4 v11, s[50:51] offset:0
M v[84:87],a[88:91],a[20:23],v[84:87]
M v[88:91],a[88:91],a[24:27],v[88:91]
X a[60:63],v8,56320
M v[92:95],a[88:91],a[28:31],v[92:95]
M v[96:99],a[92:95],a[0:3],v[96:99]
T a[88:89],v9,49152
M v[100:103],a[92:95],a[4:7],v[100:103]
T a[90:91],v9,55296
M v[104:107],a[92:95],a[8:11],v[104:107]
global_load_lds_dwordx4 v11, s[52:53] offset:1024
M v[108:111],a[92:95],a[12:15],v[108:111]
v_add_u32 v11, 229376, v11
M v[112:115],a[92:95],a[16:19],v[112:115]
M v[116:119],a[92:95],a[20:23],v[116:119]
M v[120:123],a[92:95],a[24:27],v[120:123]
M v[124:127],a[92:95],a[28:31],v[124:127]
s_waitcnt lgkmcnt(2)
M v[128:131],a[72:75],a[32:35],v[128:131]
T a[92:93],v32,49152
M v[132:135],a[72:75],a[36:39],v[132:135]
T a[94:95],v32,55296
M v[136:139],a[72:75],a[40:43],v[136:139]
v_lshrrev_b32 v10, 9, v16
M v[140:143],a[72:75],a[44:47],v[140:143]
v_lshlrev_b32 v10, 10, v10
M v[144:147],a[72:75],a[48:51],v[144:147]
v_sub_u32 v9, v9, v10
M v[148:151],a[72:75],a[52:55],v[148:151]
v_xor_b32 v32, 32, v9
M v[152:155],a[72:75],a[56:59],v[152:155]
v_add_u32 v13, 131072, v8
M v[156:159],a[72:75],a[60:63],v[156:159]
M v[48:51],a[72:75],a[68:71],v[48:51]
s_waitcnt vmcnt(26) lgkmcnt(0)
s_barrier
M v[160:163],a[76:79],a[32:35],v[160:163]
T a[72:73],v9,0
s_add_u32 m0, s54, 49152
M v[164:167],a[76:79],a[36:39],v[164:167]
T a[74:75],v9,4096
global_load_lds_dwordx4 v12, s[40:41] offset:0 sc1
M v[168:171],a[76:79],a[40:43],v[168:171]
X a[0:3],v13,0
M v[172:175],a[76:79],a[44:47],v[172:175]
M v[176:179],a[76:79],a[48:51],v[176:179]
X a[4:7],v13,1024
M v[180:183],a[76:79],a[52:55],v[180:183]
M v[184:187],a[76:79],a[56:59],v[184:187]
M v[188:191],a[76:79],a[60:63],v[188:191]
M v[52:55],a[76:79],a[68:71],v[52:55]
M v[192:195],a[32:35],a[80:83],v[192:195]
T a[76:77],v9,1024
M v[196:199],a[36:39],a[80:83],v[196:199]
T a[78:79],v9,5120
M v[200:203],a[40:43],a[80:83],v[200:203]
X a[8:11],v13,2048
M v[204:207],a[44:47],a[80:83],v[204:207]
global_load_lds_dwordx4 v12, s[40:41] offset:1024 sc1
M v[208:211],a[48:51],a[80:83],v[208:211]
M v[212:215],a[52:55],a[80:83],v[212:215]
X a[12:15],v13,3072
M v[216:219],a[56:59],a[80:83],v[216:219]
M v[220:223],a[60:63],a[80:83],v[220:223]
M v[56:59],a[68:71],a[80:83],v[56:59]
M v[224:227],a[32:35],a[84:87],v[224:227]
T a[80:81],v32,0
M v[228:231],a[36:39],a[84:87],v[228:231]
T a[82:83],v32,4096
M v[232:235],a[40:43],a[84:87],v[232:235]
X a[16:19],v13,4096
M v[236:239],a[44:47],a[84:87],v[236:239]
global_load_lds_dwordx4 v12, s[40:41] offset:2048 sc1
M v[240:243],a[48:51],a[84:87],v[240:243]
M v[244:247],a[52:55],a[84:87],v[244:247]
X a[20:23],v13,5120
M v[248:251],a[56:59],a[84:87],v[248:251]
M v[252:255],a[60:63],a[84:87],v[252:255]
M v[60:63],a[68:71],a[84:87],v[60:63]
M v[64:67],a[88:91],a[32:35],v[64:67]
M v[68:71],a[88:91],a[36:39],v[68:71]
X a[24:27],v13,6144
M v[72:75],a[88:91],a[40:43],v[72:75]
M v[76:79],a[88:91],a[44:47],v[76:79]
M v[80:83],a[88:91],a[48:51],v[80:83]
global_load_lds_dwordx4 v12, s[40:41] offset:3072 sc1
M v[84:87],a[88:91],a[52:55],v[84:87]
v_add_u32 v12, 0x4000, v12
M v[88:91],a[88:91],a[56:59],v[88:91]
X a[28:31],v13,7168
M v[92:95],a[88:91],a[60:63],v[92:95]
M v[96:99],a[92:95],a[32:35],v[96:99]
M v[100:103],a[92:95],a[36:39],v[100:103]
M v[104:107],a[92:95],a[40:43],v[104:107]
M v[108:111],a[92:95],a[44:47],v[108:111]
M v[112:115],a[92:95],a[48:51],v[112:115]
M v[116:119],a[92:95],a[52:55],v[116:119]
M v[120:123],a[92:95],a[56:59],v[120:123]
M v[124:127],a[92:95],a[60:63],v[124:127]
s_waitcnt lgkmcnt(0)
v_cvt_pk_bf16_f32 v48, v48, v49
M a[128:131],a[72:75],a[0:3],0
T a[84:85],v32,1024
v_cvt_pk_bf16_f32 v49, v50, v51
M a[132:135],a[72:75],a[4:7],0
T a[86:87],v32,5120
v_cvt_pk_bf16_f32 v50, v52, v53
M a[136:139],a[72:75],a[8:11],0
v_cvt_pk_bf16_f32 v51, v54, v55
v_cvt_pk_bf16_f32 v128, v128, v129
M a[140:143],a[72:75],a[12:15],0
v_cvt_pk_bf16_f32 v129, v130, v131
v_cvt_pk_bf16_f32 v130, v160, v161
M a[144:147],a[72:75],a[16:19],0
v_cvt_pk_bf16_f32 v131, v162, v163
v_cvt_pk_bf16_f32 v132, v132, v133
M a[148:151],a[72:75],a[20:23],0
v_cvt_pk_bf16_f32 v133, v134, v135
v_cvt_pk_bf16_f32 v134, v164, v165
M a[152:155],a[72:75],a[24:27],0
v_cvt_pk_bf16_f32 v135, v166, v167
v_cvt_pk_bf16_f32 v136, v136, v137
M a[156:159],a[72:75],a[28:31],0
s_waitcnt vmcnt(24) lgkmcnt(0)
s_barrier
M a[192:195],a[76:79],a[0:3],0
T a[72:73],v9,8192
s_add_u32 m0, s54, 131072
M a[196:199],a[76:79],a[4:7],0
T a[74:75],v9,12288
global_load_lds_dwordx4 v12, s[40:41] offset:0 sc1
M a[200:203],a[76:79],a[8:11],0
X a[32:35],v13,16384
v_cvt_pk_bf16_f32 v137, v138, v139
M a[204:207],a[76:79],a[12:15],0
X a[36:39],v13,17408
v_cvt_pk_bf16_f32 v138, v168, v169
M a[208:211],a[76:79],a[16:19],0
X a[40:43],v13,18432
v_cvt_pk_bf16_f32 v139, v170, v171
M a[212:215],a[76:79],a[20:23],0
global_load_lds_dwordx4 v12, s[40:41] offset:1024 sc1
v_cvt_pk_bf16_f32 v140, v140, v141
M a[216:219],a[76:79],a[24:27],0
X a[44:47],v13,19456
v_cvt_pk_bf16_f32 v141, v142, v143
M a[220:223],a[76:79],a[28:31],0
global_load_lds_dwordx4 v12, s[40:41] offset:2048 sc1
v_cvt_pk_bf16_f32 v142, v172, v173
M a[160:163],a[80:83],a[0:3],0
T a[76:77],v9,9216
v_cvt_pk_bf16_f32 v143, v174, v175
M a[164:167],a[80:83],a[4:7],0
T a[78:79],v9,13312
v_cvt_pk_bf16_f32 v144, v144, v145
M a[168:171],a[80:83],a[8:11],0
X a[48:51],v13,20480
v_cvt_pk_bf16_f32 v145, v146, v147
M a[172:175],a[80:83],a[12:15],0
X a[52:55],v13,21504
v_cvt_pk_bf16_f32 v146, v176, v177
M a[176:179],a[80:83],a[16:19],0
X a[56:59],v13,22528
v_cvt_pk_bf16_f32 v147, v178, v179
M a[180:183],a[80:83],a[20:23],0
global_load_lds_dwordx4 v12, s[40:41] offset:3072 sc1
v_cvt_pk_bf16_f32 v148, v148, v149
M a[184:187],a[80:83],a[24:27],0
X a[60:63],v13,23552
v_cvt_pk_bf16_f32 v149, v150, v151
M a[188:191],a[80:83],a[28:31],0
v_add_u32 v12, 0x4000, v12
v_cvt_pk_bf16_f32 v150, v180, v181
M a[224:227],a[84:87],a[0:3],0
T a[80:81],v32,8192
v_cvt_pk_bf16_f32 v151, v182, v183
M a[228:231],a[84:87],a[4:7],0
T a[82:83],v32,12288
v_cvt_pk_bf16_f32 v152, v152, v153
M a[232:235],a[84:87],a[8:11],0
s_add_u32 m0, s55, 0
v_cvt_pk_bf16_f32 v153, v154, v155
M a[236:239],a[84:87],a[12:15],0
global_load_lds_dwordx4 v11, s[50:51] offset:0
v_cvt_pk_bf16_f32 v154, v184, v185
M a[240:243],a[84:87],a[16:19],0
global_load_lds_dwordx4 v11, s[52:53] offset:1024
v_cvt_pk_bf16_f32 v155, v186, v187
M a[244:247],a[84:87],a[20:23],0
v_add_u32 v11, 229376, v11
v_cvt_pk_bf16_f32 v156, v156, v157
M a[248:251],a[84:87],a[24:27],0
v_cvt_pk_bf16_f32 v157, v158, v159
v_cvt_pk_bf16_f32 v158, v188, v189
M a[252:255],a[84:87],a[28:31],0
s_waitcnt lgkmcnt(2)
v_cvt_pk_bf16_f32 v159, v190, v191
M a[128:131],a[72:75],a[32:35],a[128:131]
T a[84:85],v32,9216
v_cvt_pk_bf16_f32 v64, v64, v65
M a[132:135],a[72:75],a[36:39],a[132:135]
T a[86:87],v32,13312
v_cvt_pk_bf16_f32 v65, v66, v67
M a[136:139],a[72:75],a[40:43],a[136:139]
v_cvt_pk_bf16_f32 v66, v96, v97
v_cvt_pk_bf16_f32 v67, v98, v99
M a[140:143],a[72:75],a[44:47],a[140:143]
v_cvt_pk_bf16_f32 v68, v68, v69
v_cvt_pk_bf16_f32 v69, v70, v71
M a[144:147],a[72:75],a[48:51],a[144:147]
v_cvt_pk_bf16_f32 v70, v100, v101
v_cvt_pk_bf16_f32 v71, v102, v103
M a[148:151],a[72:75],a[52:55],a[148:151]
v_cvt_pk_bf16_f32 v72, v72, v73
v_cvt_pk_bf16_f32 v73, v74, v75
M a[152:155],a[72:75],a[56:59],a[152:155]
v_cvt_pk_bf16_f32 v74, v104, v105
v_cvt_pk_bf16_f32 v75, v106, v107
M a[156:159],a[72:75],a[60:63],a[156:159]
s_waitcnt vmcnt(24) lgkmcnt(0)
s_barrier
M a[192:195],a[76:79],a[32:35],a[192:195]
T a[72:73],v9,16384
s_add_u32 m0, s54, 147456
M a[196:199],a[76:79],a[36:39],a[196:199]
T a[74:75],v9,20480
global_load_lds_dwordx4 v12, s[40:41] offset:0 sc1
M a[200:203],a[76:79],a[40:43],a[200:203]
X a[0:3],v8,0
v_cvt_pk_bf16_f32 v76, v76, v77
M a[204:207],a[76:79],a[44:47],a[204:207]
X a[4:7],v8,1024
v_cvt_pk_bf16_f32 v77, v78, v79
M a[208:211],a[76:79],a[48:51],a[208:211]
X a[8:11],v8,2048
v_cvt_pk_bf16_f32 v78, v108, v109
M a[212:215],a[76:79],a[52:55],a[212:215]
global_load_lds_dwordx4 v12, s[40:41] offset:1024 sc1
v_cvt_pk_bf16_f32 v79, v110, v111
M a[216:219],a[76:79],a[56:59],a[216:219]
X a[12:15],v8,3072
v_cvt_pk_bf16_f32 v80, v80, v81
M a[220:223],a[76:79],a[60:63],a[220:223]
global_load_lds_dwordx4 v12, s[40:41] offset:2048 sc1
v_cvt_pk_bf16_f32 v81, v82, v83
M a[160:163],a[80:83],a[32:35],a[160:163]
T a[76:77],v9,17408
v_cvt_pk_bf16_f32 v82, v112, v113
M a[164:167],a[80:83],a[36:39],a[164:167]
T a[78:79],v9,21504
v_cvt_pk_bf16_f32 v83, v114, v115
M a[168:171],a[80:83],a[40:43],a[168:171]
X a[16:19],v8,4096
v_cvt_pk_bf16_f32 v84, v84, v85
M a[172:175],a[80:83],a[44:47],a[172:175]
X a[20:23],v8,5120
v_cvt_pk_bf16_f32 v85, v86, v87
M a[176:179],a[80:83],a[48:51],a[176:179]
X a[24:27],v8,6144
v_cvt_pk_bf16_f32 v86, v116, v117
M a[180:183],a[80:83],a[52:55],a[180:183]
global_load_lds_dwordx4 v12, s[40:41] offset:3072 sc1
v_cvt_pk_bf16_f32 v87, v118, v119
M a[184:187],a[80:83],a[56:59],a[184:187]
X a[28:31],v8,7168
v_cvt_pk_bf16_f32 v88, v88, v89
M a[188:191],a[80:83],a[60:63],a[188:191]
v_add_u32 v12, 0x4000, v12
v_cvt_pk_bf16_f32 v89, v90, v91
M a[224:227],a[84:87],a[32:35],a[224:227]
T a[80:81],v32,16384
v_cvt_pk_bf16_f32 v90, v120, v121
M a[228:231],a[84:87],a[36:39],a[228:231]
T a[82:83],v32,20480
v_cvt_pk_bf16_f32 v91, v122, v123
M a[232:235],a[84:87],a[40:43],a[232:235]
s_add_u32 m0, s55, 8192
v_cvt_pk_bf16_f32 v92, v92, v93
M a[236:239],a[84:87],a[44:47],a[236:239]
global_load_lds_dwordx4 v11, s[50:51] offset:0
v_cvt_pk_bf16_f32 v93, v94, v95
M a[240:243],a[84:87],a[48:51],a[240:243]
global_load_lds_dwordx4 v11, s[52:53] offset:1024
v_cvt_pk_bf16_f32 v94, v124, v125
M a[244:247],a[84:87],a[52:55],a[244:247]
v_add_u32 v11, 229376, v11
v_cvt_pk_bf16_f32 v95, v126, v127
M a[248:251],a[84:87],a[56:59],a[248:251]
v_cvt_pk_bf16_f32 v96, v56, v57
v_cvt_pk_bf16_f32 v97, v58, v59
M a[252:255],a[84:87],a[60:63],a[252:255]
s_waitcnt lgkmcnt(2)
v_cvt_pk_bf16_f32 v98, v192, v193
M a[128:131],a[72:75],a[0:3],a[128:131]
T a[84:85],v32,17408
v_cvt_pk_bf16_f32 v99, v194, v195
M a[132:135],a[72:75],a[4:7],a[132:135]
T a[86:87],v32,21504
v_cvt_pk_bf16_f32 v100, v196, v197
M a[136:139],a[72:75],a[8:11],a[136:139]
v_cvt_pk_bf16_f32 v101, v198, v199
v_cvt_pk_bf16_f32 v102, v200, v201
M a[140:143],a[72:75],a[12:15],a[140:143]
v_cvt_pk_bf16_f32 v103, v202, v203
v_cvt_pk_bf16_f32 v104, v204, v205
M a[144:147],a[72:75],a[16:19],a[144:147]
v_cvt_pk_bf16_f32 v105, v206, v207
v_cvt_pk_bf16_f32 v106, v208, v209
M a[148:151],a[72:75],a[20:23],a[148:151]
v_cvt_pk_bf16_f32 v107, v210, v211
v_cvt_pk_bf16_f32 v108, v212, v213
M a[152:155],a[72:75],a[24:27],a[152:155]
v_cvt_pk_bf16_f32 v109, v214, v215
v_cvt_pk_bf16_f32 v110, v216, v217
M a[156:159],a[72:75],a[28:31],a[156:159]
s_waitcnt vmcnt(24) lgkmcnt(0)
s_barrier
M a[192:195],a[76:79],a[0:3],a[192:195]
T a[72:73],v9,24576
s_add_u32 m0, s54, 0
M a[196:199],a[76:79],a[4:7],a[196:199]
T a[74:75],v9,28672
global_load_lds_dwordx4 v12, s[40:41] offset:0 sc1
M a[200:203],a[76:79],a[8:11],a[200:203]
X a[32:35],v8,16384
v_cvt_pk_bf16_f32 v111, v218, v219
M a[204:207],a[76:79],a[12:15],a[204:207]
X a[36:39],v8,17408
v_cvt_pk_bf16_f32 v112, v220, v221
M a[208:211],a[76:79],a[16:19],a[208:211]
X a[40:43],v8,18432
v_cvt_pk_bf16_f32 v113, v222, v223
M a[212:215],a[76:79],a[20:23],a[212:215]
global_load_lds_dwordx4 v12, s[40:41] offset:1024 sc1
v_cvt_pk_bf16_f32 v160, v60, v61
M a[216:219],a[76:79],a[24:27],a[216:219]
X a[44:47],v8,19456
v_cvt_pk_bf16_f32 v161, v62, v63
M a[220:223],a[76:79],a[28:31],a[220:223]
global_load_lds_dwordx4 v12, s[40:41] offset:2048 sc1
v_cvt_pk_bf16_f32 v162, v224, v225
M a[160:163],a[80:83],a[0:3],a[160:163]
T a[76:77],v9,25600
v_cvt_pk_bf16_f32 v163, v226, v227
M a[164:167],a[80:83],a[4:7],a[164:167]
T a[78:79],v9,29696
v_cvt_pk_bf16_f32 v164, v228, v229
M a[168:171],a[80:83],a[8:11],a[168:171]
X a[48:51],v8,20480
v_cvt_pk_bf16_f32 v165, v230, v231
M a[172:175],a[80:83],a[12:15],a[172:175]
X a[52:55],v8,21504
v_cvt_pk_bf16_f32 v166, v232, v233
M a[176:179],a[80:83],a[16:19],a[176:179]
X a[56:59],v8,22528
v_cvt_pk_bf16_f32 v167, v234, v235
M a[180:183],a[80:83],a[20:23],a[180:183]
global_load_lds_dwordx4 v12, s[40:41] offset:3072 sc1
v_cvt_pk_bf16_f32 v168, v236, v237
M a[184:187],a[80:83],a[24:27],a[184:187]
X a[60:63],v8,23552
v_cvt_pk_bf16_f32 v169, v238, v239
M a[188:191],a[80:83],a[28:31],a[188:191]
v_add_u32 v12, 0x4000, v12
v_cvt_pk_bf16_f32 v170, v240, v241
M a[224:227],a[84:87],a[0:3],a[224:227]
T a[80:81],v32,24576
v_cvt_pk_bf16_f32 v171, v242, v243
M a[228:231],a[84:87],a[4:7],a[228:231]
T a[82:83],v32,28672
v_cvt_pk_bf16_f32 v172, v244, v245
M a[232:235],a[84:87],a[8:11],a[232:235]
s_add_u32 m0, s55, 16384
v_cvt_pk_bf16_f32 v173, v246, v247
M a[236:239],a[84:87],a[12:15],a[236:239]
global_load_lds_dwordx4 v11, s[50:51] offset:0
v_cvt_pk_bf16_f32 v174, v248, v249
M a[240:243],a[84:87],a[16:19],a[240:243]
global_load_lds_dwordx4 v11, s[52:53] offset:1024
v_cvt_pk_bf16_f32 v175, v250, v251
M a[244:247],a[84:87],a[20:23],a[244:247]
v_add_u32 v11, 229376, v11
v_cvt_pk_bf16_f32 v176, v252, v253
M a[248:251],a[84:87],a[24:27],a[248:251]
v_cvt_pk_bf16_f32 v177, v254, v255
M v[192:195],v[48:51],v[64:67],v[44:47]
M v[224:227],v[128:131],v[68:71],v[24:27]
M v[196:199],v[128:131],v[64:67],v[28:31]
M v[228:231],v[132:135],v[68:71],v[28:31]
M a[252:255],a[84:87],a[28:31],a[252:255]
s_waitcnt lgkmcnt(2)
M a[128:131],a[72:75],a[32:35],a[128:131]
T a[84:85],v32,25600
M a[132:135],a[72:75],a[36:39],a[132:135]
T a[86:87],v32,29696
v_max3_f32 v200, v192, v193, v194
M a[136:139],a[72:75],a[40:43],a[136:139]
v_max3_f32 v232, v224, v225, v226
v_max3_f32 v201, v195, v196, v197
M a[140:143],a[72:75],a[44:47],a[140:143]
v_max3_f32 v233, v227, v228, v229
v_max3_f32 v200, v200, v198, v199
M a[144:147],a[72:75],a[48:51],a[144:147]
v_max3_f32 v232, v232, v230, v231
v_max_f32 v200, v200, v201
M a[148:151],a[72:75],a[52:55],a[148:151]
v_max_f32 v232, v232, v233
v_mov_b32 v201, v200
M a[152:155],a[72:75],a[56:59],a[152:155]
v_mov_b32 v233, v232
v_permlane32_swap_b32 v200, v201
M a[156:159],a[72:75],a[60:63],a[156:159]
s_waitcnt vmcnt(24) lgkmcnt(0)
s_barrier
M a[192:195],a[76:79],a[32:35],a[192:195]
T a[72:73],v9,32768
s_add_u32 m0, s54, 16384
M a[196:199],a[76:79],a[36:39],a[196:199]
T a[74:75],v9,36864
global_load_lds_dwordx4 v12, s[40:41] offset:0 sc1
M a[200:203],a[76:79],a[40:43],a[200:203]
X a[0:3],v8,32768
v_permlane32_swap_b32 v232, v233
M a[204:207],a[76:79],a[44:47],a[204:207]
X a[4:7],v8,33792
v_max_f32 v200, v200, v201
M a[208:211],a[76:79],a[48:51],a[208:211]
X a[8:11],v8,34816
v_max_f32 v232, v232, v233
M a[212:215],a[76:79],a[52:55],a[212:215]
global_load_lds_dwordx4 v12, s[40:41] offset:1024 sc1
v_mov_b32 v201, v200
M a[216:219],a[76:79],a[56:59],a[216:219]
X a[12:15],v8,35840
v_mov_b32 v233, v232
M a[220:223],a[76:79],a[60:63],a[220:223]
global_load_lds_dwordx4 v12, s[40:41] offset:2048 sc1
v_permlane16_swap_b32 v200, v201
M a[160:163],a[80:83],a[32:35],a[160:163]
T a[76:77],v9,33792
v_permlane16_swap_b32 v232, v233
M a[164:167],a[80:83],a[36:39],a[164:167]
T a[78:79],v9,37888
v_max_f32 v200, v200, v201
M a[168:171],a[80:83],a[40:43],a[168:171]
X a[16:19],v8,36864
v_max_f32 v232, v232, v233
M a[172:175],a[80:83],a[44:47],a[172:175]
X a[20:23],v8,37888
v_mul_f32 v200, s74, v200
M a[176:179],a[80:83],a[48:51],a[176:179]
X a[24:27],v8,38912
v_mul_f32 v232, s74, v232
M a[180:183],a[80:83],a[52:55],a[180:183]
global_load_lds_dwordx4 v12, s[40:41] offset:3072 sc1
v_fma_f32 v192, v192, s74, -v200
M a[184:187],a[80:83],a[56:59],a[184:187]
X a[28:31],v8,39936
v_fma_f32 v224, v224, s74, -v232
M a[188:191],a[80:83],a[60:63],a[188:191]
v_add_u32 v12, 0x4000, v12
v_fma_f32 v193, v193, s74, -v200
M a[224:227],a[84:87],a[32:35],a[224:227]
T a[80:81],v32,32768
v_fma_f32 v225, v225, s74, -v232
M a[228:231],a[84:87],a[36:39],a[228:231]
T a[82:83],v32,36864
v_fma_f32 v194, v194, s74, -v200
M a[232:235],a[84:87],a[40:43],a[232:235]
s_add_u32 m0, s55, 24576
v_fma_f32 v226, v226, s74, -v232
M a[236:239],a[84:87],a[44:47],a[236:239]
global_load_lds_dwordx4 v11, s[50:51] offset:0
v_fma_f32 v195, v195, s74, -v200
M a[240:243],a[84:87],a[48:51],a[240:243]
global_load_lds_dwordx4 v11, s[52:53] offset:1024
v_fma_f32 v227, v227, s74, -v232
M a[244:247],a[84:87],a[52:55],a[244:247]
v_add_u32 v11, 229376, v11
v_fma_f32 v196, v196, s74, -v200
M a[248:251],a[84:87],a[56:59],a[248:251]
v_fma_f32 v228, v228, s74, -v232
v_fma_f32 v197, v197, s74, -v200
M a[252:255],a[84:87],a[60:63],a[252:255]
s_waitcnt lgkmcnt(2)
v_fma_f32 v229, v229, s74, -v232
M a[128:131],a[72:75],a[0:3],a[128:131]
T a[84:85],v32,33792
v_fma_f32 v198, v198, s74, -v200
M a[132:135],a[72:75],a[4:7],a[132:135]
T a[86:87],v32,37888
v_fma_f32 v230, v230, s74, -v232
M a[136:139],a[72:75],a[8:11],a[136:139]
v_fma_f32 v199, v199, s74, -v200
v_fma_f32 v231, v231, s74, -v232
M a[140:143],a[72:75],a[12:15],a[140:143]
v_exp_f32 v192, v192
M a[144:147],a[72:75],a[16:19],a[144:147]
v_exp_f32 v224, v224
M a[148:151],a[72:75],a[20:23],a[148:151]
v_exp_f32 v193, v193
M a[152:155],a[72:75],a[24:27],a[152:155]
v_exp_f32 v225, v225
M a[156:159],a[72:75],a[28:31],a[156:159]
s_waitcnt vmcnt(24) lgkmcnt(0)
s_barrier
M a[192:195],a[76:79],a[0:3],a[192:195]
T a[72:73],v9,40960
s_add_u32 m0, s54, 32768
M a[196:199],a[76:79],a[4:7],a[196:199]
T a[74:75],v9,45056
global_load_lds_dwordx4 v12, s[40:41] offset:0 sc1
M a[200:203],a[76:79],a[8:11],a[200:203]
X a[32:35],v8,49152
v_exp_f32 v194, v194
M a[204:207],a[76:79],a[12:15],a[204:207]
X a[36:39],v8,50176
v_exp_f32 v226, v226
M a[208:211],a[76:79],a[16:19],a[208:211]
X a[40:43],v8,51200
v_exp_f32 v195, v195
M a[212:215],a[76:79],a[20:23],a[212:215]
global_load_lds_dwordx4 v12, s[40:41] offset:1024 sc1
v_exp_f32 v227, v227
M a[216:219],a[76:79],a[24:27],a[216:219]
X a[44:47],v8,52224
v_exp_f32 v196, v196
M a[220:223],a[76:79],a[28:31],a[220:223]
global_load_lds_dwordx4 v12, s[40:41] offset:2048 sc1
v_exp_f32 v228, v228
M a[160:163],a[80:83],a[0:3],a[160:163]
T a[76:77],v9,41984
v_exp_f32 v197, v197
M a[164:167],a[80:83],a[4:7],a[164:167]
T a[78:79],v9,46080
v_exp_f32 v229, v229
M a[168:171],a[80:83],a[8:11],a[168:171]
X a[48:51],v8,53248
v_exp_f32 v198, v198
M a[172:175],a[80:83],a[12:15],a[172:175]
X a[52:55],v8,54272
v_exp_f32 v230, v230
M a[176:179],a[80:83],a[16:19],a[176:179]
X a[56:59],v8,55296
v_exp_f32 v199, v199
M a[180:183],a[80:83],a[20:23],a[180:183]
global_load_lds_dwordx4 v12, s[40:41] offset:3072 sc1
v_cvt_pk_bf16_f32 v204, v192, v193
M a[184:187],a[80:83],a[24:27],a[184:187]
X a[60:63],v8,56320
v_cvt_pk_bf16_f32 v205, v194, v195
M a[188:191],a[80:83],a[28:31],a[188:191]
v_add_u32 v12, 0x4000, v12
v_cvt_pk_bf16_f32 v206, v196, v197
M a[224:227],a[84:87],a[0:3],a[224:227]
T a[80:81],v32,40960
v_cvt_pk_bf16_f32 v207, v198, v199
M a[228:231],a[84:87],a[4:7],a[228:231]
T a[82:83],v32,45056
M v[216:219],v[40:43],v[204:207],0
v_exp_f32 v231, v231
M a[232:235],a[84:87],a[8:11],a[232:235]
s_add_u32 m0, s55, 32768
v_cvt_pk_bf16_f32 v236, v224, v225
M a[236:239],a[84:87],a[12:15],a[236:239]
global_load_lds_dwordx4 v11, s[50:51] offset:0
v_cvt_pk_bf16_f32 v237, v226, v227
M a[240:243],a[84:87],a[16:19],a[240:243]
global_load_lds_dwordx4 v11, s[52:53] offset:1024
v_cvt_pk_bf16_f32 v238, v228, v229
M a[244:247],a[84:87],a[20:23],a[244:247]
v_add_u32 v11, 229376, v11
v_cvt_pk_bf16_f32 v239, v230, v231
M a[248:251],a[84:87],a[24:27],a[248:251]
M v[208:211],v[96:99],v[204:207],0
M v[248:251],v[40:43],v[236:239],0
M v[240:243],v[98:101],v[236:239],0
M v[212:215],v[160:163],v[204:207],0
M v[244:247],v[162:165],v[236:239],0
v_rcp_f32 v216, v216
M a[252:255],a[84:87],a[28:31],a[252:255]
s_waitcnt lgkmcnt(2)
M a[128:131],a[72:75],a[32:35],a[128:131]
T a[84:85],v32,41984
v_mul_f32 v208, v208, v216
M a[132:135],a[72:75],a[36:39],a[132:135]
T a[86:87],v32,46080
v_mul_f32 v209, v209, v216
M a[136:139],a[72:75],a[40:43],a[136:139]
v_rcp_f32 v248, v248
M a[140:143],a[72:75],a[44:47],a[140:143]
v_mul_f32 v240, v240, v248
v_mul_f32 v241, v241, v248
M a[144:147],a[72:75],a[48:51],a[144:147]
v_mul_f32 v210, v210, v216
v_mul_f32 v242, v242, v248
M a[148:151],a[72:75],a[52:55],a[148:151]
v_mul_f32 v211, v211, v216
v_mul_f32 v243, v243, v248
M a[152:155],a[72:75],a[56:59],a[152:155]
v_mul_f32 v212, v212, v216
v_mul_f32 v244, v244, v248
M a[156:159],a[72:75],a[60:63],a[156:159]
s_waitcnt vmcnt(24) lgkmcnt(0)
s_barrier
M a[192:195],a[76:79],a[32:35],a[192:195]
T a[72:73],v9,0
s_add_u32 m0, s54, 49152
M a[196:199],a[76:79],a[36:39],a[196:199]
T a[74:75],v9,4096
global_load_lds_dwordx4 v12, s[40:41] offset:0 sc1
M a[200:203],a[76:79],a[40:43],a[200:203]
X a[0:3],v13,0
v_mul_f32 v213, v213, v216
M a[204:207],a[76:79],a[44:47],a[204:207]
X a[4:7],v13,1024
v_mul_f32 v245, v245, v248
M a[208:211],a[76:79],a[48:51],a[208:211]
X a[8:11],v13,2048
v_mul_f32 v214, v214, v216
M a[212:215],a[76:79],a[52:55],a[212:215]
global_load_lds_dwordx4 v12, s[40:41] offset:1024 sc1
v_mul_f32 v246, v246, v248
M a[216:219],a[76:79],a[56:59],a[216:219]
X a[12:15],v13,3072
v_mul_f32 v215, v215, v216
M a[220:223],a[76:79],a[60:63],a[220:223]
global_load_lds_dwordx4 v12, s[40:41] offset:2048 sc1
v_mul_f32 v247, v247, v248
M a[160:163],a[80:83],a[32:35],a[160:163]
T a[76:77],v9,1024
v_cvt_pk_bf16_f32 v204, v208, v209
M a[164:167],a[80:83],a[36:39],a[164:167]
T a[78:79],v9,5120
v_cvt_pk_bf16_f32 v236, v240, v241
M a[168:171],a[80:83],a[40:43],a[168:171]
X a[16:19],v13,4096
v_cvt_pk_bf16_f32 v205, v210, v211
M a[172:175],a[80:83],a[44:47],a[172:175]
X a[20:23],v13,5120
v_cvt_pk_bf16_f32 v237, v242, v243
M a[176:179],a[80:83],a[48:51],a[176:179]
X a[24:27],v13,6144
v_cvt_pk_bf16_f32 v206, v212, v213
M a[180:183],a[80:83],a[52:55],a[180:183]
global_load_lds_dwordx4 v12, s[40:41] offset:3072 sc1
v_cvt_pk_bf16_f32 v238, v244, v245
M a[184:187],a[80:83],a[56:59],a[184:187]
X a[28:31],v13,7168
v_cvt_pk_bf16_f32 v207, v214, v215
M a[188:191],a[80:83],a[60:63],a[188:191]
v_add_u32 v12, 0x4000, v12
v_cvt_pk_bf16_f32 v239, v246, v247
M a[224:227],a[84:87],a[32:35],a[224:227]
T a[80:81],v32,0
global_store_dwordx4 v16, v[204:207], s[58:59] sc1
M a[228:231],a[84:87],a[36:39],a[228:231]
T a[82:83],v32,4096
global_store_dwordx4 v16, v[236:239], s[68:69] sc1
M a[232:235],a[84:87],a[40:43],a[232:235]
s_add_u32 m0, s55, 40960
s_add_u32 s58, s58, 294912
s_addc_u32 s59, s59, 0
M a[236:239],a[84:87],a[44:47],a[236:239]
global_load_lds_dwordx4 v11, s[50:51] offset:0
M v[192:195],v[132:135],v[72:75],v[24:27]
s_add_u32 s68, s68, 294912
s_addc_u32 s69, s69, 0
M a[240:243],a[84:87],a[48:51],a[240:243]
global_load_lds_dwordx4 v11, s[52:53] offset:1024
M v[224:227],v[136:139],v[76:79],v[24:27]
M v[196:199],v[136:139],v[72:75],v[28:31]
M v[228:231],v[140:143],v[76:79],v[28:31]
M a[244:247],a[84:87],a[52:55],a[244:247]
v_add_u32 v11, 229376, v11
v_max3_f32 v200, v192, v193, v194
M a[248:251],a[84:87],a[56:59],a[248:251]
M a[252:255],a[84:87],a[60:63],a[252:255]
s_waitcnt lgkmcnt(2)
v_max3_f32 v232, v224, v225, v226
M a[128:131],a[72:75],a[0:3],a[128:131]
T a[84:85],v32,1024
v_max3_f32 v201, v195, v196, v197
M a[132:135],a[72:75],a[4:7],a[132:135]
T a[86:87],v32,5120
v_max3_f32 v233, v227, v228, v229
M a[136:139],a[72:75],a[8:11],a[136:139]
v_max3_f32 v200, v200, v198, v199
v_max3_f32 v232, v232, v230, v231
M a[140:143],a[72:75],a[12:15],a[140:143]
v_max_f32 v200, v200, v201
v_max_f32 v232, v232, v233
M a[144:147],a[72:75],a[16:19],a[144:147]
v_mov_b32 v201, v200
v_mov_b32 v233, v232
M a[148:151],a[72:75],a[20:23],a[148:151]
v_permlane32_swap_b32 v200, v201
v_permlane32_swap_b32 v232, v233
M a[152:155],a[72:75],a[24:27],a[152:155]
v_max_f32 v200, v200, v201
v_max_f32 v232, v232, v233
M a[156:159],a[72:75],a[28:31],a[156:159]
s_waitcnt vmcnt(26) lgkmcnt(0)
s_barrier
M a[192:195],a[76:79],a[0:3],a[192:195]
T a[72:73],v9,8192
s_add_u32 m0, s54, 131072
M a[196:199],a[76:79],a[4:7],a[196:199]
T a[74:75],v9,12288
global_load_lds_dwordx4 v12, s[40:41] offset:0 sc1
M a[200:203],a[76:79],a[8:11],a[200:203]
X a[32:35],v13,16384
v_mov_b32 v201, v200
M a[204:207],a[76:79],a[12:15],a[204:207]
X a[36:39],v13,17408
v_mov_b32 v233, v232
M a[208:211],a[76:79],a[16:19],a[208:211]
X a[40:43],v13,18432
v_permlane16_swap_b32 v200, v201
M a[212:215],a[76:79],a[20:23],a[212:215]
global_load_lds_dwordx4 v12, s[40:41] offset:1024 sc1
v_permlane16_swap_b32 v232, v233
M a[216:219],a[76:79],a[24:27],a[216:219]
X a[44:47],v13,19456
v_max_f32 v200, v200, v201
M a[220:223],a[76:79],a[28:31],a[220:223]
global_load_lds_dwordx4 v12, s[40:41] offset:2048 sc1
v_max_f32 v232, v232, v233
M a[160:163],a[80:83],a[0:3],a[160:163]
T a[76:77],v9,9216
v_mul_f32 v200, s74, v200
M a[164:167],a[80:83],a[4:7],a[164:167]
T a[78:79],v9,13312
v_mul_f32 v232, s74, v232
M a[168:171],a[80:83],a[8:11],a[168:171]
X a[48:51],v13,20480
v_fma_f32 v192, v192, s74, -v200
M a[172:175],a[80:83],a[12:15],a[172:175]
X a[52:55],v13,21504
v_fma_f32 v224, v224, s74, -v232
M a[176:179],a[80:83],a[16:19],a[176:179]
X a[56:59],v13,22528
v_fma_f32 v193, v193, s74, -v200
M a[180:183],a[80:83],a[20:23],a[180:183]
global_load_lds_dwordx4 v12, s[40:41] offset:3072 sc1
v_fma_f32 v225, v225, s74, -v232
M a[184:187],a[80:83],a[24:27],a[184:187]
X a[60:63],v13,23552
v_fma_f32 v194, v194, s74, -v200
M a[188:191],a[80:83],a[28:31],a[188:191]
v_add_u32 v12, 0x4000, v12
v_fma_f32 v226, v226, s74, -v232
M a[224:227],a[84:87],a[0:3],a[224:227]
T a[80:81],v32,8192
v_fma_f32 v195, v195, s74, -v200
M a[228:231],a[84:87],a[4:7],a[228:231]
T a[82:83],v32,12288
v_fma_f32 v227, v227, s74, -v232
M a[232:235],a[84:87],a[8:11],a[232:235]
s_add_u32 m0, s55, 0
v_fma_f32 v196, v196, s74, -v200
M a[236:239],a[84:87],a[12:15],a[236:239]
global_load_lds_dwordx4 v11, s[50:51] offset:0
v_fma_f32 v228, v228, s74, -v232
M a[240:243],a[84:87],a[16:19],a[240:243]
global_load_lds_dwordx4 v11, s[52:53] offset:1024
v_fma_f32 v197, v197, s74, -v200
M a[244:247],a[84:87],a[20:23],a[244:247]
v_add_u32 v11, 229376, v11
v_fma_f32 v229, v229, s74, -v232
M a[248:251],a[84:87],a[24:27],a[248:251]
v_fma_f32 v198, v198, s74, -v200
v_fma_f32 v230, v230, s74, -v232
M a[252:255],a[84:87],a[28:31],a[252:255]
s_waitcnt lgkmcnt(2)
v_fma_f32 v199, v199, s74, -v200
M a[128:131],a[72:75],a[32:35],a[128:131]
T a[84:85],v32,9216
v_fma_f32 v231, v231, s74, -v232
M a[132:135],a[72:75],a[36:39],a[132:135]
T a[86:87],v32,13312
v_exp_f32 v192, v192
M a[136:139],a[72:75],a[40:43],a[136:139]
v_exp_f32 v224, v224
M a[140:143],a[72:75],a[44:47],a[140:143]
v_exp_f32 v193, v193
M a[144:147],a[72:75],a[48:51],a[144:147]
v_exp_f32 v225, v225
M a[148:151],a[72:75],a[52:55],a[148:151]
v_exp_f32 v194, v194
M a[152:155],a[72:75],a[56:59],a[152:155]
v_exp_f32 v226, v226
M a[156:159],a[72:75],a[60:63],a[156:159]
s_waitcnt vmcnt(26) lgkmcnt(0)
s_barrier
M a[192:195],a[76:79],a[32:35],a[192:195]
T a[72:73],v9,16384
s_add_u32 m0, s54, 147456
M a[196:199],a[76:79],a[36:39],a[196:199]
T a[74:75],v9,20480
global_load_lds_dwordx4 v12, s[40:41] offset:0 sc1
M a[200:203],a[76:79],a[40:43],a[200:203]
X a[0:3],v8,0
v_exp_f32 v195, v195
M a[204:207],a[76:79],a[44:47],a[204:207]
X a[4:7],v8,1024
v_exp_f32 v227, v227
M a[208:211],a[76:79],a[48:51],a[208:211]
X a[8:11],v8,2048
v_exp_f32 v196, v196
M a[212:215],a[76:79],a[52:55],a[212:215]
global_load_lds_dwordx4 v12, s[40:41] offset:1024 sc1
v_exp_f32 v228, v228
M a[216:219],a[76:79],a[56:59],a[216:219]
X a[12:15],v8,3072
v_exp_f32 v197, v197
M a[220:223],a[76:79],a[60:63],a[220:223]
global_load_lds_dwordx4 v12, s[40:41] offset:2048 sc1
v_exp_f32 v229, v229
M a[160:163],a[80:83],a[32:35],a[160:163]
T a[76:77],v9,17408
v_exp_f32 v198, v198
M a[164:167],a[80:83],a[36:39],a[164:167]
T a[78:79],v9,21504
v_exp_f32 v230, v230
M a[168:171],a[80:83],a[40:43],a[168:171]
X a[16:19],v8,4096
v_exp_f32 v199, v199
M a[172:175],a[80:83],a[44:47],a[172:175]
X a[20:23],v8,5120
v_cvt_pk_bf16_f32 v204, v192, v193
M a[176:179],a[80:83],a[48:51],a[176:179]
X a[24:27],v8,6144
v_cvt_pk_bf16_f32 v205, v194, v195
M a[180:183],a[80:83],a[52:55],a[180:183]
global_load_lds_dwordx4 v12, s[40:41] offset:3072 sc1
v_cvt_pk_bf16_f32 v206, v196, v197
M a[184:187],a[80:83],a[56:59],a[184:187]
X a[28:31],v8,7168
v_cvt_pk_bf16_f32 v207, v198, v199
M a[188:191],a[80:83],a[60:63],a[188:191]
v_add_u32 v12, 0x4000, v12
M v[216:219],v[40:43],v[204:207],0
v_exp_f32 v231, v231
M a[224:227],a[84:87],a[32:35],a[224:227]
T a[80:81],v32,16384
v_cvt_pk_bf16_f32 v236, v224, v225
M a[228:231],a[84:87],a[36:39],a[228:231]
T a[82:83],v32,20480
v_cvt_pk_bf16_f32 v237, v226, v227
M a[232:235],a[84:87],a[40:43],a[232:235]
s_add_u32 m0, s55, 8192
v_cvt_pk_bf16_f32 v238, v228, v229
M a[236:239],a[84:87],a[44:47],a[236:239]
global_load_lds_dwordx4 v11, s[50:51] offset:0
v_cvt_pk_bf16_f32 v239, v230, v231
M a[240:243],a[84:87],a[48:51],a[240:243]
global_load_lds_dwordx4 v11, s[52:53] offset:1024
M v[248:251],v[40:43],v[236:239],0
M v[208:211],v[100:103],v[204:207],0
M v[240:243],v[102:105],v[236:239],0
M v[212:215],v[164:167],v[204:207],0
M v[244:247],v[166:169],v[236:239],0
v_rcp_f32 v216, v216
M a[244:247],a[84:87],a[52:55],a[244:247]
v_add_u32 v11, 229376, v11
M a[248:251],a[84:87],a[56:59],a[248:251]
v_rcp_f32 v248, v248
M a[252:255],a[84:87],a[60:63],a[252:255]
s_waitcnt lgkmcnt(2)
v_mul_f32 v208, v208, v216
M a[128:131],a[72:75],a[0:3],a[128:131]
T a[84:85],v32,17408
v_mul_f32 v240, v240, v248
M a[132:135],a[72:75],a[4:7],a[132:135]
T a[86:87],v32,21504
v_mul_f32 v209, v209, v216
M a[136:139],a[72:75],a[8:11],a[136:139]
v_mul_f32 v241, v241, v248
v_mul_f32 v210, v210, v216
M a[140:143],a[72:75],a[12:15],a[140:143]
v_mul_f32 v242, v242, v248
v_mul_f32 v211, v211, v216
M a[144:147],a[72:75],a[16:19],a[144:147]
v_mul_f32 v243, v243, v248
v_mul_f32 v212, v212, v216
M a[148:151],a[72:75],a[20:23],a[148:151]
v_mul_f32 v244, v244, v248
v_mul_f32 v213, v213, v216
M a[152:155],a[72:75],a[24:27],a[152:155]
v_mul_f32 v245, v245, v248
v_mul_f32 v214, v214, v216
M a[156:159],a[72:75],a[28:31],a[156:159]
s_waitcnt vmcnt(26) lgkmcnt(0)
s_barrier
M a[192:195],a[76:79],a[0:3],a[192:195]
T a[72:73],v9,24576
s_add_u32 m0, s54, 0
M a[196:199],a[76:79],a[4:7],a[196:199]
T a[74:75],v9,28672
global_load_lds_dwordx4 v12, s[40:41] offset:0 sc1
M a[200:203],a[76:79],a[8:11],a[200:203]
X a[32:35],v8,16384
v_mul_f32 v246, v246, v248
M a[204:207],a[76:79],a[12:15],a[204:207]
X a[36:39],v8,17408
v_mul_f32 v215, v215, v216
M a[208:211],a[76:79],a[16:19],a[208:211]
X a[40:43],v8,18432
v_mul_f32 v247, v247, v248
M a[212:215],a[76:79],a[20:23],a[212:215]
global_load_lds_dwordx4 v12, s[40:41] offset:1024 sc1
v_cvt_pk_bf16_f32 v204, v208, v209
M a[216:219],a[76:79],a[24:27],a[216:219]
X a[44:47],v8,19456
v_cvt_pk_bf16_f32 v236, v240, v241
M a[220:223],a[76:79],a[28:31],a[220:223]
global_load_lds_dwordx4 v12, s[40:41] offset:2048 sc1
v_cvt_pk_bf16_f32 v205, v210, v211
M a[160:163],a[80:83],a[0:3],a[160:163]
T a[76:77],v9,25600
v_cvt_pk_bf16_f32 v237, v242, v243
M a[164:167],a[80:83],a[4:7],a[164:167]
T a[78:79],v9,29696
v_cvt_pk_bf16_f32 v206, v212, v213
M a[168:171],a[80:83],a[8:11],a[168:171]
X a[48:51],v8,20480
v_cvt_pk_bf16_f32 v238, v244, v245
M a[172:175],a[80:83],a[12:15],a[172:175]
X a[52:55],v8,21504
v_cvt_pk_bf16_f32 v207, v214, v215
M a[176:179],a[80:83],a[16:19],a[176:179]
X a[56:59],v8,22528
v_cvt_pk_bf16_f32 v239, v246, v247
M a[180:183],a[80:83],a[20:23],a[180:183]
global_load_lds_dwordx4 v12, s[40:41] offset:3072 sc1
global_store_dwordx4 v16, v[204:207], s[58:59] sc1
M a[184:187],a[80:83],a[24:27],a[184:187]
X a[60:63],v8,23552
global_store_dwordx4 v16, v[236:239], s[68:69] sc1
M a[188:191],a[80:83],a[28:31],a[188:191]
v_add_u32 v12, 0x4000, v12
s_add_u32 s58, s58, 294912
s_addc_u32 s59, s59, 0
M a[224:227],a[84:87],a[0:3],a[224:227]
T a[80:81],v32,24576
M v[192:195],v[140:143],v[80:83],v[24:27]
s_add_u32 s68, s68, 294912
s_addc_u32 s69, s69, 0
M a[228:231],a[84:87],a[4:7],a[228:231]
T a[82:83],v32,28672
M v[224:227],v[144:147],v[84:87],v[24:27]
M v[196:199],v[144:147],v[80:83],v[28:31]
M v[228:231],v[148:151],v[84:87],v[28:31]
M a[232:235],a[84:87],a[8:11],a[232:235]
s_add_u32 m0, s55, 16384
v_max3_f32 v200, v192, v193, v194
M a[236:239],a[84:87],a[12:15],a[236:239]
global_load_lds_dwordx4 v11, s[50:51] offset:0
M a[240:243],a[84:87],a[16:19],a[240:243]
global_load_lds_dwordx4 v11, s[52:53] offset:1024
v_max3_f32 v232, v224, v225, v226
M a[244:247],a[84:87],a[20:23],a[244:247]
v_add_u32 v11, 229376, v11
v_max3_f32 v201, v195, v196, v197
M a[248:251],a[84:87],a[24:27],a[248:251]
v_max3_f32 v233, v227, v228, v229
v_max3_f32 v200, v200, v198, v199
M a[252:255],a[84:87],a[28:31],a[252:255]
s_waitcnt lgkmcnt(2)
v_max3_f32 v232, v232, v230, v231
M a[128:131],a[72:75],a[32:35],a[128:131]
T a[84:85],v32,25600
v_max_f32 v200, v200, v201
M a[132:135],a[72:75],a[36:39],a[132:135]
T a[86:87],v32,29696
v_max_f32 v232, v232, v233
M a[136:139],a[72:75],a[40:43],a[136:139]
v_mov_b32 v201, v200
v_mov_b32 v233, v232
M a[140:143],a[72:75],a[44:47],a[140:143]
v_permlane32_swap_b32 v200, v201
v_permlane32_swap_b32 v232, v233
M a[144:147],a[72:75],a[48:51],a[144:147]
v_max_f32 v200, v200, v201
v_max_f32 v232, v232, v233
M a[148:151],a[72:75],a[52:55],a[148:151]
v_mov_b32 v201, v200
v_mov_b32 v233, v232
M a[152:155],a[72:75],a[56:59],a[152:155]
v_permlane16_swap_b32 v200, v201
v_permlane16_swap_b32 v232, v233
M a[156:159],a[72:75],a[60:63],a[156:159]
s_waitcnt vmcnt(28) lgkmcnt(0)
s_barrier
M a[192:195],a[76:79],a[32:35],a[192:195]
T a[72:73],v9,32768
s_add_u32 m0, s54, 16384
M a[196:199],a[76:79],a[36:39],a[196:199]
T a[74:75],v9,36864
global_load_lds_dwordx4 v12, s[40:41] offset:0 sc1
M a[200:203],a[76:79],a[40:43],a[200:203]
X a[0:3],v8,32768
v_max_f32 v200, v200, v201
M a[204:207],a[76:79],a[44:47],a[204:207]
X a[4:7],v8,33792
v_max_f32 v232, v232, v233
M a[208:211],a[76:79],a[48:51],a[208:211]
X a[8:11],v8,34816
v_mul_f32 v200, s74, v200
M a[212:215],a[76:79],a[52:55],a[212:215]
global_load_lds_dwordx4 v12, s[40:41] offset:1024 sc1
v_mul_f32 v232, s74, v232
M a[216:219],a[76:79],a[56:59],a[216:219]
X a[12:15],v8,35840
v_fma_f32 v192, v192, s74, -v200
M a[220:223],a[76:79],a[60:63],a[220:223]
global_load_lds_dwordx4 v12, s[40:41] offset:2048 sc1
v_fma_f32 v224, v224, s74, -v232
M a[160:163],a[80:83],a[32:35],a[160:163]
T a[76:77],v9,33792
v_fma_f32 v193, v193, s74, -v200
M a[164:167],a[80:83],a[36:39],a[164:167]
T a[78:79],v9,37888
v_fma_f32 v225, v225, s74, -v232
M a[168:171],a[80:83],a[40:43],a[168:171]
X a[16:19],v8,36864
v_fma_f32 v194, v194, s74, -v200
M a[172:175],a[80:83],a[44:47],a[172:175]
X a[20:23],v8,37888
v_fma_f32 v226, v226, s74, -v232
M a[176:179],a[80:83],a[48:51],a[176:179]
X a[24:27],v8,38912
v_fma_f32 v195, v195, s74, -v200
M a[180:183],a[80:83],a[52:55],a[180:183]
global_load_lds_dwordx4 v12, s[40:41] offset:3072 sc1
v_fma_f32 v227, v227, s74, -v232
M a[184:187],a[80:83],a[56:59],a[184:187]
X a[28:31],v8,39936
v_fma_f32 v196, v196, s74, -v200
M a[188:191],a[80:83],a[60:63],a[188:191]
v_add_u32 v12, 0x4000, v12
v_fma_f32 v228, v228, s74, -v232
M a[224:227],a[84:87],a[32:35],a[224:227]
T a[80:81],v32,32768
v_fma_f32 v197, v197, s74, -v200
M a[228:231],a[84:87],a[36:39],a[228:231]
T a[82:83],v32,36864
v_fma_f32 v229, v229, s74, -v232
M a[232:235],a[84:87],a[40:43],a[232:235]
s_add_u32 m0, s55, 24576
v_fma_f32 v198, v198, s74, -v200
M a[236:239],a[84:87],a[44:47],a[236:239]
global_load_lds_dwordx4 v11, s[50:51] offset:0
v_fma_f32 v230, v230, s74, -v232
M a[240:243],a[84:87],a[48:51],a[240:243]
global_load_lds_dwordx4 v11, s[52:53] offset:1024
v_fma_f32 v199, v199, s74, -v200
M a[244:247],a[84:87],a[52:55],a[244:247]
v_add_u32 v11, 229376, v11
v_fma_f32 v231, v231, s74, -v232
M a[248:251],a[84:87],a[56:59],a[248:251]
v_exp_f32 v192, v192
M a[252:255],a[84:87],a[60:63],a[252:255]
s_waitcnt lgkmcnt(2)
v_exp_f32 v224, v224
M a[128:131],a[72:75],a[0:3],a[128:131]
T a[84:85],v32,33792
v_exp_f32 v193, v193
M a[132:135],a[72:75],a[4:7],a[132:135]
T a[86:87],v32,37888
v_exp_f32 v225, v225
M a[136:139],a[72:75],a[8:11],a[136:139]
v_exp_f32 v194, v194
M a[140:143],a[72:75],a[12:15],a[140:143]
v_exp_f32 v226, v226
M a[144:147],a[72:75],a[16:19],a[144:147]
v_exp_f32 v195, v195
M a[148:151],a[72:75],a[20:23],a[148:151]
v_exp_f32 v227, v227
M a[152:155],a[72:75],a[24:27],a[152:155]
v_exp_f32 v196, v196
M a[156:159],a[72:75],a[28:31],a[156:159]
s_waitcnt vmcnt(26) lgkmcnt(0)
s_barrier
M a[192:195],a[76:79],a[0:3],a[192:195]
T a[72:73],v9,40960
s_add_u32 m0, s54, 32768
M a[196:199],a[76:79],a[4:7],a[196:199]
T a[74:75],v9,45056
global_load_lds_dwordx4 v12, s[40:41] offset:0 sc1
M a[200:203],a[76:79],a[8:11],a[200:203]
X a[32:35],v8,49152
v_exp_f32 v228, v228
M a[204:207],a[76:79],a[12:15],a[204:207]
X a[36:39],v8,50176
v_exp_f32 v197, v197
M a[208:211],a[76:79],a[16:19],a[208:211]
X a[40:43],v8,51200
v_exp_f32 v229, v229
M a[212:215],a[76:79],a[20:23],a[212:215]
global_load_lds_dwordx4 v12, s[40:41] offset:1024 sc1
v_exp_f32 v198, v198
M a[216:219],a[76:79],a[24:27],a[216:219]
X a[44:47],v8,52224
v_exp_f32 v230, v230
M a[220:223],a[76:79],a[28:31],a[220:223]
global_load_lds_dwordx4 v12, s[40:41] offset:2048 sc1
v_exp_f32 v199, v199
M a[160:163],a[80:83],a[0:3],a[160:163]
T a[76:77],v9,41984
v_cvt_pk_bf16_f32 v204, v192, v193
M a[164:167],a[80:83],a[4:7],a[164:167]
T a[78:79],v9,46080
v_cvt_pk_bf16_f32 v205, v194, v195
M a[168:171],a[80:83],a[8:11],a[168:171]
X a[48:51],v8,53248
v_cvt_pk_bf16_f32 v206, v196, v197
M a[172:175],a[80:83],a[12:15],a[172:175]
X a[52:55],v8,54272
v_cvt_pk_bf16_f32 v207, v198, v199
M a[176:179],a[80:83],a[16:19],a[176:179]
X a[56:59],v8,55296
M v[216:219],v[40:43],v[204:207],0
v_exp_f32 v231, v231
M a[180:183],a[80:83],a[20:23],a[180:183]
global_load_lds_dwordx4 v12, s[40:41] offset:3072 sc1
v_cvt_pk_bf16_f32 v236, v224, v225
M a[184:187],a[80:83],a[24:27],a[184:187]
X a[60:63],v8,56320
v_cvt_pk_bf16_f32 v237, v226, v227
M a[188:191],a[80:83],a[28:31],a[188:191]
v_add_u32 v12, 0x4000, v12
v_cvt_pk_bf16_f32 v238, v228, v229
M a[224:227],a[84:87],a[0:3],a[224:227]
T a[80:81],v32,40960
v_cvt_pk_bf16_f32 v239, v230, v231
M a[228:231],a[84:87],a[4:7],a[228:231]
T a[82:83],v32,45056
M v[248:251],v[40:43],v[236:239],0
M v[208:211],v[104:107],v[204:207],0
M v[240:243],v[106:109],v[236:239],0
M v[212:215],v[168:171],v[204:207],0
M v[244:247],v[170:173],v[236:239],0
v_rcp_f32 v216, v216
M a[232:235],a[84:87],a[8:11],a[232:235]
s_add_u32 m0, s55, 32768
M a[236:239],a[84:87],a[12:15],a[236:239]
global_load_lds_dwordx4 v11, s[50:51] offset:0
v_mul_f32 v208, v208, v216
M a[240:243],a[84:87],a[16:19],a[240:243]
global_load_lds_dwordx4 v11, s[52:53] offset:1024
v_mul_f32 v209, v209, v216
M a[244:247],a[84:87],a[20:23],a[244:247]
v_add_u32 v11, 229376, v11
v_mul_f32 v210, v210, v216
M a[248:251],a[84:87],a[24:27],a[248:251]
v_rcp_f32 v248, v248
M a[252:255],a[84:87],a[28:31],a[252:255]
s_waitcnt lgkmcnt(2)
v_mul_f32 v240, v240, v248
M a[128:131],a[72:75],a[32:35],a[128:131]
T a[84:85],v32,41984
v_mul_f32 v241, v241, v248
M a[132:135],a[72:75],a[36:39],a[132:135]
T a[86:87],v32,46080
v_mul_f32 v242, v242, v248
M a[136:139],a[72:75],a[40:43],a[136:139]
v_mul_f32 v211, v211, v216
v_mul_f32 v243, v243, v248
M a[140:143],a[72:75],a[44:47],a[140:143]
v_mul_f32 v212, v212, v216
v_mul_f32 v244, v244, v248
M a[144:147],a[72:75],a[48:51],a[144:147]
v_mul_f32 v213, v213, v216
v_mul_f32 v245, v245, v248
M a[148:151],a[72:75],a[52:55],a[148:151]
v_mul_f32 v214, v214, v216
v_mul_f32 v246, v246, v248
M a[152:155],a[72:75],a[56:59],a[152:155]
v_mul_f32 v215, v215, v216
v_mul_f32 v247, v247, v248
M a[156:159],a[72:75],a[60:63],a[156:159]
s_waitcnt vmcnt(26) lgkmcnt(0)
s_barrier
M a[192:195],a[76:79],a[32:35],a[192:195]
T a[72:73],v9,0
s_add_u32 m0, s54, 49152
M a[196:199],a[76:79],a[36:39],a[196:199]
T a[74:75],v9,4096
global_load_lds_dwordx4 v12, s[40:41] offset:0 sc1
M a[200:203],a[76:79],a[40:43],a[200:203]
X a[0:3],v13,0
v_cvt_pk_bf16_f32 v204, v208, v209
M a[204:207],a[76:79],a[44:47],a[204:207]
X a[4:7],v13,1024
v_cvt_pk_bf16_f32 v236, v240, v241
M a[208:211],a[76:79],a[48:51],a[208:211]
X a[8:11],v13,2048
v_cvt_pk_bf16_f32 v205, v210, v211
M a[212:215],a[76:79],a[52:55],a[212:215]
global_load_lds_dwordx4 v12, s[40:41] offset:1024 sc1
v_cvt_pk_bf16_f32 v237, v242, v243
M a[216:219],a[76:79],a[56:59],a[216:219]
X a[12:15],v13,3072
v_cvt_pk_bf16_f32 v206, v212, v213
M a[220:223],a[76:79],a[60:63],a[220:223]
global_load_lds_dwordx4 v12, s[40:41] offset:2048 sc1
v_cvt_pk_bf16_f32 v238, v244, v245
M a[160:163],a[80:83],a[32:35],a[160:163]
T a[76:77],v9,1024
v_cvt_pk_bf16_f32 v207, v214, v215
M a[164:167],a[80:83],a[36:39],a[164:167]
T a[78:79],v9,5120
v_cvt_pk_bf16_f32 v239, v246, v247
M a[168:171],a[80:83],a[40:43],a[168:171]
X a[16:19],v13,4096
global_store_dwordx4 v16, v[204:207], s[58:59] sc1
M a[172:175],a[80:83],a[44:47],a[172:175]
X a[20:23],v13,5120
global_store_dwordx4 v16, v[236:239], s[68:69] sc1
M a[176:179],a[80:83],a[48:51],a[176:179]
X a[24:27],v13,6144
s_add_u32 s58, s58, 294912
s_addc_u32 s59, s59, 0
M a[180:183],a[80:83],a[52:55],a[180:183]
global_load_lds_dwordx4 v12, s[40:41] offset:3072 sc1
M v[192:195],v[148:151],v[88:91],v[24:27]
s_add_u32 s68, s68, 294912
s_addc_u32 s69, s69, 0
M a[184:187],a[80:83],a[56:59],a[184:187]
X a[28:31],v13,7168
M v[224:227],v[152:155],v[92:95],v[24:27]
M v[196:199],v[152:155],v[88:91],v[28:31]
M v[228:231],v[156:159],v[92:95],v[28:31]
M a[188:191],a[80:83],a[60:63],a[188:191]
v_add_u32 v12, 0x4000, v12
v_max3_f32 v200, v192, v193, v194
M a[224:227],a[84:87],a[32:35],a[224:227]
T a[80:81],v32,0
M a[228:231],a[84:87],a[36:39],a[228:231]
T a[82:83],v32,4096
v_max3_f32 v232, v224, v225, v226
M a[232:235],a[84:87],a[40:43],a[232:235]
s_add_u32 m0, s55, 40960
v_max3_f32 v201, v195, v196, v197
M a[236:239],a[84:87],a[44:47],a[236:239]
global_load_lds_dwordx4 v11, s[50:51] offset:0
v_max3_f32 v233, v227, v228, v229
M a[240:243],a[84:87],a[48:51],a[240:243]
global_load_lds_dwordx4 v11, s[52:53] offset:1024
v_max3_f32 v200, v200, v198, v199
M a[244:247],a[84:87],a[52:55],a[244:247]
v_add_u32 v11, 229376, v11
v_max3_f32 v232, v232, v230, v231
M a[248:251],a[84:87],a[56:59],a[248:251]
v_max_f32 v200, v200, v201
v_max_f32 v232, v232, v233
M a[252:255],a[84:87],a[60:63],a[252:255]
s_waitcnt lgkmcnt(2)
v_mov_b32 v201, v200
M a[128:131],a[72:75],a[0:3],a[128:131]
T a[84:85],v32,1024
v_mov_b32 v233, v232
M a[132:135],a[72:75],a[4:7],a[132:135]
T a[86:87],v32,5120
v_permlane32_swap_b32 v200, v201
M a[136:139],a[72:75],a[8:11],a[136:139]
v_permlane32_swap_b32 v232, v233
v_max_f32 v200, v200, v201
M a[140:143],a[72:75],a[12:15],a[140:143]
v_max_f32 v232, v232, v233
v_mov_b32 v201, v200
M a[144:147],a[72:75],a[16:19],a[144:147]
v_mov_b32 v233, v232
v_permlane16_swap_b32 v200, v201
M a[148:151],a[72:75],a[20:23],a[148:151]
v_permlane16_swap_b32 v232, v233
v_max_f32 v200, v200, v201
M a[152:155],a[72:75],a[24:27],a[152:155]
v_max_f32 v232, v232, v233
v_mul_f32 v200, s74, v200
M a[156:159],a[72:75],a[28:31],a[156:159]
s_waitcnt vmcnt(28) lgkmcnt(0)
s_barrier
M a[192:195],a[76:79],a[0:3],a[192:195]
T a[72:73],v9,8192
s_add_u32 m0, s54, 131072
M a[196:199],a[76:79],a[4:7],a[196:199]
T a[74:75],v9,12288
global_load_lds_dwordx4 v12, s[40:41] offset:0 sc1
M a[200:203],a[76:79],a[8:11],a[200:203]
X a[32:35],v13,16384
v_mul_f32 v232, s74, v232
M a[204:207],a[76:79],a[12:15],a[204:207]
X a[36:39],v13,17408
v_fma_f32 v192, v192, s74, -v200
M a[208:211],a[76:79],a[16:19],a[208:211]
X a[40:43],v13,18432
v_fma_f32 v224, v224, s74, -v232
M a[212:215],a[76:79],a[20:23],a[212:215]
global_load_lds_dwordx4 v12, s[40:41] offset:1024 sc1
v_fma_f32 v193, v193, s74, -v200
M a[216:219],a[76:79],a[24:27],a[216:219]
X a[44:47],v13,19456
v_fma_f32 v225, v225, s74, -v232
M a[220:223],a[76:79],a[28:31],a[220:223]
global_load_lds_dwordx4 v12, s[40:41] offset:2048 sc1
v_fma_f32 v194, v194, s74, -v200
M a[160:163],a[80:83],a[0:3],a[160:163]
T a[76:77],v9,9216
v_fma_f32 v226, v226, s74, -v232
M a[164:167],a[80:83],a[4:7],a[164:167]
T a[78:79],v9,13312
v_fma_f32 v195, v195, s74, -v200
M a[168:171],a[80:83],a[8:11],a[168:171]
X a[48:51],v13,20480
v_fma_f32 v227, v227, s74, -v232
M a[172:175],a[80:83],a[12:15],a[172:175]
X a[52:55],v13,21504
v_fma_f32 v196, v196, s74, -v200
M a[176:179],a[80:83],a[16:19],a[176:179]
X a[56:59],v13,22528
v_fma_f32 v228, v228, s74, -v232
M a[180:183],a[80:83],a[20:23],a[180:183]
global_load_lds_dwordx4 v12, s[40:41] offset:3072 sc1
v_fma_f32 v197, v197, s74, -v200
M a[184:187],a[80:83],a[24:27],a[184:187]
X a[60:63],v13,23552
v_fma_f32 v229, v229, s74, -v232
M a[188:191],a[80:83],a[28:31],a[188:191]
v_add_u32 v12, 0x4000, v12
v_fma_f32 v198, v198, s74, -v200
M a[224:227],a[84:87],a[0:3],a[224:227]
T a[80:81],v32,8192
v_fma_f32 v230, v230, s74, -v232
M a[228:231],a[84:87],a[4:7],a[228:231]
T a[82:83],v32,12288
v_fma_f32 v199, v199, s74, -v200
M a[232:235],a[84:87],a[8:11],a[232:235]
s_add_u32 m0, s55, 0
v_fma_f32 v231, v231, s74, -v232
M a[236:239],a[84:87],a[12:15],a[236:239]
global_load_lds_dwordx4 v11, s[50:51] offset:0
v_exp_f32 v192, v192
M a[240:243],a[84:87],a[16:19],a[240:243]
global_load_lds_dwordx4 v11, s[52:53] offset:1024
v_exp_f32 v224, v224
M a[244:247],a[84:87],a[20:23],a[244:247]
v_add_u32 v11, 229376, v11
v_exp_f32 v193, v193
M a[248:251],a[84:87],a[24:27],a[248:251]
v_exp_f32 v225, v225
M a[252:255],a[84:87],a[28:31],a[252:255]
s_waitcnt lgkmcnt(2)
v_exp_f32 v194, v194
M a[128:131],a[72:75],a[32:35],a[128:131]
T a[84:85],v32,9216
v_exp_f32 v226, v226
M a[132:135],a[72:75],a[36:39],a[132:135]
T a[86:87],v32,13312
v_exp_f32 v195, v195
M a[136:139],a[72:75],a[40:43],a[136:139]
v_exp_f32 v227, v227
M a[140:143],a[72:75],a[44:47],a[140:143]
v_exp_f32 v196, v196
M a[144:147],a[72:75],a[48:51],a[144:147]
v_exp_f32 v228, v228
M a[148:151],a[72:75],a[52:55],a[148:151]
v_exp_f32 v197, v197
M a[152:155],a[72:75],a[56:59],a[152:155]
v_exp_f32 v229, v229
M a[156:159],a[72:75],a[60:63],a[156:159]
s_waitcnt vmcnt(26) lgkmcnt(0)
s_barrier
M a[192:195],a[76:79],a[32:35],a[192:195]
T a[72:73],v9,16384
s_add_u32 m0, s54, 147456
M a[196:199],a[76:79],a[36:39],a[196:199]
T a[74:75],v9,20480
global_load_lds_dwordx4 v12, s[40:41] offset:0 sc1
M a[200:203],a[76:79],a[40:43],a[200:203]
X a[0:3],v8,0
v_exp_f32 v198, v198
M a[204:207],a[76:79],a[44:47],a[204:207]
X a[4:7],v8,1024
v_exp_f32 v230, v230
M a[208:211],a[76:79],a[48:51],a[208:211]
X a[8:11],v8,2048
v_exp_f32 v199, v199
M a[212:215],a[76:79],a[52:55],a[212:215]
global_load_lds_dwordx4 v12, s[40:41] offset:1024 sc1
v_cvt_pk_bf16_f32 v204, v192, v193
M a[216:219],a[76:79],a[56:59],a[216:219]
X a[12:15],v8,3072
v_cvt_pk_bf16_f32 v205, v194, v195
M a[220:223],a[76:79],a[60:63],a[220:223]
global_load_lds_dwordx4 v12, s[40:41] offset:2048 sc1
v_cvt_pk_bf16_f32 v206, v196, v197
M a[160:163],a[80:83],a[32:35],a[160:163]
T a[76:77],v9,17408
v_cvt_pk_bf16_f32 v207, v198, v199
M a[164:167],a[80:83],a[36:39],a[164:167]
T a[78:79],v9,21504
M v[216:219],v[40:43],v[204:207],0
v_exp_f32 v231, v231
M a[168:171],a[80:83],a[40:43],a[168:171]
X a[16:19],v8,4096
v_cvt_pk_bf16_f32 v236, v224, v225
M a[172:175],a[80:83],a[44:47],a[172:175]
X a[20:23],v8,5120
v_cvt_pk_bf16_f32 v237, v226, v227
M a[176:179],a[80:83],a[48:51],a[176:179]
X a[24:27],v8,6144
v_cvt_pk_bf16_f32 v238, v228, v229
M a[180:183],a[80:83],a[52:55],a[180:183]
global_load_lds_dwordx4 v12, s[40:41] offset:3072 sc1
v_cvt_pk_bf16_f32 v239, v230, v231
M a[184:187],a[80:83],a[56:59],a[184:187]
X a[28:31],v8,7168
M v[248:251],v[40:43],v[236:239],0
M v[208:211],v[108:111],v[204:207],0
M v[240:243],v[110:113],v[236:239],0
M v[212:215],v[172:175],v[204:207],0
M v[244:247],v[174:177],v[236:239],0
v_rcp_f32 v216, v216
M a[188:191],a[80:83],a[60:63],a[188:191]
v_add_u32 v12, 0x4000, v12
M a[224:227],a[84:87],a[32:35],a[224:227]
T a[80:81],v32,16384
v_mul_f32 v208, v208, v216
M a[228:231],a[84:87],a[36:39],a[228:231]
T a[82:83],v32,20480
v_mul_f32 v209, v209, v216
M a[232:235],a[84:87],a[40:43],a[232:235]
s_add_u32 m0, s55, 8192
v_mul_f32 v210, v210, v216
M a[236:239],a[84:87],a[44:47],a[236:239]
global_load_lds_dwordx4 v11, s[50:51] offset:0
v_mul_f32 v211, v211, v216
M a[240:243],a[84:87],a[48:51],a[240:243]
global_load_lds_dwordx4 v11, s[52:53] offset:1024
v_mul_f32 v212, v212, v216
M a[244:247],a[84:87],a[52:55],a[244:247]
v_add_u32 v11, 229376, v11
v_mul_f32 v213, v213, v216
M a[248:251],a[84:87],a[56:59],a[248:251]
v_rcp_f32 v248, v248
M a[252:255],a[84:87],a[60:63],a[252:255]
s_waitcnt lgkmcnt(2)
v_mul_f32 v240, v240, v248
M a[128:131],a[72:75],a[0:3],a[128:131]
T a[84:85],v32,17408
v_mul_f32 v241, v241, v248
M a[132:135],a[72:75],a[4:7],a[132:135]
T a[86:87],v32,21504
v_mul_f32 v242, v242, v248
M a[136:139],a[72:75],a[8:11],a[136:139]
v_mul_f32 v243, v243, v248
v_mul_f32 v244, v244, v248
M a[140:143],a[72:75],a[12:15],a[140:143]
v_mul_f32 v245, v245, v248
v_mul_f32 v214, v214, v216
M a[144:147],a[72:75],a[16:19],a[144:147]
v_mul_f32 v246, v246, v248
v_mul_f32 v215, v215, v216
M a[148:151],a[72:75],a[20:23],a[148:151]
v_mul_f32 v247, v247, v248
v_cvt_pk_bf16_f32 v204, v208, v209
M a[152:155],a[72:75],a[24:27],a[152:155]
v_cvt_pk_bf16_f32 v236, v240, v241
v_cvt_pk_bf16_f32 v205, v210, v211
M a[156:159],a[72:75],a[28:31],a[156:159]
s_waitcnt vmcnt(26) lgkmcnt(0)
s_barrier
M a[192:195],a[76:79],a[0:3],a[192:195]
T a[72:73],v9,24576
s_add_u32 m0, s54, 0
M a[196:199],a[76:79],a[4:7],a[196:199]
T a[74:75],v9,28672
global_load_lds_dwordx4 v12, s[40:41] offset:0 sc1
M a[200:203],a[76:79],a[8:11],a[200:203]
X a[32:35],v8,16384
v_cvt_pk_bf16_f32 v237, v242, v243
M a[204:207],a[76:79],a[12:15],a[204:207]
X a[36:39],v8,17408
v_cvt_pk_bf16_f32 v206, v212, v213
M a[208:211],a[76:79],a[16:19],a[208:211]
X a[40:43],v8,18432
v_cvt_pk_bf16_f32 v238, v244, v245
M a[212:215],a[76:79],a[20:23],a[212:215]
global_load_lds_dwordx4 v12, s[40:41] offset:1024 sc1
v_cvt_pk_bf16_f32 v207, v214, v215
M a[216:219],a[76:79],a[24:27],a[216:219]
X a[44:47],v8,19456
v_cvt_pk_bf16_f32 v239, v246, v247
M a[220:223],a[76:79],a[28:31],a[220:223]
global_load_lds_dwordx4 v12, s[40:41] offset:2048 sc1
global_store_dwordx4 v16, v[204:207], s[58:59] sc1
M a[160:163],a[80:83],a[0:3],a[160:163]
T a[76:77],v9,25600
global_store_dwordx4 v16, v[236:239], s[68:69] sc1
M a[164:167],a[80:83],a[4:7],a[164:167]
T a[78:79],v9,29696
M a[168:171],a[80:83],a[8:11],a[168:171]
X a[48:51],v8,20480
M a[172:175],a[80:83],a[12:15],a[172:175]
X a[52:55],v8,21504
M a[176:179],a[80:83],a[16:19],a[176:179]
X a[56:59],v8,22528
M a[180:183],a[80:83],a[20:23],a[180:183]
global_load_lds_dwordx4 v12, s[40:41] offset:3072 sc1
M a[184:187],a[80:83],a[24:27],a[184:187]
X a[60:63],v8,23552
M a[188:191],a[80:83],a[28:31],a[188:191]
v_add_u32 v12, 0x4000, v12
M a[224:227],a[84:87],a[0:3],a[224:227]
T a[80:81],v32,24576
M a[228:231],a[84:87],a[4:7],a[228:231]
T a[82:83],v32,28672
M a[232:235],a[84:87],a[8:11],a[232:235]
s_add_u32 m0, s55, 16384
M a[236:239],a[84:87],a[12:15],a[236:239]
global_load_lds_dwordx4 v11, s[50:51] offset:0
M a[240:243],a[84:87],a[16:19],a[240:243]
global_load_lds_dwordx4 v11, s[52:53] offset:1024
M a[244:247],a[84:87],a[20:23],a[244:247]
v_add_u32 v11, 229376, v11
M a[248:251],a[84:87],a[24:27],a[248:251]
M a[252:255],a[84:87],a[28:31],a[252:255]
s_waitcnt lgkmcnt(2)
M a[128:131],a[72:75],a[32:35],a[128:131]
T a[84:85],v32,25600
M a[132:135],a[72:75],a[36:39],a[132:135]
T a[86:87],v32,29696
M a[136:139],a[72:75],a[40:43],a[136:139]
M a[140:143],a[72:75],a[44:47],a[140:143]
M a[144:147],a[72:75],a[48:51],a[144:147]
M a[148:151],a[72:75],a[52:55],a[148:151]
M a[152:155],a[72:75],a[56:59],a[152:155]
M a[156:159],a[72:75],a[60:63],a[156:159]
s_waitcnt vmcnt(28) lgkmcnt(0)
s_barrier
M a[192:195],a[76:79],a[32:35],a[192:195]
T a[72:73],v9,32768
s_add_u32 m0, s54, 16384
M a[196:199],a[76:79],a[36:39],a[196:199]
T a[74:75],v9,36864
global_load_lds_dwordx4 v12, s[40:41] offset:0 sc1
M a[200:203],a[76:79],a[40:43],a[200:203]
X a[0:3],v8,32768
M a[204:207],a[76:79],a[44:47],a[204:207]
X a[4:7],v8,33792
M a[208:211],a[76:79],a[48:51],a[208:211]
X a[8:11],v8,34816
M a[212:215],a[76:79],a[52:55],a[212:215]
global_load_lds_dwordx4 v12, s[40:41] offset:1024 sc1
M a[216:219],a[76:79],a[56:59],a[216:219]
X a[12:15],v8,35840
M a[220:223],a[76:79],a[60:63],a[220:223]
global_load_lds_dwordx4 v12, s[40:41] offset:2048 sc1
M a[160:163],a[80:83],a[32:35],a[160:163]
T a[76:77],v9,33792
M a[164:167],a[80:83],a[36:39],a[164:167]
T a[78:79],v9,37888
M a[168:171],a[80:83],a[40:43],a[168:171]
X a[16:19],v8,36864
M a[172:175],a[80:83],a[44:47],a[172:175]
X a[20:23],v8,37888
M a[176:179],a[80:83],a[48:51],a[176:179]
X a[24:27],v8,38912
M a[180:183],a[80:83],a[52:55],a[180:183]
global_load_lds_dwordx4 v12, s[40:41] offset:3072 sc1
M a[184:187],a[80:83],a[56:59],a[184:187]
X a[28:31],v8,39936
M a[188:191],a[80:83],a[60:63],a[188:191]
v_add_u32 v12, 0x4000, v12
M a[224:227],a[84:87],a[32:35],a[224:227]
T a[80:81],v32,32768
M a[228:231],a[84:87],a[36:39],a[228:231]
T a[82:83],v32,36864
M a[232:235],a[84:87],a[40:43],a[232:235]
s_add_u32 m0, s55, 24576
M a[236:239],a[84:87],a[44:47],a[236:239]
global_load_lds_dwordx4 v11, s[50:51] offset:0
M a[240:243],a[84:87],a[48:51],a[240:243]
global_load_lds_dwordx4 v11, s[52:53] offset:1024
M a[244:247],a[84:87],a[52:55],a[244:247]
v_add_u32 v11, 229376, v11
M a[248:251],a[84:87],a[56:59],a[248:251]
M a[252:255],a[84:87],a[60:63],a[252:255]
s_waitcnt lgkmcnt(2)
M a[128:131],a[72:75],a[0:3],a[128:131]
T a[84:85],v32,33792
M a[132:135],a[72:75],a[4:7],a[132:135]
T a[86:87],v32,37888
M a[136:139],a[72:75],a[8:11],a[136:139]
M a[140:143],a[72:75],a[12:15],a[140:143]
M a[144:147],a[72:75],a[16:19],a[144:147]
M a[148:151],a[72:75],a[20:23],a[148:151]
M a[152:155],a[72:75],a[24:27],a[152:155]
M a[156:159],a[72:75],a[28:31],a[156:159]
s_waitcnt vmcnt(26) lgkmcnt(0)
s_barrier
M a[192:195],a[76:79],a[0:3],a[192:195]
T a[72:73],v9,40960
s_add_u32 m0, s54, 32768
M a[196:199],a[76:79],a[4:7],a[196:199]
T a[74:75],v9,45056
global_load_lds_dwordx4 v12, s[40:41] offset:0 sc1
M a[200:203],a[76:79],a[8:11],a[200:203]
X a[32:35],v8,49152
M a[204:207],a[76:79],a[12:15],a[204:207]
X a[36:39],v8,50176
M a[208:211],a[76:79],a[16:19],a[208:211]
X a[40:43],v8,51200
M a[212:215],a[76:79],a[20:23],a[212:215]
global_load_lds_dwordx4 v12, s[40:41] offset:1024 sc1
M a[216:219],a[76:79],a[24:27],a[216:219]
X a[44:47],v8,52224
M a[220:223],a[76:79],a[28:31],a[220:223]
global_load_lds_dwordx4 v12, s[40:41] offset:2048 sc1
M a[160:163],a[80:83],a[0:3],a[160:163]
T a[76:77],v9,41984
M a[164:167],a[80:83],a[4:7],a[164:167]
T a[78:79],v9,46080
M a[168:171],a[80:83],a[8:11],a[168:171]
X a[48:51],v8,53248
M a[172:175],a[80:83],a[12:15],a[172:175]
X a[52:55],v8,54272
M a[176:179],a[80:83],a[16:19],a[176:179]
X a[56:59],v8,55296
M a[180:183],a[80:83],a[20:23],a[180:183]
global_load_lds_dwordx4 v12, s[40:41] offset:3072 sc1
M a[184:187],a[80:83],a[24:27],a[184:187]
X a[60:63],v8,56320
M a[188:191],a[80:83],a[28:31],a[188:191]
v_add_u32 v12, 0x4000, v12
M a[224:227],a[84:87],a[0:3],a[224:227]
T a[80:81],v32,40960
M a[228:231],a[84:87],a[4:7],a[228:231]
T a[82:83],v32,45056
M a[232:235],a[84:87],a[8:11],a[232:235]
s_add_u32 m0, s55, 32768
M a[236:239],a[84:87],a[12:15],a[236:239]
global_load_lds_dwordx4 v11, s[50:51] offset:0
M a[240:243],a[84:87],a[16:19],a[240:243]
global_load_lds_dwordx4 v11, s[52:53] offset:1024
M a[244:247],a[84:87],a[20:23],a[244:247]
v_add_u32 v11, 229376, v11
M a[248:251],a[84:87],a[24:27],a[248:251]
M a[252:255],a[84:87],a[28:31],a[252:255]
s_waitcnt lgkmcnt(2)
M a[128:131],a[72:75],a[32:35],a[128:131]
T a[84:85],v32,41984
M a[132:135],a[72:75],a[36:39],a[132:135]
T a[86:87],v32,46080
M a[136:139],a[72:75],a[40:43],a[136:139]
M a[140:143],a[72:75],a[44:47],a[140:143]
M a[144:147],a[72:75],a[48:51],a[144:147]
M a[148:151],a[72:75],a[52:55],a[148:151]
M a[152:155],a[72:75],a[56:59],a[152:155]
M a[156:159],a[72:75],a[60:63],a[156:159]
s_waitcnt vmcnt(26) lgkmcnt(0)
s_barrier
M a[192:195],a[76:79],a[32:35],a[192:195]
T a[72:73],v9,0
s_add_u32 m0, s54, 49152
M a[196:199],a[76:79],a[36:39],a[196:199]
T a[74:75],v9,4096
global_load_lds_dwordx4 v12, s[40:41] offset:0 sc1
M a[200:203],a[76:79],a[40:43],a[200:203]
X a[0:3],v13,0
M a[204:207],a[76:79],a[44:47],a[204:207]
X a[4:7],v13,1024
M a[208:211],a[76:79],a[48:51],a[208:211]
X a[8:11],v13,2048
M a[212:215],a[76:79],a[52:55],a[212:215]
global_load_lds_dwordx4 v12, s[40:41] offset:1024 sc1
M a[216:219],a[76:79],a[56:59],a[216:219]
X a[12:15],v13,3072
M a[220:223],a[76:79],a[60:63],a[220:223]
global_load_lds_dwordx4 v12, s[40:41] offset:2048 sc1
M a[160:163],a[80:83],a[32:35],a[160:163]
T a[76:77],v9,1024
M a[164:167],a[80:83],a[36:39],a[164:167]
T a[78:79],v9,5120
M a[168:171],a[80:83],a[40:43],a[168:171]
X a[16:19],v13,4096
M a[172:175],a[80:83],a[44:47],a[172:175]
X a[20:23],v13,5120
M a[176:179],a[80:83],a[48:51],a[176:179]
X a[24:27],v13,6144
M a[180:183],a[80:83],a[52:55],a[180:183]
global_load_lds_dwordx4 v12, s[40:41] offset:3072 sc1
M a[184:187],a[80:83],a[56:59],a[184:187]
X a[28:31],v13,7168
M a[188:191],a[80:83],a[60:63],a[188:191]
v_add_u32 v12, 0x4000, v12
M a[224:227],a[84:87],a[32:35],a[224:227]
T a[80:81],v32,0
M a[228:231],a[84:87],a[36:39],a[228:231]
T a[82:83],v32,4096
M a[232:235],a[84:87],a[40:43],a[232:235]
s_add_u32 m0, s55, 40960
M a[236:239],a[84:87],a[44:47],a[236:239]
global_load_lds_dwordx4 v11, s[50:51] offset:0
M a[240:243],a[84:87],a[48:51],a[240:243]
global_load_lds_dwordx4 v11, s[52:53] offset:1024
M a[244:247],a[84:87],a[52:55],a[244:247]
v_add_u32 v11, 229376, v11
M a[248:251],a[84:87],a[56:59],a[248:251]
M a[252:255],a[84:87],a[60:63],a[252:255]
s_waitcnt lgkmcnt(2)
M a[128:131],a[72:75],a[0:3],a[128:131]
T a[84:85],v32,1024
M a[132:135],a[72:75],a[4:7],a[132:135]
T a[86:87],v32,5120
M a[136:139],a[72:75],a[8:11],a[136:139]
M a[140:143],a[72:75],a[12:15],a[140:143]
M a[144:147],a[72:75],a[16:19],a[144:147]
M a[148:151],a[72:75],a[20:23],a[148:151]
M a[152:155],a[72:75],a[24:27],a[152:155]
M a[156:159],a[72:75],a[28:31],a[156:159]
s_waitcnt vmcnt(26) lgkmcnt(0)
s_barrier
M a[192:195],a[76:79],a[0:3],a[192:195]
T a[72:73],v9,8192
s_add_u32 m0, s54, 131072
M a[196:199],a[76:79],a[4:7],a[196:199]
T a[74:75],v9,12288
global_load_lds_dwordx4 v12, s[40:41] offset:0 sc1
M a[200:203],a[76:79],a[8:11],a[200:203]
X a[32:35],v13,16384
M a[204:207],a[76:79],a[12:15],a[204:207]
X a[36:39],v13,17408
M a[208:211],a[76:79],a[16:19],a[208:211]
X a[40:43],v13,18432
M a[212:215],a[76:79],a[20:23],a[212:215]
global_load_lds_dwordx4 v12, s[40:41] offset:1024 sc1
M a[216:219],a[76:79],a[24:27],a[216:219]
X a[44:47],v13,19456
M a[220:223],a[76:79],a[28:31],a[220:223]
global_load_lds_dwordx4 v12, s[40:41] offset:2048 sc1
M a[160:163],a[80:83],a[0:3],a[160:163]
T a[76:77],v9,9216
M a[164:167],a[80:83],a[4:7],a[164:167]
T a[78:79],v9,13312
M a[168:171],a[80:83],a[8:11],a[168:171]
X a[48:51],v13,20480
M a[172:175],a[80:83],a[12:15],a[172:175]
X a[52:55],v13,21504
M a[176:179],a[80:83],a[16:19],a[176:179]
X a[56:59],v13,22528
M a[180:183],a[80:83],a[20:23],a[180:183]
global_load_lds_dwordx4 v12, s[40:41] offset:3072 sc1
M a[184:187],a[80:83],a[24:27],a[184:187]
X a[60:63],v13,23552
M a[188:191],a[80:83],a[28:31],a[188:191]
v_add_u32 v12, 0x4000, v12
M a[224:227],a[84:87],a[0:3],a[224:227]
T a[80:81],v32,8192
M a[228:231],a[84:87],a[4:7],a[228:231]
T a[82:83],v32,12288
M a[232:235],a[84:87],a[8:11],a[232:235]
s_add_u32 m0, s55, 0
M a[236:239],a[84:87],a[12:15],a[236:239]
global_load_lds_dwordx4 v11, s[50:51] offset:0
M a[240:243],a[84:87],a[16:19],a[240:243]
global_load_lds_dwordx4 v11, s[52:53] offset:1024
M a[244:247],a[84:87],a[20:23],a[244:247]
v_add_u32 v11, 229376, v11
M a[248:251],a[84:87],a[24:27],a[248:251]
M a[252:255],a[84:87],a[28:31],a[252:255]
s_waitcnt lgkmcnt(2)
M a[128:131],a[72:75],a[32:35],a[128:131]
T a[84:85],v32,9216
M a[132:135],a[72:75],a[36:39],a[132:135]
T a[86:87],v32,13312
M a[136:139],a[72:75],a[40:43],a[136:139]
M a[140:143],a[72:75],a[44:47],a[140:143]
M a[144:147],a[72:75],a[48:51],a[144:147]
M a[148:151],a[72:75],a[52:55],a[148:151]
M a[152:155],a[72:75],a[56:59],a[152:155]
M a[156:159],a[72:75],a[60:63],a[156:159]
s_waitcnt vmcnt(24) lgkmcnt(0)
s_barrier
M a[192:195],a[76:79],a[32:35],a[192:195]
T a[72:73],v9,16384
s_add_u32 m0, s54, 147456
M a[196:199],a[76:79],a[36:39],a[196:199]
T a[74:75],v9,20480
global_load_lds_dwordx4 v12, s[40:41] offset:0 sc1
M a[200:203],a[76:79],a[40:43],a[200:203]
X a[0:3],v8,0
M a[204:207],a[76:79],a[44:47],a[204:207]
X a[4:7],v8,1024
M a[208:211],a[76:79],a[48:51],a[208:211]
X a[8:11],v8,2048
M a[212:215],a[76:79],a[52:55],a[212:215]
global_load_lds_dwordx4 v12, s[40:41] offset:1024 sc1
M a[216:219],a[76:79],a[56:59],a[216:219]
X a[12:15],v8,3072
M a[220:223],a[76:79],a[60:63],a[220:223]
global_load_lds_dwordx4 v12, s[40:41] offset:2048 sc1
M a[160:163],a[80:83],a[32:35],a[160:163]
T a[76:77],v9,17408
M a[164:167],a[80:83],a[36:39],a[164:167]
T a[78:79],v9,21504
M a[168:171],a[80:83],a[40:43],a[168:171]
X a[16:19],v8,4096
M a[172:175],a[80:83],a[44:47],a[172:175]
X a[20:23],v8,5120
M a[176:179],a[80:83],a[48:51],a[176:179]
X a[24:27],v8,6144
M a[180:183],a[80:83],a[52:55],a[180:183]
global_load_lds_dwordx4 v12, s[40:41] offset:3072 sc1
M a[184:187],a[80:83],a[56:59],a[184:187]
X a[28:31],v8,7168
M a[188:191],a[80:83],a[60:63],a[188:191]
v_add_u32 v12, 0x4000, v12
M a[224:227],a[84:87],a[32:35],a[224:227]
T a[80:81],v32,16384
M a[228:231],a[84:87],a[36:39],a[228:231]
T a[82:83],v32,20480
M a[232:235],a[84:87],a[40:43],a[232:235]
s_add_u32 m0, s55, 8192
M a[236:239],a[84:87],a[44:47],a[236:239]
global_load_lds_dwordx4 v11, s[50:51] offset:0
M a[240:243],a[84:87],a[48:51],a[240:243]
global_load_lds_dwordx4 v11, s[52:53] offset:1024
M a[244:247],a[84:87],a[52:55],a[244:247]
v_add_u32 v11, 229376, v11
M a[248:251],a[84:87],a[56:59],a[248:251]
M a[252:255],a[84:87],a[60:63],a[252:255]
s_waitcnt lgkmcnt(2)
M a[128:131],a[72:75],a[0:3],a[128:131]
T a[84:85],v32,17408
M a[132:135],a[72:75],a[4:7],a[132:135]
T a[86:87],v32,21504
M a[136:139],a[72:75],a[8:11],a[136:139]
M a[140:143],a[72:75],a[12:15],a[140:143]
M a[144:147],a[72:75],a[16:19],a[144:147]
M a[148:151],a[72:75],a[20:23],a[148:151]
M a[152:155],a[72:75],a[24:27],a[152:155]
M a[156:159],a[72:75],a[28:31],a[156:159]
s_waitcnt vmcnt(24) lgkmcnt(0)
s_barrier
s_cmp_lg_u32 s99, 0
s_cbranch_scc1 Lnf1Q_%=
s_mov_b64 exec, 1
v_mov_b32 v37, s98
global_store_dword v36, v37, s[96:97] offset:0 sc1
s_mov_b64 exec, -1
Lnf1Q_%=:
s_nop 0
M a[192:195],a[76:79],a[0:3],a[192:195]
T a[72:73],v9,24576
s_add_u32 m0, s54, 0
M a[196:199],a[76:79],a[4:7],a[196:199]
T a[74:75],v9,28672
global_load_lds_dwordx4 v12, s[40:41] offset:0 sc1
M a[200:203],a[76:79],a[8:11],a[200:203]
X a[32:35],v8,16384
M a[204:207],a[76:79],a[12:15],a[204:207]
X a[36:39],v8,17408
M a[208:211],a[76:79],a[16:19],a[208:211]
X a[40:43],v8,18432
M a[212:215],a[76:79],a[20:23],a[212:215]
global_load_lds_dwordx4 v12, s[40:41] offset:1024 sc1
M a[216:219],a[76:79],a[24:27],a[216:219]
X a[44:47],v8,19456
M a[220:223],a[76:79],a[28:31],a[220:223]
global_load_lds_dwordx4 v12, s[40:41] offset:2048 sc1
M a[160:163],a[80:83],a[0:3],a[160:163]
T a[76:77],v9,25600
M a[164:167],a[80:83],a[4:7],a[164:167]
T a[78:79],v9,29696
M a[168:171],a[80:83],a[8:11],a[168:171]
X a[48:51],v8,20480
M a[172:175],a[80:83],a[12:15],a[172:175]
X a[52:55],v8,21504
M a[176:179],a[80:83],a[16:19],a[176:179]
X a[56:59],v8,22528
M a[180:183],a[80:83],a[20:23],a[180:183]
global_load_lds_dwordx4 v12, s[40:41] offset:3072 sc1
M a[184:187],a[80:83],a[24:27],a[184:187]
X a[60:63],v8,23552
M a[188:191],a[80:83],a[28:31],a[188:191]
v_add_u32 v12, 0x4000, v12
M a[224:227],a[84:87],a[0:3],a[224:227]
T a[80:81],v32,24576
M a[228:231],a[84:87],a[4:7],a[228:231]
T a[82:83],v32,28672
M a[232:235],a[84:87],a[8:11],a[232:235]
s_add_u32 m0, s55, 16384
M a[236:239],a[84:87],a[12:15],a[236:239]
global_load_lds_dwordx4 v11, s[50:51] offset:0
M a[240:243],a[84:87],a[16:19],a[240:243]
global_load_lds_dwordx4 v11, s[52:53] offset:1024
M a[244:247],a[84:87],a[20:23],a[244:247]
v_add_u32 v11, 229376, v11
M a[248:251],a[84:87],a[24:27],a[248:251]
M a[252:255],a[84:87],a[28:31],a[252:255]
s_waitcnt lgkmcnt(2)
M a[128:131],a[72:75],a[32:35],a[128:131]
T a[84:85],v32,25600
M a[132:135],a[72:75],a[36:39],a[132:135]
T a[86:87],v32,29696
M a[136:139],a[72:75],a[40:43],a[136:139]
M a[140:143],a[72:75],a[44:47],a[140:143]
M a[144:147],a[72:75],a[48:51],a[144:147]
M a[148:151],a[72:75],a[52:55],a[148:151]
M a[152:155],a[72:75],a[56:59],a[152:155]
M a[156:159],a[72:75],a[60:63],a[156:159]
s_waitcnt vmcnt(24) lgkmcnt(0)
s_barrier
M a[192:195],a[76:79],a[32:35],a[192:195]
T a[72:73],v9,32768
s_add_u32 m0, s54, 16384
M a[196:199],a[76:79],a[36:39],a[196:199]
T a[74:75],v9,36864
global_load_lds_dwordx4 v12, s[40:41] offset:0 sc1
M a[200:203],a[76:79],a[40:43],a[200:203]
X a[0:3],v8,32768
M a[204:207],a[76:79],a[44:47],a[204:207]
X a[4:7],v8,33792
M a[208:211],a[76:79],a[48:51],a[208:211]
X a[8:11],v8,34816
M a[212:215],a[76:79],a[52:55],a[212:215]
global_load_lds_dwordx4 v12, s[40:41] offset:1024 sc1
M a[216:219],a[76:79],a[56:59],a[216:219]
X a[12:15],v8,35840
M a[220:223],a[76:79],a[60:63],a[220:223]
global_load_lds_dwordx4 v12, s[40:41] offset:2048 sc1
M a[160:163],a[80:83],a[32:35],a[160:163]
T a[76:77],v9,33792
M a[164:167],a[80:83],a[36:39],a[164:167]
T a[78:79],v9,37888
M a[168:171],a[80:83],a[40:43],a[168:171]
X a[16:19],v8,36864
M a[172:175],a[80:83],a[44:47],a[172:175]
X a[20:23],v8,37888
M a[176:179],a[80:83],a[48:51],a[176:179]
X a[24:27],v8,38912
M a[180:183],a[80:83],a[52:55],a[180:183]
global_load_lds_dwordx4 v12, s[40:41] offset:3072 sc1
M a[184:187],a[80:83],a[56:59],a[184:187]
X a[28:31],v8,39936
M a[188:191],a[80:83],a[60:63],a[188:191]
v_add_u32 v12, 0x4000, v12
M a[224:227],a[84:87],a[32:35],a[224:227]
T a[80:81],v32,32768
M a[228:231],a[84:87],a[36:39],a[228:231]
T a[82:83],v32,36864
M a[232:235],a[84:87],a[40:43],a[232:235]
s_add_u32 m0, s55, 24576
M a[236:239],a[84:87],a[44:47],a[236:239]
global_load_lds_dwordx4 v11, s[50:51] offset:0
M a[240:243],a[84:87],a[48:51],a[240:243]
global_load_lds_dwordx4 v11, s[52:53] offset:1024
M a[244:247],a[84:87],a[52:55],a[244:247]
v_add_u32 v11, 229376, v11
M a[248:251],a[84:87],a[56:59],a[248:251]
M a[252:255],a[84:87],a[60:63],a[252:255]
s_waitcnt lgkmcnt(2)
M a[128:131],a[72:75],a[0:3],a[128:131]
T a[84:85],v32,33792
M a[132:135],a[72:75],a[4:7],a[132:135]
T a[86:87],v32,37888
M a[136:139],a[72:75],a[8:11],a[136:139]
M a[140:143],a[72:75],a[12:15],a[140:143]
M a[144:147],a[72:75],a[16:19],a[144:147]
M a[148:151],a[72:75],a[20:23],a[148:151]
M a[152:155],a[72:75],a[24:27],a[152:155]
M a[156:159],a[72:75],a[28:31],a[156:159]
s_waitcnt vmcnt(24) lgkmcnt(0)
s_barrier
M a[192:195],a[76:79],a[0:3],a[192:195]
T a[72:73],v9,40960
s_add_u32 m0, s54, 32768
M a[196:199],a[76:79],a[4:7],a[196:199]
T a[74:75],v9,45056
global_load_lds_dwordx4 v12, s[40:41] offset:0 sc1
M a[200:203],a[76:79],a[8:11],a[200:203]
X a[32:35],v8,49152
M a[204:207],a[76:79],a[12:15],a[204:207]
X a[36:39],v8,50176
M a[208:211],a[76:79],a[16:19],a[208:211]
X a[40:43],v8,51200
M a[212:215],a[76:79],a[20:23],a[212:215]
global_load_lds_dwordx4 v12, s[40:41] offset:1024 sc1
M a[216:219],a[76:79],a[24:27],a[216:219]
X a[44:47],v8,52224
M a[220:223],a[76:79],a[28:31],a[220:223]
global_load_lds_dwordx4 v12, s[40:41] offset:2048 sc1
M a[160:163],a[80:83],a[0:3],a[160:163]
T a[76:77],v9,41984
M a[164:167],a[80:83],a[4:7],a[164:167]
T a[78:79],v9,46080
M a[168:171],a[80:83],a[8:11],a[168:171]
X a[48:51],v8,53248
M a[172:175],a[80:83],a[12:15],a[172:175]
X a[52:55],v8,54272
M a[176:179],a[80:83],a[16:19],a[176:179]
X a[56:59],v8,55296
M a[180:183],a[80:83],a[20:23],a[180:183]
global_load_lds_dwordx4 v12, s[40:41] offset:3072 sc1
M a[184:187],a[80:83],a[24:27],a[184:187]
X a[60:63],v8,56320
M a[188:191],a[80:83],a[28:31],a[188:191]
v_add_u32 v12, 0x4000, v12
M a[224:227],a[84:87],a[0:3],a[224:227]
T a[80:81],v32,40960
M a[228:231],a[84:87],a[4:7],a[228:231]
T a[82:83],v32,45056
M a[232:235],a[84:87],a[8:11],a[232:235]
s_add_u32 m0, s55, 32768
M a[236:239],a[84:87],a[12:15],a[236:239]
global_load_lds_dwordx4 v11, s[50:51] offset:0
M a[240:243],a[84:87],a[16:19],a[240:243]
global_load_lds_dwordx4 v11, s[52:53] offset:1024
M a[244:247],a[84:87],a[20:23],a[244:247]
v_add_u32 v11, 229376, v11
M a[248:251],a[84:87],a[24:27],a[248:251]
M a[252:255],a[84:87],a[28:31],a[252:255]
s_waitcnt lgkmcnt(2)
M a[128:131],a[72:75],a[32:35],a[128:131]
T a[84:85],v32,41984
M a[132:135],a[72:75],a[36:39],a[132:135]
T a[86:87],v32,46080
M a[136:139],a[72:75],a[40:43],a[136:139]
M a[140:143],a[72:75],a[44:47],a[140:143]
M a[144:147],a[72:75],a[48:51],a[144:147]
M a[148:151],a[72:75],a[52:55],a[148:151]
M a[152:155],a[72:75],a[56:59],a[152:155]
M a[156:159],a[72:75],a[60:63],a[156:159]
s_waitcnt vmcnt(24) lgkmcnt(0)
s_barrier
M a[192:195],a[76:79],a[32:35],a[192:195]
T a[72:73],v9,0
s_add_u32 m0, s54, 49152
M a[196:199],a[76:79],a[36:39],a[196:199]
T a[74:75],v9,4096
global_load_lds_dwordx4 v12, s[40:41] offset:0 sc1
M a[200:203],a[76:79],a[40:43],a[200:203]
X a[0:3],v13,0
M a[204:207],a[76:79],a[44:47],a[204:207]
X a[4:7],v13,1024
M a[208:211],a[76:79],a[48:51],a[208:211]
X a[8:11],v13,2048
M a[212:215],a[76:79],a[52:55],a[212:215]
global_load_lds_dwordx4 v12, s[40:41] offset:1024 sc1
M a[216:219],a[76:79],a[56:59],a[216:219]
X a[12:15],v13,3072
M a[220:223],a[76:79],a[60:63],a[220:223]
global_load_lds_dwordx4 v12, s[40:41] offset:2048 sc1
M a[160:163],a[80:83],a[32:35],a[160:163]
T a[76:77],v9,1024
M a[164:167],a[80:83],a[36:39],a[164:167]
T a[78:79],v9,5120
M a[168:171],a[80:83],a[40:43],a[168:171]
X a[16:19],v13,4096
M a[172:175],a[80:83],a[44:47],a[172:175]
X a[20:23],v13,5120
M a[176:179],a[80:83],a[48:51],a[176:179]
X a[24:27],v13,6144
global_load_dword v35, v34, s[96:97] offset:0 sc1
M a[180:183],a[80:83],a[52:55],a[180:183]
global_load_lds_dwordx4 v12, s[40:41] offset:3072 sc1
M a[184:187],a[80:83],a[56:59],a[184:187]
X a[28:31],v13,7168
M a[188:191],a[80:83],a[60:63],a[188:191]
v_add_u32 v12, 0x4000, v12
M a[224:227],a[84:87],a[32:35],a[224:227]
T a[80:81],v32,0
M a[228:231],a[84:87],a[36:39],a[228:231]
T a[82:83],v32,4096
M a[232:235],a[84:87],a[40:43],a[232:235]
s_add_u32 m0, s55, 40960
M a[236:239],a[84:87],a[44:47],a[236:239]
global_load_lds_dwordx4 v11, s[50:51] offset:0
M a[240:243],a[84:87],a[48:51],a[240:243]
global_load_lds_dwordx4 v11, s[52:53] offset:1024
M a[244:247],a[84:87],a[52:55],a[244:247]
v_add_u32 v11, 229376, v11
M a[248:251],a[84:87],a[56:59],a[248:251]
M a[252:255],a[84:87],a[60:63],a[252:255]
s_waitcnt lgkmcnt(2)
M a[128:131],a[72:75],a[0:3],a[128:131]
T a[84:85],v32,1024
M a[132:135],a[72:75],a[4:7],a[132:135]
T a[86:87],v32,5120
M a[136:139],a[72:75],a[8:11],a[136:139]
M a[140:143],a[72:75],a[12:15],a[140:143]
M a[144:147],a[72:75],a[16:19],a[144:147]
M a[148:151],a[72:75],a[20:23],a[148:151]
M a[152:155],a[72:75],a[24:27],a[152:155]
M a[156:159],a[72:75],a[28:31],a[156:159]
s_waitcnt vmcnt(25) lgkmcnt(0)
s_barrier
M a[192:195],a[76:79],a[0:3],a[192:195]
T a[72:73],v9,8192
s_add_u32 m0, s54, 131072
M a[196:199],a[76:79],a[4:7],a[196:199]
T a[74:75],v9,12288
global_load_lds_dwordx4 v12, s[40:41] offset:0 sc1
M a[200:203],a[76:79],a[8:11],a[200:203]
X a[32:35],v13,16384
M a[204:207],a[76:79],a[12:15],a[204:207]
X a[36:39],v13,17408
M a[208:211],a[76:79],a[16:19],a[208:211]
X a[40:43],v13,18432
M a[212:215],a[76:79],a[20:23],a[212:215]
global_load_lds_dwordx4 v12, s[40:41] offset:1024 sc1
M a[216:219],a[76:79],a[24:27],a[216:219]
X a[44:47],v13,19456
M a[220:223],a[76:79],a[28:31],a[220:223]
global_load_lds_dwordx4 v12, s[40:41] offset:2048 sc1
M a[160:163],a[80:83],a[0:3],a[160:163]
T a[76:77],v9,9216
M a[164:167],a[80:83],a[4:7],a[164:167]
T a[78:79],v9,13312
M a[168:171],a[80:83],a[8:11],a[168:171]
X a[48:51],v13,20480
M a[172:175],a[80:83],a[12:15],a[172:175]
X a[52:55],v13,21504
M a[176:179],a[80:83],a[16:19],a[176:179]
X a[56:59],v13,22528
M a[180:183],a[80:83],a[20:23],a[180:183]
global_load_lds_dwordx4 v12, s[40:41] offset:3072 sc1
M a[184:187],a[80:83],a[24:27],a[184:187]
X a[60:63],v13,23552
M a[188:191],a[80:83],a[28:31],a[188:191]
v_add_u32 v12, 0x4000, v12
M a[224:227],a[84:87],a[0:3],a[224:227]
T a[80:81],v32,8192
M a[228:231],a[84:87],a[4:7],a[228:231]
T a[82:83],v32,12288
M a[232:235],a[84:87],a[8:11],a[232:235]
s_add_u32 m0, s55, 0
M a[236:239],a[84:87],a[12:15],a[236:239]
global_load_lds_dwordx4 v11, s[50:51] offset:0
M a[240:243],a[84:87],a[16:19],a[240:243]
global_load_lds_dwordx4 v11, s[52:53] offset:1024
M a[244:247],a[84:87],a[20:23],a[244:247]
v_add_u32 v11, 229376, v11
s_add_u32 m0, s99, 114688
M a[248:251],a[84:87],a[24:27],a[248:251]
global_load_lds_dwordx4 v18, s[82:83] offset:0
global_load_lds_dwordx4 v19, s[82:83] offset:1024
M a[252:255],a[84:87],a[28:31],a[252:255]
global_load_lds_dwordx4 v20, s[82:83] offset:2048
global_load_lds_dwordx4 v21, s[82:83] offset:3072
s_waitcnt lgkmcnt(2)
M a[128:131],a[72:75],a[32:35],a[128:131]
T a[84:85],v32,9216
M a[132:135],a[72:75],a[36:39],a[132:135]
T a[86:87],v32,13312
M a[136:139],a[72:75],a[40:43],a[136:139]
M a[140:143],a[72:75],a[44:47],a[140:143]
M a[144:147],a[72:75],a[48:51],a[144:147]
M a[148:151],a[72:75],a[52:55],a[148:151]
M a[152:155],a[72:75],a[56:59],a[152:155]
M a[156:159],a[72:75],a[60:63],a[156:159]
s_waitcnt vmcnt(29) lgkmcnt(0)
s_barrier
s_waitcnt vmcnt(13)
v_cmp_ne_u32 vcc, s98, v35
s_nop 3
s_and_b32 vcc_lo, vcc_lo, 0xffff
s_cbranch_scc0 Lok1Q_%=
s_mov_b32 s65, 0x100000
Lsp1Q_%=:
s_sleep 1
global_load_dword v35, v34, s[96:97] offset:0 sc1
s_waitcnt vmcnt(0)
v_cmp_ne_u32 vcc, s98, v35
s_nop 3
s_and_b32 vcc_lo, vcc_lo, 0xffff
s_cbranch_scc0 Lok1Q_%=
s_sub_u32 s65, s65, 1
s_cmp_lg_u32 s65, 0
s_cbranch_scc1 Lsp1Q_%=
Lok1Q_%=:
M a[192:195],a[76:79],a[32:35],a[192:195]
T a[72:73],v9,16384
s_add_u32 m0, s54, 147456
M a[196:199],a[76:79],a[36:39],a[196:199]
T a[74:75],v9,20480
global_load_lds_dwordx4 v12, s[40:41] offset:0 sc1
M a[200:203],a[76:79],a[40:43],a[200:203]
X a[0:3],v8,0
M a[204:207],a[76:79],a[44:47],a[204:207]
X a[4:7],v8,1024
M a[208:211],a[76:79],a[48:51],a[208:211]
X a[8:11],v8,2048
global_load_dwordx4 v[128:131], v16, s[80:81] offset:0 sc1
M a[212:215],a[76:79],a[52:55],a[212:215]
global_load_lds_dwordx4 v12, s[40:41] offset:1024 sc1
M a[216:219],a[76:79],a[56:59],a[216:219]
X a[12:15],v8,3072
M a[220:223],a[76:79],a[60:63],a[220:223]
global_load_lds_dwordx4 v12, s[40:41] offset:2048 sc1
M a[160:163],a[80:83],a[32:35],a[160:163]
T a[76:77],v9,17408
global_load_dwordx4 v[132:135], v17, s[80:81] offset:0 sc1
M a[164:167],a[80:83],a[36:39],a[164:167]
T a[78:79],v9,21504
M a[168:171],a[80:83],a[40:43],a[168:171]
X a[16:19],v8,4096
M a[172:175],a[80:83],a[44:47],a[172:175]
X a[20:23],v8,5120
M a[176:179],a[80:83],a[48:51],a[176:179]
X a[24:27],v8,6144
global_load_dwordx4 v[136:139], v16, s[80:81] offset:1024 sc1
M a[180:183],a[80:83],a[52:55],a[180:183]
global_load_lds_dwordx4 v12, s[40:41] offset:3072 sc1
M a[184:187],a[80:83],a[56:59],a[184:187]
X a[28:31],v8,7168
M a[188:191],a[80:83],a[60:63],a[188:191]
v_add_u32 v12, 0x4000, v12
M a[224:227],a[84:87],a[32:35],a[224:227]
T a[80:81],v32,16384
global_load_dwordx4 v[140:143], v17, s[80:81] offset:1024 sc1
M a[228:231],a[84:87],a[36:39],a[228:231]
T a[82:83],v32,20480
M a[232:235],a[84:87],a[40:43],a[232:235]
s_add_u32 m0, s55, 8192
M a[236:239],a[84:87],a[44:47],a[236:239]
global_load_lds_dwordx4 v11, s[50:51] offset:0
M a[240:243],a[84:87],a[48:51],a[240:243]
global_load_lds_dwordx4 v11, s[52:53] offset:1024
M a[244:247],a[84:87],a[52:55],a[244:247]
v_add_u32 v11, 229376, v11
M a[248:251],a[84:87],a[56:59],a[248:251]
M a[252:255],a[84:87],a[60:63],a[252:255]
s_waitcnt lgkmcnt(2)
M a[128:131],a[72:75],a[0:3],a[128:131]
T a[84:85],v32,17408
M a[132:135],a[72:75],a[4:7],a[132:135]
T a[86:87],v32,21504
M a[136:139],a[72:75],a[8:11],a[136:139]
M a[140:143],a[72:75],a[12:15],a[140:143]
M a[144:147],a[72:75],a[16:19],a[144:147]
M a[148:151],a[72:75],a[20:23],a[148:151]
M a[152:155],a[72:75],a[24:27],a[152:155]
M a[156:159],a[72:75],a[28:31],a[156:159]
s_waitcnt vmcnt(33) lgkmcnt(0)
s_barrier
M a[192:195],a[76:79],a[0:3],a[192:195]
T a[72:73],v9,24576
M a[196:199],a[76:79],a[4:7],a[196:199]
T a[74:75],v9,28672
s_add_u32 s82, s82, 131072
s_addc_u32 s83, s83, 0
s_add_u32 m0, s99, 0
M a[200:203],a[76:79],a[8:11],a[200:203]
X a[32:35],v8,16384
s_add_u32 s80, s80, 2048
s_addc_u32 s81, s81, 0
M a[204:207],a[76:79],a[12:15],a[204:207]
X a[36:39],v8,17408
global_load_lds_dwordx4 v18, s[82:83] offset:0
M a[208:211],a[76:79],a[16:19],a[208:211]
X a[40:43],v8,18432
M a[212:215],a[76:79],a[20:23],a[212:215]
global_load_dwordx4 v[144:147], v16, s[80:81] offset:0 sc1
M a[216:219],a[76:79],a[24:27],a[216:219]
X a[44:47],v8,19456
M a[220:223],a[76:79],a[28:31],a[220:223]
global_load_lds_dwordx4 v19, s[82:83] offset:1024
M a[160:163],a[80:83],a[0:3],a[160:163]
T a[76:77],v9,25600
M a[164:167],a[80:83],a[4:7],a[164:167]
T a[78:79],v9,29696
global_load_dwordx4 v[148:151], v17, s[80:81] offset:0 sc1
M a[168:171],a[80:83],a[8:11],a[168:171]
X a[48:51],v8,20480
M a[172:175],a[80:83],a[12:15],a[172:175]
X a[52:55],v8,21504
global_load_lds_dwordx4 v20, s[82:83] offset:2048
M a[176:179],a[80:83],a[16:19],a[176:179]
X a[56:59],v8,22528
M a[180:183],a[80:83],a[20:23],a[180:183]
global_load_dwordx4 v[152:155], v16, s[80:81] offset:1024 sc1
M a[184:187],a[80:83],a[24:27],a[184:187]
X a[60:63],v8,23552
M a[188:191],a[80:83],a[28:31],a[188:191]
global_load_lds_dwordx4 v21, s[82:83] offset:3072
M a[224:227],a[84:87],a[0:3],a[224:227]
T a[80:81],v32,24576
M a[228:231],a[84:87],a[4:7],a[228:231]
T a[82:83],v32,28672
global_load_dwordx4 v[156:159], v17, s[80:81] offset:1024 sc1
M a[232:235],a[84:87],a[8:11],a[232:235]
M a[236:239],a[84:87],a[12:15],a[236:239]
M a[240:243],a[84:87],a[16:19],a[240:243]
M a[244:247],a[84:87],a[20:23],a[244:247]
M a[248:251],a[84:87],a[24:27],a[248:251]
M a[252:255],a[84:87],a[28:31],a[252:255]
s_waitcnt lgkmcnt(2)
M a[128:131],a[72:75],a[32:35],a[128:131]
T a[84:85],v32,25600
M a[132:135],a[72:75],a[36:39],a[132:135]
T a[86:87],v32,29696
M a[136:139],a[72:75],a[40:43],a[136:139]
M a[140:143],a[72:75],a[44:47],a[140:143]
M a[144:147],a[72:75],a[48:51],a[144:147]
M a[148:151],a[72:75],a[52:55],a[148:151]
M a[152:155],a[72:75],a[56:59],a[152:155]
M a[156:159],a[72:75],a[60:63],a[156:159]
s_waitcnt vmcnt(35) lgkmcnt(0)
s_barrier
M a[192:195],a[76:79],a[32:35],a[192:195]
T a[72:73],v9,32768
M a[196:199],a[76:79],a[36:39],a[196:199]
T a[74:75],v9,36864
s_add_u32 s82, s82, 131072
s_addc_u32 s83, s83, 0
s_add_u32 m0, s99, 16384
M a[200:203],a[76:79],a[40:43],a[200:203]
X a[0:3],v8,32768
s_add_u32 s80, s80, 2048
s_addc_u32 s81, s81, 0
M a[204:207],a[76:79],a[44:47],a[204:207]
X a[4:7],v8,33792
global_load_lds_dwordx4 v18, s[82:83] offset:0
M a[208:211],a[76:79],a[48:51],a[208:211]
X a[8:11],v8,34816
M a[212:215],a[76:79],a[52:55],a[212:215]
global_load_dwordx4 v[160:163], v16, s[80:81] offset:0 sc1
M a[216:219],a[76:79],a[56:59],a[216:219]
X a[12:15],v8,35840
M a[220:223],a[76:79],a[60:63],a[220:223]
global_load_lds_dwordx4 v19, s[82:83] offset:1024
M a[160:163],a[80:83],a[32:35],a[160:163]
T a[76:77],v9,33792
M a[164:167],a[80:83],a[36:39],a[164:167]
T a[78:79],v9,37888
global_load_dwordx4 v[164:167], v17, s[80:81] offset:0 sc1
M a[168:171],a[80:83],a[40:43],a[168:171]
X a[16:19],v8,36864
M a[172:175],a[80:83],a[44:47],a[172:175]
X a[20:23],v8,37888
global_load_lds_dwordx4 v20, s[82:83] offset:2048
M a[176:179],a[80:83],a[48:51],a[176:179]
X a[24:27],v8,38912
M a[180:183],a[80:83],a[52:55],a[180:183]
global_load_dwordx4 v[168:171], v16, s[80:81] offset:1024 sc1
M a[184:187],a[80:83],a[56:59],a[184:187]
X a[28:31],v8,39936
M a[188:191],a[80:83],a[60:63],a[188:191]
global_load_lds_dwordx4 v21, s[82:83] offset:3072
M a[224:227],a[84:87],a[32:35],a[224:227]
T a[80:81],v32,32768
M a[228:231],a[84:87],a[36:39],a[228:231]
T a[82:83],v32,36864
global_load_dwordx4 v[172:175], v17, s[80:81] offset:1024 sc1
M a[232:235],a[84:87],a[40:43],a[232:235]
M a[236:239],a[84:87],a[44:47],a[236:239]
M a[240:243],a[84:87],a[48:51],a[240:243]
M a[244:247],a[84:87],a[52:55],a[244:247]
M a[248:251],a[84:87],a[56:59],a[248:251]
M a[252:255],a[84:87],a[60:63],a[252:255]
s_waitcnt lgkmcnt(2)
M a[128:131],a[72:75],a[0:3],a[128:131]
T a[84:85],v32,33792
M a[132:135],a[72:75],a[4:7],a[132:135]
T a[86:87],v32,37888
M a[136:139],a[72:75],a[8:11],a[136:139]
M a[140:143],a[72:75],a[12:15],a[140:143]
M a[144:147],a[72:75],a[16:19],a[144:147]
M a[148:151],a[72:75],a[20:23],a[148:151]
M a[152:155],a[72:75],a[24:27],a[152:155]
M a[156:159],a[72:75],a[28:31],a[156:159]
s_waitcnt vmcnt(36) lgkmcnt(0)
s_barrier
M a[192:195],a[76:79],a[0:3],a[192:195]
T a[72:73],v9,40960
M a[196:199],a[76:79],a[4:7],a[196:199]
T a[74:75],v9,45056
s_add_u32 s82, s82, 131072
s_addc_u32 s83, s83, 0
s_add_u32 m0, s99, 81920
M a[200:203],a[76:79],a[8:11],a[200:203]
X a[32:35],v8,49152
s_add_u32 s80, s80, 2048
s_addc_u32 s81, s81, 0
M a[204:207],a[76:79],a[12:15],a[204:207]
X a[36:39],v8,50176
global_load_lds_dwordx4 v18, s[82:83] offset:0
M a[208:211],a[76:79],a[16:19],a[208:211]
X a[40:43],v8,51200
M a[212:215],a[76:79],a[20:23],a[212:215]
global_load_dwordx4 v[176:179], v16, s[80:81] offset:0 sc1
M a[216:219],a[76:79],a[24:27],a[216:219]
X a[44:47],v8,52224
M a[220:223],a[76:79],a[28:31],a[220:223]
global_load_lds_dwordx4 v19, s[82:83] offset:1024
M a[160:163],a[80:83],a[0:3],a[160:163]
T a[76:77],v9,41984
M a[164:167],a[80:83],a[4:7],a[164:167]
T a[78:79],v9,46080
global_load_dwordx4 v[180:183], v17, s[80:81] offset:0 sc1
M a[168:171],a[80:83],a[8:11],a[168:171]
X a[48:51],v8,53248
M a[172:175],a[80:83],a[12:15],a[172:175]
X a[52:55],v8,54272
global_load_lds_dwordx4 v20, s[82:83] offset:2048
M a[176:179],a[80:83],a[16:19],a[176:179]
X a[56:59],v8,55296
M a[180:183],a[80:83],a[20:23],a[180:183]
global_load_dwordx4 v[184:187], v16, s[80:81] offset:1024 sc1
M a[184:187],a[80:83],a[24:27],a[184:187]
X a[60:63],v8,56320
M a[188:191],a[80:83],a[28:31],a[188:191]
global_load_lds_dwordx4 v21, s[82:83] offset:3072
M a[224:227],a[84:87],a[0:3],a[224:227]
T a[80:81],v32,40960
M a[228:231],a[84:87],a[4:7],a[228:231]
T a[82:83],v32,45056
global_load_dwordx4 v[188:191], v17, s[80:81] offset:1024 sc1
M a[232:235],a[84:87],a[8:11],a[232:235]
M a[236:239],a[84:87],a[12:15],a[236:239]
M a[240:243],a[84:87],a[16:19],a[240:243]
M a[244:247],a[84:87],a[20:23],a[244:247]
M a[248:251],a[84:87],a[24:27],a[248:251]
M a[252:255],a[84:87],a[28:31],a[252:255]
s_waitcnt lgkmcnt(2)
M a[128:131],a[72:75],a[32:35],a[128:131]
T a[84:85],v32,41984
M a[132:135],a[72:75],a[36:39],a[132:135]
T a[86:87],v32,46080
M a[136:139],a[72:75],a[40:43],a[136:139]
M a[140:143],a[72:75],a[44:47],a[140:143]
M a[144:147],a[72:75],a[48:51],a[144:147]
M a[148:151],a[72:75],a[52:55],a[148:151]
M a[152:155],a[72:75],a[56:59],a[152:155]
M a[156:159],a[72:75],a[60:63],a[156:159]
s_waitcnt vmcnt(38) lgkmcnt(0)
s_barrier
M a[192:195],a[76:79],a[32:35],a[192:195]
T a[72:73],v9,0
M a[196:199],a[76:79],a[36:39],a[196:199]
T a[74:75],v9,4096
s_add_u32 s82, s82, 131072
s_addc_u32 s83, s83, 0
s_add_u32 m0, s99, 32768
M a[200:203],a[76:79],a[40:43],a[200:203]
X a[0:3],v13,0
s_add_u32 s80, s80, 2048
s_addc_u32 s81, s81, 0
M a[204:207],a[76:79],a[44:47],a[204:207]
X a[4:7],v13,1024
global_load_lds_dwordx4 v18, s[82:83] offset:0
M a[208:211],a[76:79],a[48:51],a[208:211]
X a[8:11],v13,2048
M a[212:215],a[76:79],a[52:55],a[212:215]
global_load_dwordx4 v[192:195], v16, s[80:81] offset:0 sc1
M a[216:219],a[76:79],a[56:59],a[216:219]
X a[12:15],v13,3072
M a[220:223],a[76:79],a[60:63],a[220:223]
global_load_lds_dwordx4 v19, s[82:83] offset:1024
M a[160:163],a[80:83],a[32:35],a[160:163]
T a[76:77],v9,1024
M a[164:167],a[80:83],a[36:39],a[164:167]
T a[78:79],v9,5120
global_load_dwordx4 v[196:199], v17, s[80:81] offset:0 sc1
M a[168:171],a[80:83],a[40:43],a[168:171]
X a[16:19],v13,4096
M a[172:175],a[80:83],a[44:47],a[172:175]
X a[20:23],v13,5120
global_load_lds_dwordx4 v20, s[82:83] offset:2048
M a[176:179],a[80:83],a[48:51],a[176:179]
X a[24:27],v13,6144
M a[180:183],a[80:83],a[52:55],a[180:183]
global_load_dwordx4 v[200:203], v16, s[80:81] offset:1024 sc1
M a[184:187],a[80:83],a[56:59],a[184:187]
X a[28:31],v13,7168
M a[188:191],a[80:83],a[60:63],a[188:191]
global_load_lds_dwordx4 v21, s[82:83] offset:3072
M a[224:227],a[84:87],a[32:35],a[224:227]
T a[80:81],v32,0
M a[228:231],a[84:87],a[36:39],a[228:231]
T a[82:83],v32,4096
global_load_dwordx4 v[204:207], v17, s[80:81] offset:1024 sc1
M a[232:235],a[84:87],a[40:43],a[232:235]
M a[236:239],a[84:87],a[44:47],a[236:239]
M a[240:243],a[84:87],a[48:51],a[240:243]
M a[244:247],a[84:87],a[52:55],a[244:247]
M a[248:251],a[84:87],a[56:59],a[248:251]
M a[252:255],a[84:87],a[60:63],a[252:255]
s_waitcnt lgkmcnt(2)
M a[128:131],a[72:75],a[0:3],a[128:131]
T a[84:85],v32,1024
M a[132:135],a[72:75],a[4:7],a[132:135]
T a[86:87],v32,5120
M a[136:139],a[72:75],a[8:11],a[136:139]
v_xor_b32 v24, 0, v22
v_xor_b32 v25, 32, v22
M a[140:143],a[72:75],a[12:15],a[140:143]
v_xor_b32 v26, 64, v22
v_xor_b32 v27, 96, v22
M a[144:147],a[72:75],a[16:19],a[144:147]
v_add_u32 v28, 0x10000, v24
v_add_u32 v29, 0x10000, v25
M a[148:151],a[72:75],a[20:23],a[148:151]
v_add_u32 v30, 0x10000, v26
v_add_u32 v31, 0x10000, v27
M a[152:155],a[72:75],a[24:27],a[152:155]
M a[156:159],a[72:75],a[28:31],a[156:159]
s_waitcnt vmcnt(32) lgkmcnt(0)
s_barrier
M a[192:195],a[76:79],a[0:3],a[192:195]
T a[72:73],v9,8192
M a[196:199],a[76:79],a[4:7],a[196:199]
T a[74:75],v9,12288
s_add_u32 s82, s82, 131072
s_addc_u32 s83, s83, 0
s_add_u32 m0, s99, 49152
M a[200:203],a[76:79],a[8:11],a[200:203]
X a[32:35],v13,16384
s_add_u32 s80, s80, 2048
s_addc_u32 s81, s81, 0
M a[204:207],a[76:79],a[12:15],a[204:207]
X a[36:39],v13,17408
global_load_lds_dwordx4 v18, s[82:83] offset:0
M a[208:211],a[76:79],a[16:19],a[208:211]
X a[40:43],v13,18432
M a[212:215],a[76:79],a[20:23],a[212:215]
global_load_dwordx4 v[208:211], v16, s[80:81] offset:0 sc1
M a[216:219],a[76:79],a[24:27],a[216:219]
X a[44:47],v13,19456
M a[220:223],a[76:79],a[28:31],a[220:223]
global_load_lds_dwordx4 v19, s[82:83] offset:1024
M a[160:163],a[80:83],a[0:3],a[160:163]
T a[76:77],v9,9216
M a[164:167],a[80:83],a[4:7],a[164:167]
T a[78:79],v9,13312
global_load_dwordx4 v[212:215], v17, s[80:81] offset:0 sc1
M a[168:171],a[80:83],a[8:11],a[168:171]
X a[48:51],v13,20480
M a[172:175],a[80:83],a[12:15],a[172:175]
X a[52:55],v13,21504
global_load_lds_dwordx4 v20, s[82:83] offset:2048
M a[176:179],a[80:83],a[16:19],a[176:179]
X a[56:59],v13,22528
M a[180:183],a[80:83],a[20:23],a[180:183]
global_load_dwordx4 v[216:219], v16, s[80:81] offset:1024 sc1
M a[184:187],a[80:83],a[24:27],a[184:187]
X a[60:63],v13,23552
M a[188:191],a[80:83],a[28:31],a[188:191]
global_load_lds_dwordx4 v21, s[82:83] offset:3072
M a[224:227],a[84:87],a[0:3],a[224:227]
T a[80:81],v32,8192
M a[228:231],a[84:87],a[4:7],a[228:231]
T a[82:83],v32,12288
global_load_dwordx4 v[220:223], v17, s[80:81] offset:1024 sc1
M a[232:235],a[84:87],a[8:11],a[232:235]
M a[236:239],a[84:87],a[12:15],a[236:239]
M a[240:243],a[84:87],a[16:19],a[240:243]
M a[244:247],a[84:87],a[20:23],a[244:247]
M a[248:251],a[84:87],a[24:27],a[248:251]
M a[252:255],a[84:87],a[28:31],a[252:255]
s_waitcnt lgkmcnt(2)
M a[128:131],a[72:75],a[32:35],a[128:131]
T a[84:85],v32,9216
M a[132:135],a[72:75],a[36:39],a[132:135]
T a[86:87],v32,13312
M a[136:139],a[72:75],a[40:43],a[136:139]
M a[140:143],a[72:75],a[44:47],a[140:143]
M a[144:147],a[72:75],a[48:51],a[144:147]
M a[148:151],a[72:75],a[52:55],a[148:151]
M a[152:155],a[72:75],a[56:59],a[152:155]
M a[156:159],a[72:75],a[60:63],a[156:159]
s_waitcnt lgkmcnt(0)
s_barrier
M a[192:195],a[76:79],a[32:35],a[192:195]
T a[96:97],v28,49152
M a[196:199],a[76:79],a[36:39],a[196:199]
s_add_u32 s82, s82, 131072
s_addc_u32 s83, s83, 0
s_add_u32 m0, s99, 98304
T a[98:99],v28,57344
M a[200:203],a[76:79],a[40:43],a[200:203]
s_add_u32 s80, s80, 2048
s_addc_u32 s81, s81, 0
M a[204:207],a[76:79],a[44:47],a[204:207]
global_load_lds_dwordx4 v18, s[82:83] offset:0
T a[100:101],v29,49152
M a[208:211],a[76:79],a[48:51],a[208:211]
T a[102:103],v29,57344
M a[212:215],a[76:79],a[52:55],a[212:215]
global_load_dwordx4 v[224:227], v16, s[80:81] offset:0 sc1
M a[216:219],a[76:79],a[56:59],a[216:219]
T a[104:105],v30,49152
M a[220:223],a[76:79],a[60:63],a[220:223]
global_load_lds_dwordx4 v19, s[82:83] offset:1024
T a[106:107],v30,57344
M a[160:163],a[80:83],a[32:35],a[160:163]
M a[164:167],a[80:83],a[36:39],a[164:167]
global_load_dwordx4 v[228:231], v17, s[80:81] offset:0 sc1
T a[108:109],v31,49152
M a[168:171],a[80:83],a[40:43],a[168:171]
T a[110:111],v31,57344
M a[172:175],a[80:83],a[44:47],a[172:175]
M a[176:179],a[80:83],a[48:51],a[176:179]
T a[112:113],v28,50176
M a[180:183],a[80:83],a[52:55],a[180:183]
T a[114:115],v28,58368
M a[184:187],a[80:83],a[56:59],a[184:187]
M a[188:191],a[80:83],a[60:63],a[188:191]
T a[116:117],v29,50176
M a[224:227],a[84:87],a[32:35],a[224:227]
T a[118:119],v29,58368
M a[228:231],a[84:87],a[36:39],a[228:231]
M a[232:235],a[84:87],a[40:43],a[232:235]
T a[120:121],v30,50176
M a[236:239],a[84:87],a[44:47],a[236:239]
T a[122:123],v30,58368
M a[240:243],a[84:87],a[48:51],a[240:243]
M a[244:247],a[84:87],a[52:55],a[244:247]
T a[124:125],v31,50176
M a[248:251],a[84:87],a[56:59],a[248:251]
T a[126:127],v31,58368
M a[252:255],a[84:87],a[60:63],a[252:255]
s_waitcnt vmcnt(46)
s_waitcnt lgkmcnt(8)
M v[64:67],a[96:99],v[128:131],0
s_add_u32 s70, s60, 147456
s_addc_u32 s71, s61, 0
v_accvgpr_read_b32 v48, a128
v_accvgpr_read_b32 v40, a132
v_accvgpr_read_b32 v49, a129
v_accvgpr_read_b32 v41, a133
M v[96:99],a[96:99],v[132:135],0
T a[96:97],v28,51200
global_load_lds_dwordx4 v20, s[82:83] offset:2048
v_accvgpr_read_b32 v50, a130
v_accvgpr_read_b32 v42, a134
v_accvgpr_read_b32 v51, a131
v_accvgpr_read_b32 v43, a135
M v[68:71],a[100:103],v[128:131],0
T a[98:99],v28,59392
s_mov_b32 s66, 0xffff
s_mov_b32 s67, 0xffff0000
v_accvgpr_read_b32 v56, a192
v_accvgpr_read_b32 v8, a196
v_accvgpr_read_b32 v57, a193
M v[100:103],a[100:103],v[132:135],0
T a[100:101],v29,51200
v_accvgpr_read_b32 v9, a197
v_accvgpr_read_b32 v58, a194
v_accvgpr_read_b32 v10, a198
v_accvgpr_read_b32 v59, a195
v_accvgpr_read_b32 v11, a199
M v[72:75],a[104:107],v[128:131],0
T a[102:103],v29,59392
v_accvgpr_read_b32 v52, a160
v_accvgpr_read_b32 v44, a164
v_accvgpr_read_b32 v53, a161
v_accvgpr_read_b32 v45, a165
v_accvgpr_read_b32 v54, a162
M v[104:107],a[104:107],v[132:135],0
T a[104:105],v30,51200
global_load_dwordx4 v[232:235], v16, s[80:81] offset:1024 sc1
v_accvgpr_read_b32 v46, a166
v_accvgpr_read_b32 v55, a163
v_accvgpr_read_b32 v47, a167
v_accvgpr_read_b32 v60, a224
M v[76:79],a[108:111],v[128:131],0
T a[106:107],v30,59392
v_accvgpr_read_b32 v12, a228
v_accvgpr_read_b32 v61, a225
v_accvgpr_read_b32 v13, a229
v_accvgpr_read_b32 v62, a226
v_accvgpr_read_b32 v14, a230
M v[108:111],a[108:111],v[132:135],0
T a[108:109],v31,51200
s_waitcnt lgkmcnt(7)
v_accvgpr_read_b32 v63, a227
v_accvgpr_read_b32 v15, a231
v_mul_f32 v48, v48, v56
v_mul_f32 v40, v40, v8
M v[80:83],a[112:115],v[128:131],0
T a[110:111],v31,59392
v_mul_f32 v49, v49, v57
v_mul_f32 v41, v41, v9
v_mul_f32 v50, v50, v58
v_mul_f32 v42, v42, v10
v_mul_f32 v51, v51, v59
M v[112:115],a[112:115],v[132:135],0
T a[112:113],v28,52224
global_load_lds_dwordx4 v21, s[82:83] offset:3072
v_mul_f32 v43, v43, v11
v_mul_f32 v52, v52, v60
v_mul_f32 v44, v44, v12
v_mul_f32 v53, v53, v61
M v[84:87],a[116:119],v[128:131],0
T a[114:115],v28,60416
s_add_u32 s82, s82, 131072
s_addc_u32 s83, s83, 0
v_mul_f32 v45, v45, v13
v_mul_f32 v54, v54, v62
v_mul_f32 v46, v46, v14
M v[116:119],a[116:119],v[132:135],0
T a[116:117],v29,52224
v_mul_f32 v55, v55, v63
v_mul_f32 v47, v47, v15
v_cvt_pk_bf16_f32 v48, v48, v49
v_cvt_pk_bf16_f32 v40, v40, v41
v_cvt_pk_bf16_f32 v49, v50, v51
M v[88:91],a[120:123],v[128:131],0
T a[118:119],v29,60416
v_cvt_pk_bf16_f32 v41, v42, v43
v_cvt_pk_bf16_f32 v50, v52, v53
v_cvt_pk_bf16_f32 v42, v44, v45
v_cvt_pk_bf16_f32 v51, v54, v55
v_cvt_pk_bf16_f32 v43, v46, v47
M v[120:123],a[120:123],v[132:135],0
T a[120:121],v30,52224
global_load_dwordx4 v[236:239], v17, s[80:81] offset:1024 sc1
s_add_u32 m0, s99, 65536
global_store_dwordx4 v16, v[48:51], s[60:61] sc1
global_store_dwordx4 v16, v[40:43], s[70:71] sc1
s_add_u32 s60, s60, 294912
s_addc_u32 s61, s61, 0
M v[92:95],a[124:127],v[128:131],0
T a[122:123],v30,60416
s_add_u32 s80, s80, 2048
s_addc_u32 s81, s81, 0
s_add_u32 s70, s70, 294912
s_addc_u32 s71, s71, 0
v_accvgpr_read_b32 v48, a136
M v[124:127],a[124:127],v[132:135],0
T a[124:125],v31,52224
s_waitcnt vmcnt(42) lgkmcnt(7)
s_barrier
v_accvgpr_read_b32 v40, a140
v_accvgpr_read_b32 v49, a137
v_accvgpr_read_b32 v41, a141
M v[64:67],a[96:99],v[136:139],v[64:67]
T a[126:127],v31,60416
v_accvgpr_read_b32 v50, a138
v_accvgpr_read_b32 v42, a142
v_accvgpr_read_b32 v51, a139
v_accvgpr_read_b32 v43, a143
v_accvgpr_read_b32 v56, a200
M v[96:99],a[96:99],v[140:143],v[96:99]
T a[96:97],v24,0
v_accvgpr_read_b32 v8, a204
v_accvgpr_read_b32 v57, a201
v_accvgpr_read_b32 v9, a205
v_accvgpr_read_b32 v58, a202
v_accvgpr_read_b32 v10, a206
M v[68:71],a[100:103],v[136:139],v[68:71]
T a[98:99],v24,8192
global_load_lds_dwordx4 v18, s[82:83] offset:0
v_accvgpr_read_b32 v59, a203
v_accvgpr_read_b32 v11, a207
v_accvgpr_read_b32 v52, a168
v_accvgpr_read_b32 v44, a172
M v[100:103],a[100:103],v[140:143],v[100:103]
T a[100:101],v25,0
v_accvgpr_read_b32 v53, a169
v_accvgpr_read_b32 v45, a173
v_accvgpr_read_b32 v54, a170
v_accvgpr_read_b32 v46, a174
v_accvgpr_read_b32 v55, a171
M v[72:75],a[104:107],v[136:139],v[72:75]
T a[102:103],v25,8192
v_accvgpr_read_b32 v47, a175
v_accvgpr_read_b32 v60, a232
v_accvgpr_read_b32 v12, a236
v_accvgpr_read_b32 v61, a233
v_accvgpr_read_b32 v13, a237
M v[104:107],a[104:107],v[140:143],v[104:107]
T a[104:105],v26,0
v_accvgpr_read_b32 v62, a234
v_accvgpr_read_b32 v14, a238
v_accvgpr_read_b32 v63, a235
v_accvgpr_read_b32 v15, a239
v_mul_f32 v48, v48, v56
M v[76:79],a[108:111],v[136:139],v[76:79]
T a[106:107],v26,8192
global_load_dwordx4 v[240:243], v16, s[80:81] offset:0 sc1
v_mul_f32 v40, v40, v8
v_mul_f32 v49, v49, v57
v_mul_f32 v41, v41, v9
v_mul_f32 v50, v50, v58
M v[108:111],a[108:111],v[140:143],v[108:111]
T a[108:109],v27,0
s_waitcnt lgkmcnt(7)
v_mul_f32 v42, v42, v10
v_mul_f32 v51, v51, v59
v_mul_f32 v43, v43, v11
v_mul_f32 v52, v52, v60
M v[80:83],a[112:115],v[136:139],v[80:83]
T a[110:111],v27,8192
v_mul_f32 v44, v44, v12
v_mul_f32 v53, v53, v61
v_mul_f32 v45, v45, v13
v_mul_f32 v54, v54, v62
v_mul_f32 v46, v46, v14
M v[112:115],a[112:115],v[140:143],v[112:115]
T a[112:113],v24,1024
v_mul_f32 v55, v55, v63
v_mul_f32 v47, v47, v15
v_cvt_pk_bf16_f32 v48, v48, v49
v_cvt_pk_bf16_f32 v40, v40, v41
v_cvt_pk_bf16_f32 v49, v50, v51
M v[84:87],a[116:119],v[136:139],v[84:87]
T a[114:115],v24,9216
global_load_lds_dwordx4 v19, s[82:83] offset:1024
v_cvt_pk_bf16_f32 v41, v42, v43
v_cvt_pk_bf16_f32 v50, v52, v53
v_cvt_pk_bf16_f32 v42, v44, v45
v_cvt_pk_bf16_f32 v51, v54, v55
M v[116:119],a[116:119],v[140:143],v[116:119]
T a[116:117],v25,1024
v_cvt_pk_bf16_f32 v43, v46, v47
global_store_dwordx4 v16, v[48:51], s[60:61] sc1
global_store_dwordx4 v16, v[40:43], s[70:71] sc1
s_add_u32 s60, s60, 294912
s_addc_u32 s61, s61, 0
M v[88:91],a[120:123],v[136:139],v[88:91]
T a[118:119],v25,9216
s_add_u32 s70, s70, 294912
s_addc_u32 s71, s71, 0
v_accvgpr_read_b32 v48, a144
v_accvgpr_read_b32 v40, a148
v_accvgpr_read_b32 v49, a145
M v[120:123],a[120:123],v[140:143],v[120:123]
T a[120:121],v26,1024
v_accvgpr_read_b32 v41, a149
v_accvgpr_read_b32 v50, a146
v_accvgpr_read_b32 v42, a150
v_accvgpr_read_b32 v51, a147
v_accvgpr_read_b32 v43, a151
M v[92:95],a[124:127],v[136:139],v[92:95]
T a[122:123],v26,9216
global_load_dwordx4 v[244:247], v17, s[80:81] offset:0 sc1
v_accvgpr_read_b32 v56, a208
v_accvgpr_read_b32 v8, a212
v_accvgpr_read_b32 v57, a209
v_accvgpr_read_b32 v9, a213
M v[124:127],a[124:127],v[140:143],v[124:127]
T a[124:125],v27,1024
T a[126:127],v27,9216
s_waitcnt lgkmcnt(8)
v_accvgpr_read_b32 v58, a210
v_accvgpr_read_b32 v10, a214
v_accvgpr_read_b32 v59, a211
M v[64:67],a[96:99],v[144:147],v[64:67]
v_accvgpr_read_b32 v11, a215
v_accvgpr_read_b32 v52, a176
v_accvgpr_read_b32 v44, a180
v_accvgpr_read_b32 v53, a177
v_accvgpr_read_b32 v45, a181
v_accvgpr_read_b32 v54, a178
M v[96:99],a[96:99],v[148:151],v[96:99]
T a[96:97],v24,2048
global_load_lds_dwordx4 v20, s[82:83] offset:2048
v_accvgpr_read_b32 v46, a182
v_accvgpr_read_b32 v55, a179
v_accvgpr_read_b32 v47, a183
v_accvgpr_read_b32 v60, a240
M v[68:71],a[100:103],v[144:147],v[68:71]
T a[98:99],v24,10240
v_accvgpr_read_b32 v12, a244
v_accvgpr_read_b32 v61, a241
v_accvgpr_read_b32 v13, a245
v_accvgpr_read_b32 v62, a242
v_accvgpr_read_b32 v14, a246
M v[100:103],a[100:103],v[148:151],v[100:103]
T a[100:101],v25,2048
v_accvgpr_read_b32 v63, a243
v_accvgpr_read_b32 v15, a247
v_mul_f32 v48, v48, v56
v_mul_f32 v40, v40, v8
v_mul_f32 v49, v49, v57
M v[72:75],a[104:107],v[144:147],v[72:75]
T a[102:103],v25,10240
v_mul_f32 v41, v41, v9
v_mul_f32 v50, v50, v58
v_mul_f32 v42, v42, v10
v_mul_f32 v51, v51, v59
v_mul_f32 v43, v43, v11
M v[104:107],a[104:107],v[148:151],v[104:107]
T a[104:105],v26,2048
global_load_dwordx4 v[248:251], v16, s[80:81] offset:1024 sc1
v_mul_f32 v52, v52, v60
v_mul_f32 v44, v44, v12
v_mul_f32 v53, v53, v61
v_mul_f32 v45, v45, v13
M v[76:79],a[108:111],v[144:147],v[76:79]
T a[106:107],v26,10240
v_mul_f32 v54, v54, v62
v_mul_f32 v46, v46, v14
v_mul_f32 v55, v55, v63
v_mul_f32 v47, v47, v15
v_cvt_pk_bf16_f32 v48, v48, v49
M v[108:111],a[108:111],v[148:151],v[108:111]
T a[108:109],v27,2048
s_waitcnt lgkmcnt(7)
v_cvt_pk_bf16_f32 v40, v40, v41
v_cvt_pk_bf16_f32 v49, v50, v51
v_cvt_pk_bf16_f32 v41, v42, v43
v_cvt_pk_bf16_f32 v50, v52, v53
M v[80:83],a[112:115],v[144:147],v[80:83]
T a[110:111],v27,10240
v_cvt_pk_bf16_f32 v42, v44, v45
v_cvt_pk_bf16_f32 v51, v54, v55
v_cvt_pk_bf16_f32 v43, v46, v47
global_store_dwordx4 v16, v[48:51], s[60:61] sc1
global_store_dwordx4 v16, v[40:43], s[70:71] sc1
M v[112:115],a[112:115],v[148:151],v[112:115]
T a[112:113],v24,3072
global_load_lds_dwordx4 v21, s[82:83] offset:3072
s_add_u32 s60, s60, 294912
s_addc_u32 s61, s61, 0
s_add_u32 s70, s70, 294912
s_addc_u32 s71, s71, 0
M v[84:87],a[116:119],v[144:147],v[84:87]
T a[114:115],v24,11264
s_add_u32 s82, s82, 131072
s_addc_u32 s83, s83, 0
v_accvgpr_read_b32 v48, a152
v_accvgpr_read_b32 v40, a156
v_accvgpr_read_b32 v49, a153
M v[116:119],a[116:119],v[148:151],v[116:119]
T a[116:117],v25,3072
v_accvgpr_read_b32 v41, a157
v_accvgpr_read_b32 v50, a154
v_accvgpr_read_b32 v42, a158
v_accvgpr_read_b32 v51, a155
v_accvgpr_read_b32 v43, a159
M v[88:91],a[120:123],v[144:147],v[88:91]
T a[118:119],v25,11264
v_accvgpr_read_b32 v56, a216
v_accvgpr_read_b32 v8, a220
v_accvgpr_read_b32 v57, a217
v_accvgpr_read_b32 v9, a221
v_accvgpr_read_b32 v58, a218
M v[120:123],a[120:123],v[148:151],v[120:123]
T a[120:121],v26,3072
global_load_dwordx4 v[252:255], v17, s[80:81] offset:1024 sc1
s_add_u32 m0, s99, 114688
v_accvgpr_read_b32 v10, a222
v_accvgpr_read_b32 v59, a219
v_accvgpr_read_b32 v11, a223
M v[92:95],a[124:127],v[144:147],v[92:95]
T a[122:123],v26,11264
s_add_u32 s80, s80, 2048
s_addc_u32 s81, s81, 0
v_accvgpr_read_b32 v52, a184
v_accvgpr_read_b32 v44, a188
v_accvgpr_read_b32 v53, a185
M v[124:127],a[124:127],v[148:151],v[124:127]
T a[124:125],v27,3072
s_waitcnt vmcnt(46) lgkmcnt(7)
s_barrier
v_accvgpr_read_b32 v45, a189
v_accvgpr_read_b32 v54, a186
v_accvgpr_read_b32 v46, a190
M v[64:67],a[96:99],v[152:155],v[64:67]
T a[126:127],v27,11264
v_accvgpr_read_b32 v55, a187
v_accvgpr_read_b32 v47, a191
v_accvgpr_read_b32 v60, a248
v_accvgpr_read_b32 v12, a252
v_accvgpr_read_b32 v61, a249
M v[96:99],a[96:99],v[156:159],v[96:99]
T a[96:97],v24,16384
v_accvgpr_read_b32 v13, a253
v_accvgpr_read_b32 v62, a250
v_accvgpr_read_b32 v14, a254
v_accvgpr_read_b32 v63, a251
v_accvgpr_read_b32 v15, a255
M v[68:71],a[100:103],v[152:155],v[68:71]
T a[98:99],v24,24576
global_load_lds_dwordx4 v18, s[82:83] offset:0
v_mul_f32 v48, v48, v56
v_mul_f32 v40, v40, v8
v_mul_f32 v49, v49, v57
v_mul_f32 v41, v41, v9
M v[100:103],a[100:103],v[156:159],v[100:103]
T a[100:101],v25,16384
v_mul_f32 v50, v50, v58
v_mul_f32 v42, v42, v10
v_mul_f32 v51, v51, v59
v_mul_f32 v43, v43, v11
v_mul_f32 v52, v52, v60
M v[72:75],a[104:107],v[152:155],v[72:75]
T a[102:103],v25,24576
v_mul_f32 v44, v44, v12
v_mul_f32 v53, v53, v61
v_mul_f32 v45, v45, v13
v_mul_f32 v54, v54, v62
v_mul_f32 v46, v46, v14
M v[104:107],a[104:107],v[156:159],v[104:107]
T a[104:105],v26,16384
v_mul_f32 v55, v55, v63
v_mul_f32 v47, v47, v15
v_cvt_pk_bf16_f32 v48, v48, v49
v_cvt_pk_bf16_f32 v40, v40, v41
v_cvt_pk_bf16_f32 v49, v50, v51
M v[76:79],a[108:111],v[152:155],v[76:79]
T a[106:107],v26,24576
global_load_dwordx4 v[128:131], v16, s[80:81] offset:0 sc1
v_cvt_pk_bf16_f32 v41, v42, v43
v_cvt_pk_bf16_f32 v50, v52, v53
v_cvt_pk_bf16_f32 v42, v44, v45
v_cvt_pk_bf16_f32 v51, v54, v55
M v[108:111],a[108:111],v[156:159],v[108:111]
T a[108:109],v27,16384
s_waitcnt lgkmcnt(7)
v_cvt_pk_bf16_f32 v43, v46, v47
global_store_dwordx4 v16, v[48:51], s[60:61] sc1
global_store_dwordx4 v16, v[40:43], s[70:71] sc1
M v[80:83],a[112:115],v[152:155],v[80:83]
T a[110:111],v27,24576
M v[112:115],a[112:115],v[156:159],v[112:115]
T a[112:113],v24,17408
M v[84:87],a[116:119],v[152:155],v[84:87]
T a[114:115],v24,25600
global_load_lds_dwordx4 v19, s[82:83] offset:1024
M v[116:119],a[116:119],v[156:159],v[116:119]
T a[116:117],v25,17408
M v[88:91],a[120:123],v[152:155],v[88:91]
T a[118:119],v25,25600
M v[120:123],a[120:123],v[156:159],v[120:123]
T a[120:121],v26,17408
M v[92:95],a[124:127],v[152:155],v[92:95]
T a[122:123],v26,25600
global_load_dwordx4 v[132:135], v17, s[80:81] offset:0 sc1
M v[124:127],a[124:127],v[156:159],v[124:127]
T a[124:125],v27,17408
T a[126:127],v27,25600
s_waitcnt lgkmcnt(8)
M v[64:67],a[96:99],v[160:163],v[64:67]
M v[96:99],a[96:99],v[164:167],v[96:99]
T a[96:97],v24,18432
global_load_lds_dwordx4 v20, s[82:83] offset:2048
M v[68:71],a[100:103],v[160:163],v[68:71]
T a[98:99],v24,26624
M v[100:103],a[100:103],v[164:167],v[100:103]
T a[100:101],v25,18432
M v[72:75],a[104:107],v[160:163],v[72:75]
T a[102:103],v25,26624
M v[104:107],a[104:107],v[164:167],v[104:107]
T a[104:105],v26,18432
global_load_dwordx4 v[136:139], v16, s[80:81] offset:1024 sc1
M v[76:79],a[108:111],v[160:163],v[76:79]
T a[106:107],v26,26624
M v[108:111],a[108:111],v[164:167],v[108:111]
T a[108:109],v27,18432
s_waitcnt lgkmcnt(7)
M v[80:83],a[112:115],v[160:163],v[80:83]
T a[110:111],v27,26624
M v[112:115],a[112:115],v[164:167],v[112:115]
T a[112:113],v24,19456
global_load_lds_dwordx4 v21, s[82:83] offset:3072
M v[84:87],a[116:119],v[160:163],v[84:87]
T a[114:115],v24,27648
s_add_u32 s82, s82, 131072
s_addc_u32 s83, s83, 0
M v[116:119],a[116:119],v[164:167],v[116:119]
T a[116:117],v25,19456
M v[88:91],a[120:123],v[160:163],v[88:91]
T a[118:119],v25,27648
M v[120:123],a[120:123],v[164:167],v[120:123]
T a[120:121],v26,19456
global_load_dwordx4 v[140:143], v17, s[80:81] offset:1024 sc1
s_add_u32 m0, s99, 0
M v[92:95],a[124:127],v[160:163],v[92:95]
T a[122:123],v26,27648
s_add_u32 s80, s80, 2048
s_addc_u32 s81, s81, 0
M v[124:127],a[124:127],v[164:167],v[124:127]
T a[124:125],v27,19456
s_waitcnt vmcnt(48) lgkmcnt(7)
s_barrier
M v[64:67],a[96:99],v[168:171],v[64:67]
T a[126:127],v27,27648
M v[96:99],a[96:99],v[172:175],v[96:99]
T a[96:97],v28,16384
M v[68:71],a[100:103],v[168:171],v[68:71]
T a[98:99],v28,24576
global_load_lds_dwordx4 v18, s[82:83] offset:0
M v[100:103],a[100:103],v[172:175],v[100:103]
T a[100:101],v29,16384
M v[72:75],a[104:107],v[168:171],v[72:75]
T a[102:103],v29,24576
M v[104:107],a[104:107],v[172:175],v[104:107]
T a[104:105],v30,16384
M v[76:79],a[108:111],v[168:171],v[76:79]
T a[106:107],v30,24576
global_load_dwordx4 v[144:147], v16, s[80:81] offset:0 sc1
M v[108:111],a[108:111],v[172:175],v[108:111]
T a[108:109],v31,16384
s_waitcnt lgkmcnt(7)
M v[80:83],a[112:115],v[168:171],v[80:83]
T a[110:111],v31,24576
M v[112:115],a[112:115],v[172:175],v[112:115]
T a[112:113],v28,17408
M v[84:87],a[116:119],v[168:171],v[84:87]
T a[114:115],v28,25600
global_load_lds_dwordx4 v19, s[82:83] offset:1024
M v[116:119],a[116:119],v[172:175],v[116:119]
T a[116:117],v29,17408
M v[88:91],a[120:123],v[168:171],v[88:91]
T a[118:119],v29,25600
M v[120:123],a[120:123],v[172:175],v[120:123]
T a[120:121],v30,17408
M v[92:95],a[124:127],v[168:171],v[92:95]
T a[122:123],v30,25600
global_load_dwordx4 v[148:151], v17, s[80:81] offset:0 sc1
M v[124:127],a[124:127],v[172:175],v[124:127]
T a[124:125],v31,17408
T a[126:127],v31,25600
s_waitcnt lgkmcnt(8)
M v[64:67],a[96:99],v[176:179],v[64:67]
M v[96:99],a[96:99],v[180:183],v[96:99]
T a[96:97],v28,18432
global_load_lds_dwordx4 v20, s[82:83] offset:2048
M v[68:71],a[100:103],v[176:179],v[68:71]
T a[98:99],v28,26624
M v[100:103],a[100:103],v[180:183],v[100:103]
T a[100:101],v29,18432
M v[72:75],a[104:107],v[176:179],v[72:75]
T a[102:103],v29,26624
M v[104:107],a[104:107],v[180:183],v[104:107]
T a[104:105],v30,18432
global_load_dwordx4 v[152:155], v16, s[80:81] offset:1024 sc1
M v[76:79],a[108:111],v[176:179],v[76:79]
T a[106:107],v30,26624
M v[108:111],a[108:111],v[180:183],v[108:111]
T a[108:109],v31,18432
s_waitcnt lgkmcnt(7)
M v[80:83],a[112:115],v[176:179],v[80:83]
T a[110:111],v31,26624
M v[112:115],a[112:115],v[180:183],v[112:115]
T a[112:113],v28,19456
global_load_lds_dwordx4 v21, s[82:83] offset:3072
M v[84:87],a[116:119],v[176:179],v[84:87]
T a[114:115],v28,27648
s_add_u32 s82, s82, 131072
s_addc_u32 s83, s83, 0
M v[116:119],a[116:119],v[180:183],v[116:119]
T a[116:117],v29,19456
M v[88:91],a[120:123],v[176:179],v[88:91]
T a[118:119],v29,27648
M v[120:123],a[120:123],v[180:183],v[120:123]
T a[120:121],v30,19456
global_load_dwordx4 v[156:159], v17, s[80:81] offset:1024 sc1
s_add_u32 m0, s99, 16384
M v[92:95],a[124:127],v[176:179],v[92:95]
T a[122:123],v30,27648
s_add_u32 s80, s80, 2048
s_addc_u32 s81, s81, 0
M v[124:127],a[124:127],v[180:183],v[124:127]
T a[124:125],v31,19456
s_waitcnt vmcnt(48) lgkmcnt(7)
s_barrier
M v[64:67],a[96:99],v[184:187],v[64:67]
T a[126:127],v31,27648
M v[96:99],a[96:99],v[188:191],v[96:99]
T a[96:97],v24,32768
M v[68:71],a[100:103],v[184:187],v[68:71]
T a[98:99],v24,40960
global_load_lds_dwordx4 v18, s[82:83] offset:0
M v[100:103],a[100:103],v[188:191],v[100:103]
T a[100:101],v25,32768
M v[72:75],a[104:107],v[184:187],v[72:75]
T a[102:103],v25,40960
M v[104:107],a[104:107],v[188:191],v[104:107]
T a[104:105],v26,32768
M v[76:79],a[108:111],v[184:187],v[76:79]
T a[106:107],v26,40960
global_load_dwordx4 v[160:163], v16, s[80:81] offset:0 sc1
M v[108:111],a[108:111],v[188:191],v[108:111]
T a[108:109],v27,32768
s_waitcnt lgkmcnt(7)
M v[80:83],a[112:115],v[184:187],v[80:83]
T a[110:111],v27,40960
M v[112:115],a[112:115],v[188:191],v[112:115]
T a[112:113],v24,33792
M v[84:87],a[116:119],v[184:187],v[84:87]
T a[114:115],v24,41984
global_load_lds_dwordx4 v19, s[82:83] offset:1024
M v[116:119],a[116:119],v[188:191],v[116:119]
T a[116:117],v25,33792
M v[88:91],a[120:123],v[184:187],v[88:91]
T a[118:119],v25,41984
M v[120:123],a[120:123],v[188:191],v[120:123]
T a[120:121],v26,33792
M v[92:95],a[124:127],v[184:187],v[92:95]
T a[122:123],v26,41984
global_load_dwordx4 v[164:167], v17, s[80:81] offset:0 sc1
M v[124:127],a[124:127],v[188:191],v[124:127]
T a[124:125],v27,33792
T a[126:127],v27,41984
s_waitcnt lgkmcnt(8)
M v[64:67],a[96:99],v[192:195],v[64:67]
M v[96:99],a[96:99],v[196:199],v[96:99]
T a[96:97],v24,34816
global_load_lds_dwordx4 v20, s[82:83] offset:2048
M v[68:71],a[100:103],v[192:195],v[68:71]
T a[98:99],v24,43008
M v[100:103],a[100:103],v[196:199],v[100:103]
T a[100:101],v25,34816
M v[72:75],a[104:107],v[192:195],v[72:75]
T a[102:103],v25,43008
M v[104:107],a[104:107],v[196:199],v[104:107]
T a[104:105],v26,34816
global_load_dwordx4 v[168:171], v16, s[80:81] offset:1024 sc1
M v[76:79],a[108:111],v[192:195],v[76:79]
T a[106:107],v26,43008
M v[108:111],a[108:111],v[196:199],v[108:111]
T a[108:109],v27,34816
s_waitcnt lgkmcnt(7)
M v[80:83],a[112:115],v[192:195],v[80:83]
T a[110:111],v27,43008
M v[112:115],a[112:115],v[196:199],v[112:115]
T a[112:113],v24,35840
global_load_lds_dwordx4 v21, s[82:83] offset:3072
M v[84:87],a[116:119],v[192:195],v[84:87]
T a[114:115],v24,44032
s_add_u32 s82, s82, 131072
s_addc_u32 s83, s83, 0
M v[116:119],a[116:119],v[196:199],v[116:119]
T a[116:117],v25,35840
M v[88:91],a[120:123],v[192:195],v[88:91]
T a[118:119],v25,44032
M v[120:123],a[120:123],v[196:199],v[120:123]
T a[120:121],v26,35840
global_load_dwordx4 v[172:175], v17, s[80:81] offset:1024 sc1
s_add_u32 m0, s99, 81920
M v[92:95],a[124:127],v[192:195],v[92:95]
T a[122:123],v26,44032
s_add_u32 s80, s80, 2048
s_addc_u32 s81, s81, 0
M v[124:127],a[124:127],v[196:199],v[124:127]
T a[124:125],v27,35840
s_waitcnt vmcnt(48) lgkmcnt(7)
s_barrier
M v[64:67],a[96:99],v[200:203],v[64:67]
T a[126:127],v27,44032
M v[96:99],a[96:99],v[204:207],v[96:99]
T a[96:97],v24,49152
M v[68:71],a[100:103],v[200:203],v[68:71]
T a[98:99],v24,57344
global_load_lds_dwordx4 v18, s[82:83] offset:0
M v[100:103],a[100:103],v[204:207],v[100:103]
T a[100:101],v25,49152
M v[72:75],a[104:107],v[200:203],v[72:75]
T a[102:103],v25,57344
M v[104:107],a[104:107],v[204:207],v[104:107]
T a[104:105],v26,49152
M v[76:79],a[108:111],v[200:203],v[76:79]
T a[106:107],v26,57344
global_load_dwordx4 v[176:179], v16, s[80:81] offset:0 sc1
M v[108:111],a[108:111],v[204:207],v[108:111]
T a[108:109],v27,49152
s_waitcnt lgkmcnt(7)
M v[80:83],a[112:115],v[200:203],v[80:83]
T a[110:111],v27,57344
M v[112:115],a[112:115],v[204:207],v[112:115]
T a[112:113],v24,50176
M v[84:87],a[116:119],v[200:203],v[84:87]
T a[114:115],v24,58368
global_load_lds_dwordx4 v19, s[82:83] offset:1024
M v[116:119],a[116:119],v[204:207],v[116:119]
T a[116:117],v25,50176
M v[88:91],a[120:123],v[200:203],v[88:91]
T a[118:119],v25,58368
M v[120:123],a[120:123],v[204:207],v[120:123]
T a[120:121],v26,50176
M v[92:95],a[124:127],v[200:203],v[92:95]
T a[122:123],v26,58368
global_load_dwordx4 v[180:183], v17, s[80:81] offset:0 sc1
M v[124:127],a[124:127],v[204:207],v[124:127]
T a[124:125],v27,50176
T a[126:127],v27,58368
s_waitcnt lgkmcnt(8)
M v[64:67],a[96:99],v[208:211],v[64:67]
M v[96:99],a[96:99],v[212:215],v[96:99]
T a[96:97],v24,51200
global_load_lds_dwordx4 v20, s[82:83] offset:2048
M v[68:71],a[100:103],v[208:211],v[68:71]
T a[98:99],v24,59392
M v[100:103],a[100:103],v[212:215],v[100:103]
T a[100:101],v25,51200
M v[72:75],a[104:107],v[208:211],v[72:75]
T a[102:103],v25,59392
M v[104:107],a[104:107],v[212:215],v[104:107]
T a[104:105],v26,51200
global_load_dwordx4 v[184:187], v16, s[80:81] offset:1024 sc1
M v[76:79],a[108:111],v[208:211],v[76:79]
T a[106:107],v26,59392
M v[108:111],a[108:111],v[212:215],v[108:111]
T a[108:109],v27,51200
s_waitcnt lgkmcnt(7)
M v[80:83],a[112:115],v[208:211],v[80:83]
T a[110:111],v27,59392
M v[112:115],a[112:115],v[212:215],v[112:115]
T a[112:113],v24,52224
global_load_lds_dwordx4 v21, s[82:83] offset:3072
M v[84:87],a[116:119],v[208:211],v[84:87]
T a[114:115],v24,60416
s_add_u32 s82, s82, 131072
s_addc_u32 s83, s83, 0
M v[116:119],a[116:119],v[212:215],v[116:119]
T a[116:117],v25,52224
M v[88:91],a[120:123],v[208:211],v[88:91]
T a[118:119],v25,60416
M v[120:123],a[120:123],v[212:215],v[120:123]
T a[120:121],v26,52224
global_load_dwordx4 v[188:191], v17, s[80:81] offset:1024 sc1
s_add_u32 m0, s99, 32768
M v[92:95],a[124:127],v[208:211],v[92:95]
T a[122:123],v26,60416
s_add_u32 s80, s80, 2048
s_addc_u32 s81, s81, 0
M v[124:127],a[124:127],v[212:215],v[124:127]
T a[124:125],v27,52224
s_waitcnt vmcnt(30) lgkmcnt(7)
s_barrier
s_cmp_lg_u32 s99, 0
s_cbranch_scc1 Lnf2Q_%=
s_mov_b64 exec, 1
v_mov_b32 v37, s98
global_store_dword v36, v37, s[96:97] offset:1024 sc1
s_mov_b64 exec, -1
Lnf2Q_%=:
s_nop 0
M v[64:67],a[96:99],v[216:219],v[64:67]
T a[126:127],v27,60416
M v[96:99],a[96:99],v[220:223],v[96:99]
T a[96:97],v28,32768
M v[68:71],a[100:103],v[216:219],v[68:71]
T a[98:99],v28,40960
global_load_lds_dwordx4 v18, s[82:83] offset:0
M v[100:103],a[100:103],v[220:223],v[100:103]
T a[100:101],v29,32768
M v[72:75],a[104:107],v[216:219],v[72:75]
T a[102:103],v29,40960
M v[104:107],a[104:107],v[220:223],v[104:107]
T a[104:105],v30,32768
M v[76:79],a[108:111],v[216:219],v[76:79]
T a[106:107],v30,40960
global_load_dwordx4 v[192:195], v16, s[80:81] offset:0 sc1
M v[108:111],a[108:111],v[220:223],v[108:111]
T a[108:109],v31,32768
s_waitcnt lgkmcnt(7)
M v[80:83],a[112:115],v[216:219],v[80:83]
T a[110:111],v31,40960
M v[112:115],a[112:115],v[220:223],v[112:115]
T a[112:113],v28,33792
M v[84:87],a[116:119],v[216:219],v[84:87]
T a[114:115],v28,41984
global_load_lds_dwordx4 v19, s[82:83] offset:1024
M v[116:119],a[116:119],v[220:223],v[116:119]
T a[116:117],v29,33792
M v[88:91],a[120:123],v[216:219],v[88:91]
T a[118:119],v29,41984
M v[120:123],a[120:123],v[220:223],v[120:123]
T a[120:121],v30,33792
M v[92:95],a[124:127],v[216:219],v[92:95]
T a[122:123],v30,41984
global_load_dwordx4 v[196:199], v17, s[80:81] offset:0 sc1
M v[124:127],a[124:127],v[220:223],v[124:127]
T a[124:125],v31,33792
T a[126:127],v31,41984
s_waitcnt lgkmcnt(8)
M v[64:67],a[96:99],v[224:227],v[64:67]
M v[96:99],a[96:99],v[228:231],v[96:99]
T a[96:97],v28,34816
global_load_lds_dwordx4 v20, s[82:83] offset:2048
M v[68:71],a[100:103],v[224:227],v[68:71]
T a[98:99],v28,43008
M v[100:103],a[100:103],v[228:231],v[100:103]
T a[100:101],v29,34816
M v[72:75],a[104:107],v[224:227],v[72:75]
T a[102:103],v29,43008
M v[104:107],a[104:107],v[228:231],v[104:107]
T a[104:105],v30,34816
global_load_dwordx4 v[200:203], v16, s[80:81] offset:1024 sc1
M v[76:79],a[108:111],v[224:227],v[76:79]
T a[106:107],v30,43008
M v[108:111],a[108:111],v[228:231],v[108:111]
T a[108:109],v31,34816
s_waitcnt lgkmcnt(7)
M v[80:83],a[112:115],v[224:227],v[80:83]
T a[110:111],v31,43008
M v[112:115],a[112:115],v[228:231],v[112:115]
T a[112:113],v28,35840
global_load_lds_dwordx4 v21, s[82:83] offset:3072
M v[84:87],a[116:119],v[224:227],v[84:87]
T a[114:115],v28,44032
s_add_u32 s82, s82, 131072
s_addc_u32 s83, s83, 0
M v[116:119],a[116:119],v[228:231],v[116:119]
T a[116:117],v29,35840
M v[88:91],a[120:123],v[224:227],v[88:91]
T a[118:119],v29,44032
M v[120:123],a[120:123],v[228:231],v[120:123]
T a[120:121],v30,35840
global_load_dwordx4 v[204:207], v17, s[80:81] offset:1024 sc1
s_add_u32 m0, s99, 49152
M v[92:95],a[124:127],v[224:227],v[92:95]
T a[122:123],v30,44032
s_add_u32 s80, s80, 2048
s_addc_u32 s81, s81, 0
M v[124:127],a[124:127],v[228:231],v[124:127]
T a[124:125],v31,35840
s_waitcnt vmcnt(42) lgkmcnt(7)
s_barrier
M v[64:67],a[96:99],v[232:235],v[64:67]
T a[126:127],v31,44032
M v[96:99],a[96:99],v[236:239],v[96:99]
T a[96:97],v28,0
M v[68:71],a[100:103],v[232:235],v[68:71]
T a[98:99],v28,8192
global_load_lds_dwordx4 v18, s[82:83] offset:0
M v[100:103],a[100:103],v[236:239],v[100:103]
T a[100:101],v29,0
M v[72:75],a[104:107],v[232:235],v[72:75]
T a[102:103],v29,8192
M v[104:107],a[104:107],v[236:239],v[104:107]
T a[104:105],v30,0
M v[76:79],a[108:111],v[232:235],v[76:79]
T a[106:107],v30,8192
global_load_dwordx4 v[208:211], v16, s[80:81] offset:0 sc1
M v[108:111],a[108:111],v[236:239],v[108:111]
T a[108:109],v31,0
s_waitcnt lgkmcnt(7)
M v[80:83],a[112:115],v[232:235],v[80:83]
T a[110:111],v31,8192
M v[112:115],a[112:115],v[236:239],v[112:115]
T a[112:113],v28,1024
M v[84:87],a[116:119],v[232:235],v[84:87]
T a[114:115],v28,9216
global_load_lds_dwordx4 v19, s[82:83] offset:1024
M v[116:119],a[116:119],v[236:239],v[116:119]
T a[116:117],v29,1024
M v[88:91],a[120:123],v[232:235],v[88:91]
T a[118:119],v29,9216
M v[120:123],a[120:123],v[236:239],v[120:123]
T a[120:121],v30,1024
M v[92:95],a[124:127],v[232:235],v[92:95]
T a[122:123],v30,9216
global_load_dwordx4 v[212:215], v17, s[80:81] offset:0 sc1
M v[124:127],a[124:127],v[236:239],v[124:127]
T a[124:125],v31,1024
T a[126:127],v31,9216
s_waitcnt lgkmcnt(8)
M v[64:67],a[96:99],v[240:243],v[64:67]
M v[96:99],a[96:99],v[244:247],v[96:99]
T a[96:97],v28,2048
global_load_lds_dwordx4 v20, s[82:83] offset:2048
M v[68:71],a[100:103],v[240:243],v[68:71]
T a[98:99],v28,10240
M v[100:103],a[100:103],v[244:247],v[100:103]
T a[100:101],v29,2048
M v[72:75],a[104:107],v[240:243],v[72:75]
T a[102:103],v29,10240
M v[104:107],a[104:107],v[244:247],v[104:107]
T a[104:105],v30,2048
global_load_dwordx4 v[216:219], v16, s[80:81] offset:1024 sc1
M v[76:79],a[108:111],v[240:243],v[76:79]
T a[106:107],v30,10240
M v[108:111],a[108:111],v[244:247],v[108:111]
T a[108:109],v31,2048
s_waitcnt lgkmcnt(7)
M v[80:83],a[112:115],v[240:243],v[80:83]
T a[110:111],v31,10240
M v[112:115],a[112:115],v[244:247],v[112:115]
T a[112:113],v28,3072
global_load_lds_dwordx4 v21, s[82:83] offset:3072
M v[84:87],a[116:119],v[240:243],v[84:87]
T a[114:115],v28,11264
s_add_u32 s82, s82, 131072
s_addc_u32 s83, s83, 0
M v[116:119],a[116:119],v[244:247],v[116:119]
T a[116:117],v29,3072
M v[88:91],a[120:123],v[240:243],v[88:91]
T a[118:119],v29,11264
M v[120:123],a[120:123],v[244:247],v[120:123]
T a[120:121],v30,3072
global_load_dwordx4 v[220:223], v17, s[80:81] offset:1024 sc1
s_add_u32 m0, s99, 98304
M v[92:95],a[124:127],v[240:243],v[92:95]
T a[122:123],v30,11264
s_add_u32 s80, s80, 2048
s_addc_u32 s81, s81, 0
M v[124:127],a[124:127],v[244:247],v[124:127]
T a[124:125],v31,3072
s_waitcnt vmcnt(40) lgkmcnt(7)
s_barrier
M v[64:67],a[96:99],v[248:251],v[64:67]
T a[126:127],v31,11264
global_load_dword v35, v34, s[96:97] offset:1024 sc1
M v[96:99],a[96:99],v[252:255],v[96:99]
T a[96:97],v28,49152
M v[68:71],a[100:103],v[248:251],v[68:71]
T a[98:99],v28,57344
global_load_lds_dwordx4 v18, s[82:83] offset:0
M v[100:103],a[100:103],v[252:255],v[100:103]
T a[100:101],v29,49152
M v[72:75],a[104:107],v[248:251],v[72:75]
T a[102:103],v29,57344
M v[104:107],a[104:107],v[252:255],v[104:107]
T a[104:105],v30,49152
M v[76:79],a[108:111],v[248:251],v[76:79]
T a[106:107],v30,57344
global_load_dwordx4 v[224:227], v16, s[80:81] offset:0 sc1
M v[108:111],a[108:111],v[252:255],v[108:111]
T a[108:109],v31,49152
s_waitcnt lgkmcnt(7)
M v[80:83],a[112:115],v[248:251],v[80:83]
T a[110:111],v31,57344
M v[112:115],a[112:115],v[252:255],v[112:115]
T a[112:113],v28,50176
M v[84:87],a[116:119],v[248:251],v[84:87]
T a[114:115],v28,58368
global_load_lds_dwordx4 v19, s[82:83] offset:1024
M v[116:119],a[116:119],v[252:255],v[116:119]
T a[116:117],v29,50176
M v[88:91],a[120:123],v[248:251],v[88:91]
T a[118:119],v29,58368
M v[120:123],a[120:123],v[252:255],v[120:123]
T a[120:121],v30,50176
M v[92:95],a[124:127],v[248:251],v[92:95]
T a[122:123],v30,58368
global_load_dwordx4 v[228:231], v17, s[80:81] offset:0 sc1
M v[124:127],a[124:127],v[252:255],v[124:127]
T a[124:125],v31,50176
T a[126:127],v31,58368
s_mov_b32 s64, 3
Lg2Q_%=:
s_waitcnt lgkmcnt(8)
M v[64:67],a[96:99],v[128:131],v[64:67]
M v[96:99],a[96:99],v[132:135],v[96:99]
T a[96:97],v28,51200
global_load_lds_dwordx4 v20, s[82:83] offset:2048
M v[68:71],a[100:103],v[128:131],v[68:71]
T a[98:99],v28,59392
M v[100:103],a[100:103],v[132:135],v[100:103]
T a[100:101],v29,51200
M v[72:75],a[104:107],v[128:131],v[72:75]
T a[102:103],v29,59392
M v[104:107],a[104:107],v[132:135],v[104:107]
T a[104:105],v30,51200
global_load_dwordx4 v[232:235], v16, s[80:81] offset:1024 sc1
M v[76:79],a[108:111],v[128:131],v[76:79]
T a[106:107],v30,59392
M v[108:111],a[108:111],v[132:135],v[108:111]
T a[108:109],v31,51200
s_waitcnt lgkmcnt(7)
M v[80:83],a[112:115],v[128:131],v[80:83]
T a[110:111],v31,59392
M v[112:115],a[112:115],v[132:135],v[112:115]
T a[112:113],v28,52224
global_load_lds_dwordx4 v21, s[82:83] offset:3072
M v[84:87],a[116:119],v[128:131],v[84:87]
T a[114:115],v28,60416
s_add_u32 s82, s82, 131072
s_addc_u32 s83, s83, 0
M v[116:119],a[116:119],v[132:135],v[116:119]
T a[116:117],v29,52224
M v[88:91],a[120:123],v[128:131],v[88:91]
T a[118:119],v29,60416
M v[120:123],a[120:123],v[132:135],v[120:123]
T a[120:121],v30,52224
global_load_dwordx4 v[236:239], v17, s[80:81] offset:1024 sc1
s_add_u32 m0, s99, 65536
M v[92:95],a[124:127],v[128:131],v[92:95]
T a[122:123],v30,60416
s_add_u32 s80, s80, 2048
s_addc_u32 s81, s81, 0
M v[124:127],a[124:127],v[132:135],v[124:127]
T a[124:125],v31,52224
s_waitcnt vmcnt(41) lgkmcnt(7)
s_barrier
M v[64:67],a[96:99],v[136:139],v[64:67]
T a[126:127],v31,60416
M v[96:99],a[96:99],v[140:143],v[96:99]
T a[96:97],v24,0
M v[68:71],a[100:103],v[136:139],v[68:71]
T a[98:99],v24,8192
global_load_lds_dwordx4 v18, s[82:83] offset:0
M v[100:103],a[100:103],v[140:143],v[100:103]
T a[100:101],v25,0
M v[72:75],a[104:107],v[136:139],v[72:75]
T a[102:103],v25,8192
M v[104:107],a[104:107],v[140:143],v[104:107]
T a[104:105],v26,0
M v[76:79],a[108:111],v[136:139],v[76:79]
T a[106:107],v26,8192
global_load_dwordx4 v[240:243], v16, s[80:81] offset:0 sc1
M v[108:111],a[108:111],v[140:143],v[108:111]
T a[108:109],v27,0
s_waitcnt lgkmcnt(7)
M v[80:83],a[112:115],v[136:139],v[80:83]
T a[110:111],v27,8192
M v[112:115],a[112:115],v[140:143],v[112:115]
T a[112:113],v24,1024
M v[84:87],a[116:119],v[136:139],v[84:87]
T a[114:115],v24,9216
global_load_lds_dwordx4 v19, s[82:83] offset:1024
M v[116:119],a[116:119],v[140:143],v[116:119]
T a[116:117],v25,1024
M v[88:91],a[120:123],v[136:139],v[88:91]
T a[118:119],v25,9216
M v[120:123],a[120:123],v[140:143],v[120:123]
T a[120:121],v26,1024
M v[92:95],a[124:127],v[136:139],v[92:95]
T a[122:123],v26,9216
global_load_dwordx4 v[244:247], v17, s[80:81] offset:0 sc1
M v[124:127],a[124:127],v[140:143],v[124:127]
T a[124:125],v27,1024
T a[126:127],v27,9216
s_waitcnt lgkmcnt(8)
M v[64:67],a[96:99],v[144:147],v[64:67]
M v[96:99],a[96:99],v[148:151],v[96:99]
T a[96:97],v24,2048
global_load_lds_dwordx4 v20, s[82:83] offset:2048
M v[68:71],a[100:103],v[144:147],v[68:71]
T a[98:99],v24,10240
M v[100:103],a[100:103],v[148:151],v[100:103]
T a[100:101],v25,2048
M v[72:75],a[104:107],v[144:147],v[72:75]
T a[102:103],v25,10240
M v[104:107],a[104:107],v[148:151],v[104:107]
T a[104:105],v26,2048
global_load_dwordx4 v[248:251], v16, s[80:81] offset:1024 sc1
M v[76:79],a[108:111],v[144:147],v[76:79]
T a[106:107],v26,10240
M v[108:111],a[108:111],v[148:151],v[108:111]
T a[108:109],v27,2048
s_waitcnt lgkmcnt(7)
M v[80:83],a[112:115],v[144:147],v[80:83]
T a[110:111],v27,10240
M v[112:115],a[112:115],v[148:151],v[112:115]
T a[112:113],v24,3072
global_load_lds_dwordx4 v21, s[82:83] offset:3072
M v[84:87],a[116:119],v[144:147],v[84:87]
T a[114:115],v24,11264
s_add_u32 s82, s82, 131072
s_addc_u32 s83, s83, 0
M v[116:119],a[116:119],v[148:151],v[116:119]
T a[116:117],v25,3072
s_cmp_eq_u32 s64, 3
s_cselect_b64 s[82:83], s[84:85], s[82:83]
M v[88:91],a[120:123],v[144:147],v[88:91]
T a[118:119],v25,11264
s_cmp_eq_u32 s64, 2
s_cselect_b64 s[82:83], s[88:89], s[82:83]
M v[120:123],a[120:123],v[148:151],v[120:123]
T a[120:121],v26,3072
global_load_dwordx4 v[252:255], v17, s[80:81] offset:1024 sc1
s_add_u32 m0, s99, 114688
M v[92:95],a[124:127],v[144:147],v[92:95]
T a[122:123],v26,11264
s_add_u32 s80, s80, 2048
s_addc_u32 s81, s81, 0
M v[124:127],a[124:127],v[148:151],v[124:127]
T a[124:125],v27,3072
s_waitcnt vmcnt(41) lgkmcnt(7)
s_barrier
s_waitcnt vmcnt(16)
v_cmp_ne_u32 vcc, s98, v35
s_nop 3
s_and_b32 vcc_lo, vcc_lo, s66
s_cbranch_scc0 LoklpQ_%=
s_mov_b32 s65, 0x100000
LsplpQ_%=:
s_sleep 1
global_load_dword v35, v34, s[96:97] offset:1024 sc1
s_waitcnt vmcnt(0)
v_cmp_ne_u32 vcc, s98, v35
s_nop 3
s_and_b32 vcc_lo, vcc_lo, s66
s_cbranch_scc0 LoklpQ_%=
s_sub_u32 s65, s65, 1
s_cmp_lg_u32 s65, 0
s_cbranch_scc1 LsplpQ_%=
LoklpQ_%=:
M v[64:67],a[96:99],v[152:155],v[64:67]
T a[126:127],v27,11264
s_cmp_eq_u32 s64, 3
s_cselect_b64 s[80:81], s[86:87], s[80:81]
M v[96:99],a[96:99],v[156:159],v[96:99]
T a[96:97],v24,16384
s_cmp_eq_u32 s64, 2
s_cselect_b64 s[80:81], s[90:91], s[80:81]
M v[68:71],a[100:103],v[152:155],v[68:71]
T a[98:99],v24,24576
global_load_lds_dwordx4 v18, s[82:83] offset:0
M v[100:103],a[100:103],v[156:159],v[100:103]
T a[100:101],v25,16384
M v[72:75],a[104:107],v[152:155],v[72:75]
T a[102:103],v25,24576
s_lshr_b64 s[66:67], s[66:67], 32
M v[104:107],a[104:107],v[156:159],v[104:107]
T a[104:105],v26,16384
M v[76:79],a[108:111],v[152:155],v[76:79]
T a[106:107],v26,24576
global_load_dwordx4 v[128:131], v16, s[80:81] offset:0 sc1
M v[108:111],a[108:111],v[156:159],v[108:111]
T a[108:109],v27,16384
s_waitcnt lgkmcnt(7)
M v[80:83],a[112:115],v[152:155],v[80:83]
T a[110:111],v27,24576
M v[112:115],a[112:115],v[156:159],v[112:115]
T a[112:113],v24,17408
M v[84:87],a[116:119],v[152:155],v[84:87]
T a[114:115],v24,25600
global_load_lds_dwordx4 v19, s[82:83] offset:1024
M v[116:119],a[116:119],v[156:159],v[116:119]
T a[116:117],v25,17408
M v[88:91],a[120:123],v[152:155],v[88:91]
T a[118:119],v25,25600
M v[120:123],a[120:123],v[156:159],v[120:123]
T a[120:121],v26,17408
M v[92:95],a[124:127],v[152:155],v[92:95]
T a[122:123],v26,25600
global_load_dwordx4 v[132:135], v17, s[80:81] offset:0 sc1
M v[124:127],a[124:127],v[156:159],v[124:127]
T a[124:125],v27,17408
T a[126:127],v27,25600
s_waitcnt lgkmcnt(8)
M v[64:67],a[96:99],v[160:163],v[64:67]
M v[96:99],a[96:99],v[164:167],v[96:99]
T a[96:97],v24,18432
global_load_lds_dwordx4 v20, s[82:83] offset:2048
M v[68:71],a[100:103],v[160:163],v[68:71]
T a[98:99],v24,26624
M v[100:103],a[100:103],v[164:167],v[100:103]
T a[100:101],v25,18432
M v[72:75],a[104:107],v[160:163],v[72:75]
T a[102:103],v25,26624
M v[104:107],a[104:107],v[164:167],v[104:107]
T a[104:105],v26,18432
global_load_dwordx4 v[136:139], v16, s[80:81] offset:1024 sc1
M v[76:79],a[108:111],v[160:163],v[76:79]
T a[106:107],v26,26624
M v[108:111],a[108:111],v[164:167],v[108:111]
T a[108:109],v27,18432
s_waitcnt lgkmcnt(7)
M v[80:83],a[112:115],v[160:163],v[80:83]
T a[110:111],v27,26624
M v[112:115],a[112:115],v[164:167],v[112:115]
T a[112:113],v24,19456
global_load_lds_dwordx4 v21, s[82:83] offset:3072
M v[84:87],a[116:119],v[160:163],v[84:87]
T a[114:115],v24,27648
s_add_u32 s82, s82, 131072
s_addc_u32 s83, s83, 0
M v[116:119],a[116:119],v[164:167],v[116:119]
T a[116:117],v25,19456
M v[88:91],a[120:123],v[160:163],v[88:91]
T a[118:119],v25,27648
M v[120:123],a[120:123],v[164:167],v[120:123]
T a[120:121],v26,19456
global_load_dwordx4 v[140:143], v17, s[80:81] offset:1024 sc1
s_add_u32 m0, s99, 0
M v[92:95],a[124:127],v[160:163],v[92:95]
T a[122:123],v26,27648
s_add_u32 s80, s80, 2048
s_addc_u32 s81, s81, 0
M v[124:127],a[124:127],v[164:167],v[124:127]
T a[124:125],v27,19456
s_waitcnt vmcnt(41) lgkmcnt(7)
s_barrier
M v[64:67],a[96:99],v[168:171],v[64:67]
T a[126:127],v27,27648
M v[96:99],a[96:99],v[172:175],v[96:99]
T a[96:97],v28,16384
M v[68:71],a[100:103],v[168:171],v[68:71]
T a[98:99],v28,24576
global_load_lds_dwordx4 v18, s[82:83] offset:0
M v[100:103],a[100:103],v[172:175],v[100:103]
T a[100:101],v29,16384
M v[72:75],a[104:107],v[168:171],v[72:75]
T a[102:103],v29,24576
M v[104:107],a[104:107],v[172:175],v[104:107]
T a[104:105],v30,16384
M v[76:79],a[108:111],v[168:171],v[76:79]
T a[106:107],v30,24576
global_load_dwordx4 v[144:147], v16, s[80:81] offset:0 sc1
M v[108:111],a[108:111],v[172:175],v[108:111]
T a[108:109],v31,16384
s_waitcnt lgkmcnt(7)
M v[80:83],a[112:115],v[168:171],v[80:83]
T a[110:111],v31,24576
M v[112:115],a[112:115],v[172:175],v[112:115]
T a[112:113],v28,17408
M v[84:87],a[116:119],v[168:171],v[84:87]
T a[114:115],v28,25600
global_load_lds_dwordx4 v19, s[82:83] offset:1024
M v[116:119],a[116:119],v[172:175],v[116:119]
T a[116:117],v29,17408
M v[88:91],a[120:123],v[168:171],v[88:91]
T a[118:119],v29,25600
M v[120:123],a[120:123],v[172:175],v[120:123]
T a[120:121],v30,17408
M v[92:95],a[124:127],v[168:171],v[92:95]
T a[122:123],v30,25600
global_load_dwordx4 v[148:151], v17, s[80:81] offset:0 sc1
M v[124:127],a[124:127],v[172:175],v[124:127]
T a[124:125],v31,17408
T a[126:127],v31,25600
s_waitcnt lgkmcnt(8)
M v[64:67],a[96:99],v[176:179],v[64:67]
M v[96:99],a[96:99],v[180:183],v[96:99]
T a[96:97],v28,18432
global_load_lds_dwordx4 v20, s[82:83] offset:2048
M v[68:71],a[100:103],v[176:179],v[68:71]
T a[98:99],v28,26624
M v[100:103],a[100:103],v[180:183],v[100:103]
T a[100:101],v29,18432
M v[72:75],a[104:107],v[176:179],v[72:75]
T a[102:103],v29,26624
M v[104:107],a[104:107],v[180:183],v[104:107]
T a[104:105],v30,18432
global_load_dwordx4 v[152:155], v16, s[80:81] offset:1024 sc1
M v[76:79],a[108:111],v[176:179],v[76:79]
T a[106:107],v30,26624
M v[108:111],a[108:111],v[180:183],v[108:111]
T a[108:109],v31,18432
s_waitcnt lgkmcnt(7)
M v[80:83],a[112:115],v[176:179],v[80:83]
T a[110:111],v31,26624
M v[112:115],a[112:115],v[180:183],v[112:115]
T a[112:113],v28,19456
global_load_lds_dwordx4 v21, s[82:83] offset:3072
M v[84:87],a[116:119],v[176:179],v[84:87]
T a[114:115],v28,27648
s_add_u32 s82, s82, 131072
s_addc_u32 s83, s83, 0
M v[116:119],a[116:119],v[180:183],v[116:119]
T a[116:117],v29,19456
M v[88:91],a[120:123],v[176:179],v[88:91]
T a[118:119],v29,27648
M v[120:123],a[120:123],v[180:183],v[120:123]
T a[120:121],v30,19456
global_load_dwordx4 v[156:159], v17, s[80:81] offset:1024 sc1
s_add_u32 m0, s99, 16384
M v[92:95],a[124:127],v[176:179],v[92:95]
T a[122:123],v30,27648
s_add_u32 s80, s80, 2048
s_addc_u32 s81, s81, 0
M v[124:127],a[124:127],v[180:183],v[124:127]
T a[124:125],v31,19456
s_waitcnt vmcnt(41) lgkmcnt(7)
s_barrier
M v[64:67],a[96:99],v[184:187],v[64:67]
T a[126:127],v31,27648
M v[96:99],a[96:99],v[188:191],v[96:99]
T a[96:97],v24,32768
M v[68:71],a[100:103],v[184:187],v[68:71]
T a[98:99],v24,40960
global_load_lds_dwordx4 v18, s[82:83] offset:0
M v[100:103],a[100:103],v[188:191],v[100:103]
T a[100:101],v25,32768
M v[72:75],a[104:107],v[184:187],v[72:75]
T a[102:103],v25,40960
M v[104:107],a[104:107],v[188:191],v[104:107]
T a[104:105],v26,32768
M v[76:79],a[108:111],v[184:187],v[76:79]
T a[106:107],v26,40960
global_load_dwordx4 v[160:163], v16, s[80:81] offset:0 sc1
M v[108:111],a[108:111],v[188:191],v[108:111]
T a[108:109],v27,32768
s_waitcnt lgkmcnt(7)
M v[80:83],a[112:115],v[184:187],v[80:83]
T a[110:111],v27,40960
M v[112:115],a[112:115],v[188:191],v[112:115]
T a[112:113],v24,33792
M v[84:87],a[116:119],v[184:187],v[84:87]
T a[114:115],v24,41984
global_load_lds_dwordx4 v19, s[82:83] offset:1024
M v[116:119],a[116:119],v[188:191],v[116:119]
T a[116:117],v25,33792
M v[88:91],a[120:123],v[184:187],v[88:91]
T a[118:119],v25,41984
M v[120:123],a[120:123],v[188:191],v[120:123]
T a[120:121],v26,33792
M v[92:95],a[124:127],v[184:187],v[92:95]
T a[122:123],v26,41984
global_load_dwordx4 v[164:167], v17, s[80:81] offset:0 sc1
M v[124:127],a[124:127],v[188:191],v[124:127]
T a[124:125],v27,33792
T a[126:127],v27,41984
s_waitcnt lgkmcnt(8)
M v[64:67],a[96:99],v[192:195],v[64:67]
M v[96:99],a[96:99],v[196:199],v[96:99]
T a[96:97],v24,34816
global_load_lds_dwordx4 v20, s[82:83] offset:2048
M v[68:71],a[100:103],v[192:195],v[68:71]
T a[98:99],v24,43008
M v[100:103],a[100:103],v[196:199],v[100:103]
T a[100:101],v25,34816
M v[72:75],a[104:107],v[192:195],v[72:75]
T a[102:103],v25,43008
M v[104:107],a[104:107],v[196:199],v[104:107]
T a[104:105],v26,34816
global_load_dwordx4 v[168:171], v16, s[80:81] offset:1024 sc1
M v[76:79],a[108:111],v[192:195],v[76:79]
T a[106:107],v26,43008
M v[108:111],a[108:111],v[196:199],v[108:111]
T a[108:109],v27,34816
s_waitcnt lgkmcnt(7)
M v[80:83],a[112:115],v[192:195],v[80:83]
T a[110:111],v27,43008
M v[112:115],a[112:115],v[196:199],v[112:115]
T a[112:113],v24,35840
global_load_lds_dwordx4 v21, s[82:83] offset:3072
M v[84:87],a[116:119],v[192:195],v[84:87]
T a[114:115],v24,44032
s_add_u32 s82, s82, 131072
s_addc_u32 s83, s83, 0
M v[116:119],a[116:119],v[196:199],v[116:119]
T a[116:117],v25,35840
M v[88:91],a[120:123],v[192:195],v[88:91]
T a[118:119],v25,44032
M v[120:123],a[120:123],v[196:199],v[120:123]
T a[120:121],v26,35840
global_load_dwordx4 v[172:175], v17, s[80:81] offset:1024 sc1
s_add_u32 m0, s99, 81920
M v[92:95],a[124:127],v[192:195],v[92:95]
T a[122:123],v26,44032
s_add_u32 s80, s80, 2048
s_addc_u32 s81, s81, 0
M v[124:127],a[124:127],v[196:199],v[124:127]
T a[124:125],v27,35840
s_waitcnt vmcnt(41) lgkmcnt(7)
s_barrier
M v[64:67],a[96:99],v[200:203],v[64:67]
T a[126:127],v27,44032
M v[96:99],a[96:99],v[204:207],v[96:99]
T a[96:97],v24,49152
M v[68:71],a[100:103],v[200:203],v[68:71]
T a[98:99],v24,57344
global_load_lds_dwordx4 v18, s[82:83] offset:0
M v[100:103],a[100:103],v[204:207],v[100:103]
T a[100:101],v25,49152
M v[72:75],a[104:107],v[200:203],v[72:75]
T a[102:103],v25,57344
M v[104:107],a[104:107],v[204:207],v[104:107]
T a[104:105],v26,49152
M v[76:79],a[108:111],v[200:203],v[76:79]
T a[106:107],v26,57344
global_load_dwordx4 v[176:179], v16, s[80:81] offset:0 sc1
M v[108:111],a[108:111],v[204:207],v[108:111]
T a[108:109],v27,49152
s_waitcnt lgkmcnt(7)
M v[80:83],a[112:115],v[200:203],v[80:83]
T a[110:111],v27,57344
M v[112:115],a[112:115],v[204:207],v[112:115]
T a[112:113],v24,50176
M v[84:87],a[116:119],v[200:203],v[84:87]
T a[114:115],v24,58368
global_load_lds_dwordx4 v19, s[82:83] offset:1024
M v[116:119],a[116:119],v[204:207],v[116:119]
T a[116:117],v25,50176
M v[88:91],a[120:123],v[200:203],v[88:91]
T a[118:119],v25,58368
M v[120:123],a[120:123],v[204:207],v[120:123]
T a[120:121],v26,50176
M v[92:95],a[124:127],v[200:203],v[92:95]
T a[122:123],v26,58368
global_load_dwordx4 v[180:183], v17, s[80:81] offset:0 sc1
M v[124:127],a[124:127],v[204:207],v[124:127]
T a[124:125],v27,50176
T a[126:127],v27,58368
s_waitcnt lgkmcnt(8)
M v[64:67],a[96:99],v[208:211],v[64:67]
M v[96:99],a[96:99],v[212:215],v[96:99]
T a[96:97],v24,51200
global_load_lds_dwordx4 v20, s[82:83] offset:2048
M v[68:71],a[100:103],v[208:211],v[68:71]
T a[98:99],v24,59392
M v[100:103],a[100:103],v[212:215],v[100:103]
T a[100:101],v25,51200
M v[72:75],a[104:107],v[208:211],v[72:75]
T a[102:103],v25,59392
M v[104:107],a[104:107],v[212:215],v[104:107]
T a[104:105],v26,51200
global_load_dwordx4 v[184:187], v16, s[80:81] offset:1024 sc1
M v[76:79],a[108:111],v[208:211],v[76:79]
T a[106:107],v26,59392
M v[108:111],a[108:111],v[212:215],v[108:111]
T a[108:109],v27,51200
s_waitcnt lgkmcnt(7)
M v[80:83],a[112:115],v[208:211],v[80:83]
T a[110:111],v27,59392
M v[112:115],a[112:115],v[212:215],v[112:115]
T a[112:113],v24,52224
global_load_lds_dwordx4 v21, s[82:83] offset:3072
M v[84:87],a[116:119],v[208:211],v[84:87]
T a[114:115],v24,60416
s_add_u32 s82, s82, 131072
s_addc_u32 s83, s83, 0
M v[116:119],a[116:119],v[212:215],v[116:119]
T a[116:117],v25,52224
M v[88:91],a[120:123],v[208:211],v[88:91]
T a[118:119],v25,60416
M v[120:123],a[120:123],v[212:215],v[120:123]
T a[120:121],v26,52224
global_load_dwordx4 v[188:191], v17, s[80:81] offset:1024 sc1
s_add_u32 m0, s99, 32768
M v[92:95],a[124:127],v[208:211],v[92:95]
T a[122:123],v26,60416
s_add_u32 s80, s80, 2048
s_addc_u32 s81, s81, 0
M v[124:127],a[124:127],v[212:215],v[124:127]
T a[124:125],v27,52224
s_waitcnt vmcnt(40) lgkmcnt(7)
s_barrier
M v[64:67],a[96:99],v[216:219],v[64:67]
T a[126:127],v27,60416
M v[96:99],a[96:99],v[220:223],v[96:99]
T a[96:97],v28,32768
M v[68:71],a[100:103],v[216:219],v[68:71]
T a[98:99],v28,40960
global_load_lds_dwordx4 v18, s[82:83] offset:0
M v[100:103],a[100:103],v[220:223],v[100:103]
T a[100:101],v29,32768
M v[72:75],a[104:107],v[216:219],v[72:75]
T a[102:103],v29,40960
M v[104:107],a[104:107],v[220:223],v[104:107]
T a[104:105],v30,32768
M v[76:79],a[108:111],v[216:219],v[76:79]
T a[106:107],v30,40960
global_load_dwordx4 v[192:195], v16, s[80:81] offset:0 sc1
M v[108:111],a[108:111],v[220:223],v[108:111]
T a[108:109],v31,32768
s_waitcnt lgkmcnt(7)
M v[80:83],a[112:115],v[216:219],v[80:83]
T a[110:111],v31,40960
M v[112:115],a[112:115],v[220:223],v[112:115]
T a[112:113],v28,33792
M v[84:87],a[116:119],v[216:219],v[84:87]
T a[114:115],v28,41984
global_load_lds_dwordx4 v19, s[82:83] offset:1024
M v[116:119],a[116:119],v[220:223],v[116:119]
T a[116:117],v29,33792
M v[88:91],a[120:123],v[216:219],v[88:91]
T a[118:119],v29,41984
M v[120:123],a[120:123],v[220:223],v[120:123]
T a[120:121],v30,33792
M v[92:95],a[124:127],v[216:219],v[92:95]
T a[122:123],v30,41984
global_load_dwordx4 v[196:199], v17, s[80:81] offset:0 sc1
M v[124:127],a[124:127],v[220:223],v[124:127]
T a[124:125],v31,33792
T a[126:127],v31,41984
s_waitcnt lgkmcnt(8)
M v[64:67],a[96:99],v[224:227],v[64:67]
M v[96:99],a[96:99],v[228:231],v[96:99]
T a[96:97],v28,34816
global_load_lds_dwordx4 v20, s[82:83] offset:2048
M v[68:71],a[100:103],v[224:227],v[68:71]
T a[98:99],v28,43008
M v[100:103],a[100:103],v[228:231],v[100:103]
T a[100:101],v29,34816
M v[72:75],a[104:107],v[224:227],v[72:75]
T a[102:103],v29,43008
M v[104:107],a[104:107],v[228:231],v[104:107]
T a[104:105],v30,34816
global_load_dwordx4 v[200:203], v16, s[80:81] offset:1024 sc1
M v[76:79],a[108:111],v[224:227],v[76:79]
T a[106:107],v30,43008
M v[108:111],a[108:111],v[228:231],v[108:111]
T a[108:109],v31,34816
s_waitcnt lgkmcnt(7)
M v[80:83],a[112:115],v[224:227],v[80:83]
T a[110:111],v31,43008
M v[112:115],a[112:115],v[228:231],v[112:115]
T a[112:113],v28,35840
global_load_lds_dwordx4 v21, s[82:83] offset:3072
M v[84:87],a[116:119],v[224:227],v[84:87]
T a[114:115],v28,44032
s_add_u32 s82, s82, 131072
s_addc_u32 s83, s83, 0
M v[116:119],a[116:119],v[228:231],v[116:119]
T a[116:117],v29,35840
M v[88:91],a[120:123],v[224:227],v[88:91]
T a[118:119],v29,44032
M v[120:123],a[120:123],v[228:231],v[120:123]
T a[120:121],v30,35840
global_load_dwordx4 v[204:207], v17, s[80:81] offset:1024 sc1
s_add_u32 m0, s99, 49152
M v[92:95],a[124:127],v[224:227],v[92:95]
T a[122:123],v30,44032
s_add_u32 s80, s80, 2048
s_addc_u32 s81, s81, 0
M v[124:127],a[124:127],v[228:231],v[124:127]
T a[124:125],v31,35840
s_waitcnt vmcnt(40) lgkmcnt(7)
s_barrier
M v[64:67],a[96:99],v[232:235],v[64:67]
T a[126:127],v31,44032
M v[96:99],a[96:99],v[236:239],v[96:99]
T a[96:97],v28,0
M v[68:71],a[100:103],v[232:235],v[68:71]
T a[98:99],v28,8192
global_load_lds_dwordx4 v18, s[82:83] offset:0
M v[100:103],a[100:103],v[236:239],v[100:103]
T a[100:101],v29,0
M v[72:75],a[104:107],v[232:235],v[72:75]
T a[102:103],v29,8192
M v[104:107],a[104:107],v[236:239],v[104:107]
T a[104:105],v30,0
M v[76:79],a[108:111],v[232:235],v[76:79]
T a[106:107],v30,8192
global_load_dwordx4 v[208:211], v16, s[80:81] offset:0 sc1
M v[108:111],a[108:111],v[236:239],v[108:111]
T a[108:109],v31,0
s_waitcnt lgkmcnt(7)
M v[80:83],a[112:115],v[232:235],v[80:83]
T a[110:111],v31,8192
M v[112:115],a[112:115],v[236:239],v[112:115]
T a[112:113],v28,1024
M v[84:87],a[116:119],v[232:235],v[84:87]
T a[114:115],v28,9216
global_load_lds_dwordx4 v19, s[82:83] offset:1024
M v[116:119],a[116:119],v[236:239],v[116:119]
T a[116:117],v29,1024
M v[88:91],a[120:123],v[232:235],v[88:91]
T a[118:119],v29,9216
M v[120:123],a[120:123],v[236:239],v[120:123]
T a[120:121],v30,1024
M v[92:95],a[124:127],v[232:235],v[92:95]
T a[122:123],v30,9216
global_load_dwordx4 v[212:215], v17, s[80:81] offset:0 sc1
M v[124:127],a[124:127],v[236:239],v[124:127]
T a[124:125],v31,1024
T a[126:127],v31,9216
s_waitcnt lgkmcnt(8)
M v[64:67],a[96:99],v[240:243],v[64:67]
M v[96:99],a[96:99],v[244:247],v[96:99]
T a[96:97],v28,2048
global_load_lds_dwordx4 v20, s[82:83] offset:2048
M v[68:71],a[100:103],v[240:243],v[68:71]
T a[98:99],v28,10240
M v[100:103],a[100:103],v[244:247],v[100:103]
T a[100:101],v29,2048
M v[72:75],a[104:107],v[240:243],v[72:75]
T a[102:103],v29,10240
M v[104:107],a[104:107],v[244:247],v[104:107]
T a[104:105],v30,2048
global_load_dwordx4 v[216:219], v16, s[80:81] offset:1024 sc1
M v[76:79],a[108:111],v[240:243],v[76:79]
T a[106:107],v30,10240
M v[108:111],a[108:111],v[244:247],v[108:111]
T a[108:109],v31,2048
s_waitcnt lgkmcnt(7)
M v[80:83],a[112:115],v[240:243],v[80:83]
T a[110:111],v31,10240
M v[112:115],a[112:115],v[244:247],v[112:115]
T a[112:113],v28,3072
global_load_lds_dwordx4 v21, s[82:83] offset:3072
M v[84:87],a[116:119],v[240:243],v[84:87]
T a[114:115],v28,11264
s_add_u32 s82, s82, 131072
s_addc_u32 s83, s83, 0
M v[116:119],a[116:119],v[244:247],v[116:119]
T a[116:117],v29,3072
M v[88:91],a[120:123],v[240:243],v[88:91]
T a[118:119],v29,11264
M v[120:123],a[120:123],v[244:247],v[120:123]
T a[120:121],v30,3072
global_load_dwordx4 v[220:223], v17, s[80:81] offset:1024 sc1
s_add_u32 m0, s99, 98304
M v[92:95],a[124:127],v[240:243],v[92:95]
T a[122:123],v30,11264
s_add_u32 s80, s80, 2048
s_addc_u32 s81, s81, 0
M v[124:127],a[124:127],v[244:247],v[124:127]
T a[124:125],v31,3072
s_waitcnt vmcnt(40) lgkmcnt(7)
s_barrier
M v[64:67],a[96:99],v[248:251],v[64:67]
T a[126:127],v31,11264
global_load_dword v35, v34, s[96:97] offset:1024 sc1
M v[96:99],a[96:99],v[252:255],v[96:99]
T a[96:97],v28,49152
M v[68:71],a[100:103],v[248:251],v[68:71]
T a[98:99],v28,57344
global_load_lds_dwordx4 v18, s[82:83] offset:0
M v[100:103],a[100:103],v[252:255],v[100:103]
T a[100:101],v29,49152
M v[72:75],a[104:107],v[248:251],v[72:75]
T a[102:103],v29,57344
M v[104:107],a[104:107],v[252:255],v[104:107]
T a[104:105],v30,49152
M v[76:79],a[108:111],v[248:251],v[76:79]
T a[106:107],v30,57344
global_load_dwordx4 v[224:227], v16, s[80:81] offset:0 sc1
M v[108:111],a[108:111],v[252:255],v[108:111]
T a[108:109],v31,49152
s_waitcnt lgkmcnt(7)
M v[80:83],a[112:115],v[248:251],v[80:83]
T a[110:111],v31,57344
M v[112:115],a[112:115],v[252:255],v[112:115]
T a[112:113],v28,50176
M v[84:87],a[116:119],v[248:251],v[84:87]
T a[114:115],v28,58368
global_load_lds_dwordx4 v19, s[82:83] offset:1024
M v[116:119],a[116:119],v[252:255],v[116:119]
T a[116:117],v29,50176
M v[88:91],a[120:123],v[248:251],v[88:91]
T a[118:119],v29,58368
s_sub_u32 s64, s64, 1
M v[120:123],a[120:123],v[252:255],v[120:123]
T a[120:121],v30,50176
M v[92:95],a[124:127],v[248:251],v[92:95]
T a[122:123],v30,58368
global_load_dwordx4 v[228:231], v17, s[80:81] offset:0 sc1
s_cmp_lg_u32 s64, 0
M v[124:127],a[124:127],v[252:255],v[124:127]
T a[124:125],v31,50176
T a[126:127],v31,58368
s_cbranch_scc1 Lg2Q_%=
s_waitcnt lgkmcnt(8)
M v[64:67],a[96:99],v[128:131],v[64:67]
M v[96:99],a[96:99],v[132:135],v[96:99]
T a[96:97],v28,51200
global_load_lds_dwordx4 v20, s[82:83] offset:2048
M v[68:71],a[100:103],v[128:131],v[68:71]
T a[98:99],v28,59392
M v[100:103],a[100:103],v[132:135],v[100:103]
T a[100:101],v29,51200
M v[72:75],a[104:107],v[128:131],v[72:75]
T a[102:103],v29,59392
M v[104:107],a[104:107],v[132:135],v[104:107]
T a[104:105],v30,51200
global_load_dwordx4 v[232:235], v16, s[80:81] offset:1024 sc1
M v[76:79],a[108:111],v[128:131],v[76:79]
T a[106:107],v30,59392
M v[108:111],a[108:111],v[132:135],v[108:111]
T a[108:109],v31,51200
s_waitcnt lgkmcnt(7)
M v[80:83],a[112:115],v[128:131],v[80:83]
T a[110:111],v31,59392
M v[112:115],a[112:115],v[132:135],v[112:115]
T a[112:113],v28,52224
global_load_lds_dwordx4 v21, s[82:83] offset:3072
M v[84:87],a[116:119],v[128:131],v[84:87]
T a[114:115],v28,60416
s_add_u32 s82, s82, 131072
s_addc_u32 s83, s83, 0
M v[116:119],a[116:119],v[132:135],v[116:119]
T a[116:117],v29,52224
M v[88:91],a[120:123],v[128:131],v[88:91]
T a[118:119],v29,60416
M v[120:123],a[120:123],v[132:135],v[120:123]
T a[120:121],v30,52224
global_load_dwordx4 v[236:239], v17, s[80:81] offset:1024 sc1
s_add_u32 m0, s99, 65536
M v[92:95],a[124:127],v[128:131],v[92:95]
T a[122:123],v30,60416
s_add_u32 s80, s80, 2048
s_addc_u32 s81, s81, 0
M v[124:127],a[124:127],v[132:135],v[124:127]
T a[124:125],v31,52224
s_waitcnt vmcnt(41) lgkmcnt(7)
s_barrier
M v[64:67],a[96:99],v[136:139],v[64:67]
T a[126:127],v31,60416
M v[96:99],a[96:99],v[140:143],v[96:99]
T a[96:97],v24,0
M v[68:71],a[100:103],v[136:139],v[68:71]
T a[98:99],v24,8192
global_load_lds_dwordx4 v18, s[82:83] offset:0
M v[100:103],a[100:103],v[140:143],v[100:103]
T a[100:101],v25,0
M v[72:75],a[104:107],v[136:139],v[72:75]
T a[102:103],v25,8192
M v[104:107],a[104:107],v[140:143],v[104:107]
T a[104:105],v26,0
M v[76:79],a[108:111],v[136:139],v[76:79]
T a[106:107],v26,8192
global_load_dwordx4 v[240:243], v16, s[80:81] offset:0 sc1
M v[108:111],a[108:111],v[140:143],v[108:111]
T a[108:109],v27,0
s_waitcnt lgkmcnt(7)
M v[80:83],a[112:115],v[136:139],v[80:83]
T a[110:111],v27,8192
M v[112:115],a[112:115],v[140:143],v[112:115]
T a[112:113],v24,1024
M v[84:87],a[116:119],v[136:139],v[84:87]
T a[114:115],v24,9216
global_load_lds_dwordx4 v19, s[82:83] offset:1024
M v[116:119],a[116:119],v[140:143],v[116:119]
T a[116:117],v25,1024
M v[88:91],a[120:123],v[136:139],v[88:91]
T a[118:119],v25,9216
M v[120:123],a[120:123],v[140:143],v[120:123]
T a[120:121],v26,1024
M v[92:95],a[124:127],v[136:139],v[92:95]
T a[122:123],v26,9216
global_load_dwordx4 v[244:247], v17, s[80:81] offset:0 sc1
M v[124:127],a[124:127],v[140:143],v[124:127]
T a[124:125],v27,1024
T a[126:127],v27,9216
s_waitcnt lgkmcnt(8)
M v[64:67],a[96:99],v[144:147],v[64:67]
M v[96:99],a[96:99],v[148:151],v[96:99]
T a[96:97],v24,2048
global_load_lds_dwordx4 v20, s[82:83] offset:2048
M v[68:71],a[100:103],v[144:147],v[68:71]
T a[98:99],v24,10240
M v[100:103],a[100:103],v[148:151],v[100:103]
T a[100:101],v25,2048
M v[72:75],a[104:107],v[144:147],v[72:75]
T a[102:103],v25,10240
M v[104:107],a[104:107],v[148:151],v[104:107]
T a[104:105],v26,2048
global_load_dwordx4 v[248:251], v16, s[80:81] offset:1024 sc1
M v[76:79],a[108:111],v[144:147],v[76:79]
T a[106:107],v26,10240
M v[108:111],a[108:111],v[148:151],v[108:111]
T a[108:109],v27,2048
s_waitcnt lgkmcnt(7)
M v[80:83],a[112:115],v[144:147],v[80:83]
T a[110:111],v27,10240
M v[112:115],a[112:115],v[148:151],v[112:115]
T a[112:113],v24,3072
global_load_lds_dwordx4 v21, s[82:83] offset:3072
M v[84:87],a[116:119],v[144:147],v[84:87]
T a[114:115],v24,11264
M v[116:119],a[116:119],v[148:151],v[116:119]
T a[116:117],v25,3072
M v[88:91],a[120:123],v[144:147],v[88:91]
T a[118:119],v25,11264
M v[120:123],a[120:123],v[148:151],v[120:123]
T a[120:121],v26,3072
global_load_dwordx4 v[252:255], v17, s[80:81] offset:1024 sc1
M v[92:95],a[124:127],v[144:147],v[92:95]
T a[122:123],v26,11264
M v[124:127],a[124:127],v[148:151],v[124:127]
T a[124:125],v27,3072
s_waitcnt vmcnt(41) lgkmcnt(7)
s_barrier
M v[64:67],a[96:99],v[152:155],v[64:67]
T a[126:127],v27,11264
M v[96:99],a[96:99],v[156:159],v[96:99]
T a[96:97],v24,16384
M v[68:71],a[100:103],v[152:155],v[68:71]
T a[98:99],v24,24576
M v[100:103],a[100:103],v[156:159],v[100:103]
T a[100:101],v25,16384
M v[72:75],a[104:107],v[152:155],v[72:75]
T a[102:103],v25,24576
M v[104:107],a[104:107],v[156:159],v[104:107]
T a[104:105],v26,16384
M v[76:79],a[108:111],v[152:155],v[76:79]
T a[106:107],v26,24576
M v[108:111],a[108:111],v[156:159],v[108:111]
T a[108:109],v27,16384
s_waitcnt lgkmcnt(7)
M v[80:83],a[112:115],v[152:155],v[80:83]
T a[110:111],v27,24576
M v[112:115],a[112:115],v[156:159],v[112:115]
T a[112:113],v24,17408
M v[84:87],a[116:119],v[152:155],v[84:87]
T a[114:115],v24,25600
M v[116:119],a[116:119],v[156:159],v[116:119]
T a[116:117],v25,17408
M v[88:91],a[120:123],v[152:155],v[88:91]
T a[118:119],v25,25600
M v[120:123],a[120:123],v[156:159],v[120:123]
T a[120:121],v26,17408
M v[92:95],a[124:127],v[152:155],v[92:95]
T a[122:123],v26,25600
M v[124:127],a[124:127],v[156:159],v[124:127]
T a[124:125],v27,17408
T a[126:127],v27,25600
s_waitcnt lgkmcnt(8)
M v[64:67],a[96:99],v[160:163],v[64:67]
M v[96:99],a[96:99],v[164:167],v[96:99]
T a[96:97],v24,18432
M v[68:71],a[100:103],v[160:163],v[68:71]
T a[98:99],v24,26624
M v[100:103],a[100:103],v[164:167],v[100:103]
T a[100:101],v25,18432
M v[72:75],a[104:107],v[160:163],v[72:75]
T a[102:103],v25,26624
M v[104:107],a[104:107],v[164:167],v[104:107]
T a[104:105],v26,18432
M v[76:79],a[108:111],v[160:163],v[76:79]
T a[106:107],v26,26624
M v[108:111],a[108:111],v[164:167],v[108:111]
T a[108:109],v27,18432
s_waitcnt lgkmcnt(7)
M v[80:83],a[112:115],v[160:163],v[80:83]
T a[110:111],v27,26624
M v[112:115],a[112:115],v[164:167],v[112:115]
T a[112:113],v24,19456
M v[84:87],a[116:119],v[160:163],v[84:87]
T a[114:115],v24,27648
M v[116:119],a[116:119],v[164:167],v[116:119]
T a[116:117],v25,19456
M v[88:91],a[120:123],v[160:163],v[88:91]
T a[118:119],v25,27648
M v[120:123],a[120:123],v[164:167],v[120:123]
T a[120:121],v26,19456
M v[92:95],a[124:127],v[160:163],v[92:95]
T a[122:123],v26,27648
M v[124:127],a[124:127],v[164:167],v[124:127]
T a[124:125],v27,19456
s_waitcnt vmcnt(33) lgkmcnt(7)
s_barrier
M v[64:67],a[96:99],v[168:171],v[64:67]
T a[126:127],v27,27648
M v[96:99],a[96:99],v[172:175],v[96:99]
T a[96:97],v28,16384
M v[68:71],a[100:103],v[168:171],v[68:71]
T a[98:99],v28,24576
M v[100:103],a[100:103],v[172:175],v[100:103]
T a[100:101],v29,16384
M v[72:75],a[104:107],v[168:171],v[72:75]
T a[102:103],v29,24576
M v[104:107],a[104:107],v[172:175],v[104:107]
T a[104:105],v30,16384
M v[76:79],a[108:111],v[168:171],v[76:79]
T a[106:107],v30,24576
M v[108:111],a[108:111],v[172:175],v[108:111]
T a[108:109],v31,16384
s_waitcnt lgkmcnt(7)
M v[80:83],a[112:115],v[168:171],v[80:83]
T a[110:111],v31,24576
M v[112:115],a[112:115],v[172:175],v[112:115]
T a[112:113],v28,17408
M v[84:87],a[116:119],v[168:171],v[84:87]
T a[114:115],v28,25600
M v[116:119],a[116:119],v[172:175],v[116:119]
T a[116:117],v29,17408
M v[88:91],a[120:123],v[168:171],v[88:91]
T a[118:119],v29,25600
M v[120:123],a[120:123],v[172:175],v[120:123]
T a[120:121],v30,17408
M v[92:95],a[124:127],v[168:171],v[92:95]
T a[122:123],v30,25600
M v[124:127],a[124:127],v[172:175],v[124:127]
T a[124:125],v31,17408
T a[126:127],v31,25600
s_waitcnt lgkmcnt(8)
M v[64:67],a[96:99],v[176:179],v[64:67]
M v[96:99],a[96:99],v[180:183],v[96:99]
T a[96:97],v28,18432
M v[68:71],a[100:103],v[176:179],v[68:71]
T a[98:99],v28,26624
M v[100:103],a[100:103],v[180:183],v[100:103]
T a[100:101],v29,18432
M v[72:75],a[104:107],v[176:179],v[72:75]
T a[102:103],v29,26624
M v[104:107],a[104:107],v[180:183],v[104:107]
T a[104:105],v30,18432
M v[76:79],a[108:111],v[176:179],v[76:79]
T a[106:107],v30,26624
M v[108:111],a[108:111],v[180:183],v[108:111]
T a[108:109],v31,18432
s_waitcnt lgkmcnt(7)
M v[80:83],a[112:115],v[176:179],v[80:83]
T a[110:111],v31,26624
M v[112:115],a[112:115],v[180:183],v[112:115]
T a[112:113],v28,19456
M v[84:87],a[116:119],v[176:179],v[84:87]
T a[114:115],v28,27648
M v[116:119],a[116:119],v[180:183],v[116:119]
T a[116:117],v29,19456
M v[88:91],a[120:123],v[176:179],v[88:91]
T a[118:119],v29,27648
M v[120:123],a[120:123],v[180:183],v[120:123]
T a[120:121],v30,19456
M v[92:95],a[124:127],v[176:179],v[92:95]
T a[122:123],v30,27648
M v[124:127],a[124:127],v[180:183],v[124:127]
T a[124:125],v31,19456
s_waitcnt vmcnt(25) lgkmcnt(7)
s_barrier
M v[64:67],a[96:99],v[184:187],v[64:67]
T a[126:127],v31,27648
M v[96:99],a[96:99],v[188:191],v[96:99]
T a[96:97],v24,32768
global_load_dword v37, v38, s[76:77] sc1
M v[68:71],a[100:103],v[184:187],v[68:71]
T a[98:99],v24,40960
M v[100:103],a[100:103],v[188:191],v[100:103]
T a[100:101],v25,32768
M v[72:75],a[104:107],v[184:187],v[72:75]
T a[102:103],v25,40960
M v[104:107],a[104:107],v[188:191],v[104:107]
T a[104:105],v26,32768
M v[76:79],a[108:111],v[184:187],v[76:79]
T a[106:107],v26,40960
M v[108:111],a[108:111],v[188:191],v[108:111]
T a[108:109],v27,32768
s_waitcnt lgkmcnt(7)
M v[80:83],a[112:115],v[184:187],v[80:83]
T a[110:111],v27,40960
M v[112:115],a[112:115],v[188:191],v[112:115]
T a[112:113],v24,33792
M v[84:87],a[116:119],v[184:187],v[84:87]
T a[114:115],v24,41984
M v[116:119],a[116:119],v[188:191],v[116:119]
T a[116:117],v25,33792
M v[88:91],a[120:123],v[184:187],v[88:91]
T a[118:119],v25,41984
M v[120:123],a[120:123],v[188:191],v[120:123]
T a[120:121],v26,33792
M v[92:95],a[124:127],v[184:187],v[92:95]
T a[122:123],v26,41984
M v[124:127],a[124:127],v[188:191],v[124:127]
T a[124:125],v27,33792
T a[126:127],v27,41984
s_waitcnt lgkmcnt(8)
M v[64:67],a[96:99],v[192:195],v[64:67]
M v[96:99],a[96:99],v[196:199],v[96:99]
T a[96:97],v24,34816
M v[68:71],a[100:103],v[192:195],v[68:71]
T a[98:99],v24,43008
M v[100:103],a[100:103],v[196:199],v[100:103]
T a[100:101],v25,34816
M v[72:75],a[104:107],v[192:195],v[72:75]
T a[102:103],v25,43008
M v[104:107],a[104:107],v[196:199],v[104:107]
T a[104:105],v26,34816
M v[76:79],a[108:111],v[192:195],v[76:79]
T a[106:107],v26,43008
M v[108:111],a[108:111],v[196:199],v[108:111]
T a[108:109],v27,34816
s_waitcnt lgkmcnt(7)
M v[80:83],a[112:115],v[192:195],v[80:83]
T a[110:111],v27,43008
M v[112:115],a[112:115],v[196:199],v[112:115]
T a[112:113],v24,35840
M v[84:87],a[116:119],v[192:195],v[84:87]
T a[114:115],v24,44032
M v[116:119],a[116:119],v[196:199],v[116:119]
T a[116:117],v25,35840
M v[88:91],a[120:123],v[192:195],v[88:91]
T a[118:119],v25,44032
M v[120:123],a[120:123],v[196:199],v[120:123]
T a[120:121],v26,35840
M v[92:95],a[124:127],v[192:195],v[92:95]
T a[122:123],v26,44032
M v[124:127],a[124:127],v[196:199],v[124:127]
T a[124:125],v27,35840
s_waitcnt vmcnt(0) lgkmcnt(7)
s_barrier
v_cmp_ne_u32 vcc, s98, v37
s_cbranch_vccz Lokpp_%=
s_mov_b32 s65, 0x100000
Lsppp_%=:
s_sleep 1
global_load_dword v37, v38, s[76:77] offset:0 sc1
s_waitcnt vmcnt(0)
v_cmp_ne_u32 vcc, s98, v37
s_cbranch_vccz Lokpp_%=
s_sub_u32 s65, s65, 1
s_cmp_lg_u32 s65, 0
s_cbranch_scc1 Lsppp_%=
Lokpp_%=:
M v[64:67],a[96:99],v[200:203],v[64:67]
T a[126:127],v27,44032
M v[96:99],a[96:99],v[204:207],v[96:99]
T a[96:97],v24,49152
global_load_dwordx4 v[128:131], v16, s[94:95] offset:0 sc1
M v[68:71],a[100:103],v[200:203],v[68:71]
T a[98:99],v24,57344
M v[100:103],a[100:103],v[204:207],v[100:103]
T a[100:101],v25,49152
M v[72:75],a[104:107],v[200:203],v[72:75]
T a[102:103],v25,57344
M v[104:107],a[104:107],v[204:207],v[104:107]
T a[104:105],v26,49152
global_load_dwordx4 v[132:135], v16, s[94:95] offset:1024 sc1
M v[76:79],a[108:111],v[200:203],v[76:79]
T a[106:107],v26,57344
M v[108:111],a[108:111],v[204:207],v[108:111]
T a[108:109],v27,49152
s_waitcnt lgkmcnt(7)
M v[80:83],a[112:115],v[200:203],v[80:83]
T a[110:111],v27,57344
M v[112:115],a[112:115],v[204:207],v[112:115]
T a[112:113],v24,50176
global_load_dwordx4 v[136:139], v16, s[94:95] offset:2048 sc1
M v[84:87],a[116:119],v[200:203],v[84:87]
T a[114:115],v24,58368
M v[116:119],a[116:119],v[204:207],v[116:119]
T a[116:117],v25,50176
M v[88:91],a[120:123],v[200:203],v[88:91]
T a[118:119],v25,58368
M v[120:123],a[120:123],v[204:207],v[120:123]
T a[120:121],v26,50176
global_load_dwordx4 v[140:143], v16, s[94:95] offset:3072 sc1
s_add_u32 s94, s94, 4096
s_addc_u32 s95, s95, 0
M v[92:95],a[124:127],v[200:203],v[92:95]
T a[122:123],v26,58368
M v[124:127],a[124:127],v[204:207],v[124:127]
T a[124:125],v27,50176
T a[126:127],v27,58368
s_waitcnt lgkmcnt(8)
M v[64:67],a[96:99],v[208:211],v[64:67]
M v[96:99],a[96:99],v[212:215],v[96:99]
T a[96:97],v24,51200
global_load_dwordx4 v[144:147], v16, s[94:95] offset:0 sc1
M v[68:71],a[100:103],v[208:211],v[68:71]
T a[98:99],v24,59392
M v[100:103],a[100:103],v[212:215],v[100:103]
T a[100:101],v25,51200
M v[72:75],a[104:107],v[208:211],v[72:75]
T a[102:103],v25,59392
M v[104:107],a[104:107],v[212:215],v[104:107]
T a[104:105],v26,51200
global_load_dwordx4 v[148:151], v16, s[94:95] offset:1024 sc1
M v[76:79],a[108:111],v[208:211],v[76:79]
T a[106:107],v26,59392
M v[108:111],a[108:111],v[212:215],v[108:111]
T a[108:109],v27,51200
s_waitcnt lgkmcnt(7)
M v[80:83],a[112:115],v[208:211],v[80:83]
T a[110:111],v27,59392
M v[112:115],a[112:115],v[212:215],v[112:115]
T a[112:113],v24,52224
global_load_dwordx4 v[152:155], v16, s[94:95] offset:2048 sc1
M v[84:87],a[116:119],v[208:211],v[84:87]
T a[114:115],v24,60416
M v[116:119],a[116:119],v[212:215],v[116:119]
T a[116:117],v25,52224
M v[88:91],a[120:123],v[208:211],v[88:91]
T a[118:119],v25,60416
M v[120:123],a[120:123],v[212:215],v[120:123]
T a[120:121],v26,52224
global_load_dwordx4 v[156:159], v16, s[94:95] offset:3072 sc1
s_add_u32 s94, s94, 4096
s_addc_u32 s95, s95, 0
M v[92:95],a[124:127],v[208:211],v[92:95]
T a[122:123],v26,60416
M v[124:127],a[124:127],v[212:215],v[124:127]
T a[124:125],v27,52224
s_waitcnt vmcnt(17) lgkmcnt(7)
s_barrier
M v[64:67],a[96:99],v[216:219],v[64:67]
T a[126:127],v27,60416
M v[96:99],a[96:99],v[220:223],v[96:99]
T a[96:97],v28,32768
global_load_dwordx4 v[160:163], v16, s[94:95] offset:0 sc1
M v[68:71],a[100:103],v[216:219],v[68:71]
T a[98:99],v28,40960
M v[100:103],a[100:103],v[220:223],v[100:103]
T a[100:101],v29,32768
M v[72:75],a[104:107],v[216:219],v[72:75]
T a[102:103],v29,40960
M v[104:107],a[104:107],v[220:223],v[104:107]
T a[104:105],v30,32768
global_load_dwordx4 v[164:167], v16, s[94:95] offset:1024 sc1
M v[76:79],a[108:111],v[216:219],v[76:79]
T a[106:107],v30,40960
M v[108:111],a[108:111],v[220:223],v[108:111]
T a[108:109],v31,32768
s_waitcnt lgkmcnt(7)
M v[80:83],a[112:115],v[216:219],v[80:83]
T a[110:111],v31,40960
M v[112:115],a[112:115],v[220:223],v[112:115]
T a[112:113],v28,33792
global_load_dwordx4 v[168:171], v16, s[94:95] offset:2048 sc1
M v[84:87],a[116:119],v[216:219],v[84:87]
T a[114:115],v28,41984
M v[116:119],a[116:119],v[220:223],v[116:119]
T a[116:117],v29,33792
M v[88:91],a[120:123],v[216:219],v[88:91]
T a[118:119],v29,41984
M v[120:123],a[120:123],v[220:223],v[120:123]
T a[120:121],v30,33792
global_load_dwordx4 v[172:175], v16, s[94:95] offset:3072 sc1
s_add_u32 s94, s94, 4096
s_addc_u32 s95, s95, 0
M v[92:95],a[124:127],v[216:219],v[92:95]
T a[122:123],v30,41984
M v[124:127],a[124:127],v[220:223],v[124:127]
T a[124:125],v31,33792
T a[126:127],v31,41984
s_waitcnt lgkmcnt(8)
M v[64:67],a[96:99],v[224:227],v[64:67]
M v[96:99],a[96:99],v[228:231],v[96:99]
T a[96:97],v28,34816
global_load_dwordx4 v[176:179], v16, s[94:95] offset:0 sc1
M v[68:71],a[100:103],v[224:227],v[68:71]
T a[98:99],v28,43008
M v[100:103],a[100:103],v[228:231],v[100:103]
T a[100:101],v29,34816
M v[72:75],a[104:107],v[224:227],v[72:75]
T a[102:103],v29,43008
M v[104:107],a[104:107],v[228:231],v[104:107]
T a[104:105],v30,34816
global_load_dwordx4 v[180:183], v16, s[94:95] offset:1024 sc1
M v[76:79],a[108:111],v[224:227],v[76:79]
T a[106:107],v30,43008
M v[108:111],a[108:111],v[228:231],v[108:111]
T a[108:109],v31,34816
s_waitcnt lgkmcnt(7)
M v[80:83],a[112:115],v[224:227],v[80:83]
T a[110:111],v31,43008
M v[112:115],a[112:115],v[228:231],v[112:115]
T a[112:113],v28,35840
global_load_dwordx4 v[184:187], v16, s[94:95] offset:2048 sc1
M v[84:87],a[116:119],v[224:227],v[84:87]
T a[114:115],v28,44032
M v[116:119],a[116:119],v[228:231],v[116:119]
T a[116:117],v29,35840
M v[88:91],a[120:123],v[224:227],v[88:91]
T a[118:119],v29,44032
M v[120:123],a[120:123],v[228:231],v[120:123]
T a[120:121],v30,35840
global_load_dwordx4 v[188:191], v16, s[94:95] offset:3072 sc1
M v[92:95],a[124:127],v[224:227],v[92:95]
T a[122:123],v30,44032
M v[124:127],a[124:127],v[228:231],v[124:127]
T a[124:125],v31,35840
s_waitcnt vmcnt(17) lgkmcnt(7)
s_barrier
M v[64:67],a[96:99],v[232:235],v[64:67]
T a[126:127],v31,44032
M v[96:99],a[96:99],v[236:239],v[96:99]
T a[96:97],v28,0
M v[68:71],a[100:103],v[232:235],v[68:71]
T a[98:99],v28,8192
M v[100:103],a[100:103],v[236:239],v[100:103]
T a[100:101],v29,0
M v[72:75],a[104:107],v[232:235],v[72:75]
T a[102:103],v29,8192
M v[104:107],a[104:107],v[236:239],v[104:107]
T a[104:105],v30,0
M v[76:79],a[108:111],v[232:235],v[76:79]
T a[106:107],v30,8192
M v[108:111],a[108:111],v[236:239],v[108:111]
T a[108:109],v31,0
s_waitcnt lgkmcnt(7)
M v[80:83],a[112:115],v[232:235],v[80:83]
T a[110:111],v31,8192
M v[112:115],a[112:115],v[236:239],v[112:115]
T a[112:113],v28,1024
M v[84:87],a[116:119],v[232:235],v[84:87]
T a[114:115],v28,9216
M v[116:119],a[116:119],v[236:239],v[116:119]
T a[116:117],v29,1024
M v[88:91],a[120:123],v[232:235],v[88:91]
T a[118:119],v29,9216
M v[120:123],a[120:123],v[236:239],v[120:123]
T a[120:121],v30,1024
M v[92:95],a[124:127],v[232:235],v[92:95]
T a[122:123],v30,9216
M v[124:127],a[124:127],v[236:239],v[124:127]
T a[124:125],v31,1024
T a[126:127],v31,9216
s_waitcnt lgkmcnt(8)
M v[64:67],a[96:99],v[240:243],v[64:67]
M v[96:99],a[96:99],v[244:247],v[96:99]
T a[96:97],v28,2048
M v[68:71],a[100:103],v[240:243],v[68:71]
T a[98:99],v28,10240
M v[100:103],a[100:103],v[244:247],v[100:103]
T a[100:101],v29,2048
M v[72:75],a[104:107],v[240:243],v[72:75]
T a[102:103],v29,10240
M v[104:107],a[104:107],v[244:247],v[104:107]
T a[104:105],v30,2048
M v[76:79],a[108:111],v[240:243],v[76:79]
T a[106:107],v30,10240
M v[108:111],a[108:111],v[244:247],v[108:111]
T a[108:109],v31,2048
s_waitcnt lgkmcnt(7)
M v[80:83],a[112:115],v[240:243],v[80:83]
T a[110:111],v31,10240
M v[112:115],a[112:115],v[244:247],v[112:115]
T a[112:113],v28,3072
M v[84:87],a[116:119],v[240:243],v[84:87]
T a[114:115],v28,11264
M v[116:119],a[116:119],v[244:247],v[116:119]
T a[116:117],v29,3072
M v[88:91],a[120:123],v[240:243],v[88:91]
T a[118:119],v29,11264
M v[120:123],a[120:123],v[244:247],v[120:123]
T a[120:121],v30,3072
M v[92:95],a[124:127],v[240:243],v[92:95]
T a[122:123],v30,11264
M v[124:127],a[124:127],v[244:247],v[124:127]
T a[124:125],v31,3072
s_waitcnt lgkmcnt(7)
M v[64:67],a[96:99],v[248:251],v[64:67]
T a[126:127],v31,11264
M v[96:99],a[96:99],v[252:255],v[96:99]
M v[68:71],a[100:103],v[248:251],v[68:71]
M v[100:103],a[100:103],v[252:255],v[100:103]
M v[72:75],a[104:107],v[248:251],v[72:75]
M v[104:107],a[104:107],v[252:255],v[104:107]
M v[76:79],a[108:111],v[248:251],v[76:79]
M v[108:111],a[108:111],v[252:255],v[108:111]
s_waitcnt lgkmcnt(0)
M v[80:83],a[112:115],v[248:251],v[80:83]
M v[112:115],a[112:115],v[252:255],v[112:115]
M v[84:87],a[116:119],v[248:251],v[84:87]
M v[116:119],a[116:119],v[252:255],v[116:119]
M v[88:91],a[120:123],v[248:251],v[88:91]
M v[120:123],a[120:123],v[252:255],v[120:123]
M v[92:95],a[124:127],v[248:251],v[92:95]
M v[124:127],a[124:127],v[252:255],v[124:127]
s_waitcnt vmcnt(0)
s_nop 4
v_add_f32 v64, v64, v128
v_add_f32 v65, v65, v129
v_add_f32 v66, v66, v130
v_add_f32 v67, v67, v131
v_add_f32 v68, v68, v132
v_add_f32 v69, v69, v133
v_add_f32 v70, v70, v134
v_add_f32 v71, v71, v135
v_cvt_pk_bf16_f32 v128, v64, v65
v_cvt_pk_bf16_f32 v129, v66, v67
v_cvt_pk_bf16_f32 v130, v68, v69
v_cvt_pk_bf16_f32 v131, v70, v71
s_nop 0
v_permlane16_swap_b32 v128, v130
v_permlane16_swap_b32 v129, v131
global_store_dwordx4 v23, v[128:131], s[92:93] offset:0 sc1
v_add_f32 v72, v72, v136
v_add_f32 v73, v73, v137
v_add_f32 v74, v74, v138
v_add_f32 v75, v75, v139
v_add_f32 v76, v76, v140
v_add_f32 v77, v77, v141
v_add_f32 v78, v78, v142
v_add_f32 v79, v79, v143
v_cvt_pk_bf16_f32 v136, v72, v73
v_cvt_pk_bf16_f32 v137, v74, v75
v_cvt_pk_bf16_f32 v138, v76, v77
v_cvt_pk_bf16_f32 v139, v78, v79
s_nop 0
v_permlane16_swap_b32 v136, v138
v_permlane16_swap_b32 v137, v139
global_store_dwordx4 v23, v[136:139], s[92:93] offset:64 sc1
v_add_f32 v80, v80, v144
v_add_f32 v81, v81, v145
v_add_f32 v82, v82, v146
v_add_f32 v83, v83, v147
v_add_f32 v84, v84, v148
v_add_f32 v85, v85, v149
v_add_f32 v86, v86, v150
v_add_f32 v87, v87, v151
v_cvt_pk_bf16_f32 v144, v80, v81
v_cvt_pk_bf16_f32 v145, v82, v83
v_cvt_pk_bf16_f32 v146, v84, v85
v_cvt_pk_bf16_f32 v147, v86, v87
s_nop 0
v_permlane16_swap_b32 v144, v146
v_permlane16_swap_b32 v145, v147
global_store_dwordx4 v23, v[144:147], s[92:93] offset:128 sc1
v_add_f32 v88, v88, v152
v_add_f32 v89, v89, v153
v_add_f32 v90, v90, v154
v_add_f32 v91, v91, v155
v_add_f32 v92, v92, v156
v_add_f32 v93, v93, v157
v_add_f32 v94, v94, v158
v_add_f32 v95, v95, v159
v_cvt_pk_bf16_f32 v152, v88, v89
v_cvt_pk_bf16_f32 v153, v90, v91
v_cvt_pk_bf16_f32 v154, v92, v93
v_cvt_pk_bf16_f32 v155, v94, v95
s_nop 0
v_permlane16_swap_b32 v152, v154
v_permlane16_swap_b32 v153, v155
global_store_dwordx4 v23, v[152:155], s[92:93] offset:192 sc1
v_add_f32 v96, v96, v160
v_add_f32 v97, v97, v161
v_add_f32 v98, v98, v162
v_add_f32 v99, v99, v163
v_add_f32 v100, v100, v164
v_add_f32 v101, v101, v165
v_add_f32 v102, v102, v166
v_add_f32 v103, v103, v167
v_cvt_pk_bf16_f32 v160, v96, v97
v_cvt_pk_bf16_f32 v161, v98, v99
v_cvt_pk_bf16_f32 v162, v100, v101
v_cvt_pk_bf16_f32 v163, v102, v103
s_nop 0
v_permlane16_swap_b32 v160, v162
v_permlane16_swap_b32 v161, v163
global_store_dwordx4 v39, v[160:163], s[92:93] offset:0 sc1
v_add_f32 v104, v104, v168
v_add_f32 v105, v105, v169
v_add_f32 v106, v106, v170
v_add_f32 v107, v107, v171
v_add_f32 v108, v108, v172
v_add_f32 v109, v109, v173
v_add_f32 v110, v110, v174
v_add_f32 v111, v111, v175
v_cvt_pk_bf16_f32 v168, v104, v105
v_cvt_pk_bf16_f32 v169, v106, v107
v_cvt_pk_bf16_f32 v170, v108, v109
v_cvt_pk_bf16_f32 v171, v110, v111
s_nop 0
v_permlane16_swap_b32 v168, v170
v_permlane16_swap_b32 v169, v171
global_store_dwordx4 v39, v[168:171], s[92:93] offset:64 sc1
v_add_f32 v112, v112, v176
v_add_f32 v113, v113, v177
v_add_f32 v114, v114, v178
v_add_f32 v115, v115, v179
v_add_f32 v116, v116, v180
v_add_f32 v117, v117, v181
v_add_f32 v118, v118, v182
v_add_f32 v119, v119, v183
v_cvt_pk_bf16_f32 v176, v112, v113
v_cvt_pk_bf16_f32 v177, v114, v115
v_cvt_pk_bf16_f32 v178, v116, v117
v_cvt_pk_bf16_f32 v179, v118, v119
s_nop 0
v_permlane16_swap_b32 v176, v178
v_permlane16_swap_b32 v177, v179
global_store_dwordx4 v39, v[176:179], s[92:93] offset:128 sc1
v_add_f32 v120, v120, v184
v_add_f32 v121, v121, v185
v_add_f32 v122, v122, v186
v_add_f32 v123, v123, v187
v_add_f32 v124, v124, v188
v_add_f32 v125, v125, v189
v_add_f32 v126, v126, v190
v_add_f32 v127, v127, v191
v_cvt_pk_bf16_f32 v184, v120, v121
v_cvt_pk_bf16_f32 v185, v122, v123
v_cvt_pk_bf16_f32 v186, v124, v125
v_cvt_pk_bf16_f32 v187, v126, v127
s_nop 0
v_permlane16_swap_b32 v184, v186
v_permlane16_swap_b32 v185, v187
global_store_dwordx4 v39, v[184:187], s[92:93] offset:192 sc1
s_branch Lend_%=
LtypM_%=:
s_cmp_lt_u32 s99, 8192
s_cbranch_scc1 LrolLM_%=
s_nop 1
v_xor_b32 v32, 32, v9
s_sleep 40
global_load_dword v5, v38, s[8:9]
s_add_u32 s66, s8, 0
s_addc_u32 s67, s9, 0
v_writelane_b32 v38, s66, 0
v_writelane_b32 v38, s67, 16
s_mov_b32 s68, -1
v_writelane_b32 v38, s68, 32
s_add_u32 s66, s8, 1835008
s_addc_u32 s67, s9, 0
v_writelane_b32 v38, s66, 1
v_writelane_b32 v38, s67, 17
s_mov_b32 s68, -1
v_writelane_b32 v38, s68, 33
s_add_u32 s66, s8, 3670016
s_addc_u32 s67, s9, 0
v_writelane_b32 v38, s66, 2
v_writelane_b32 v38, s67, 18
s_mov_b32 s68, -1
v_writelane_b32 v38, s68, 34
s_add_u32 s66, s8, 5505024
s_addc_u32 s67, s9, 0
v_writelane_b32 v38, s66, 3
v_writelane_b32 v38, s67, 19
s_mov_b32 s68, -1
v_writelane_b32 v38, s68, 35
s_add_u32 s66, s8, 0
s_addc_u32 s67, s9, 0
v_writelane_b32 v38, s66, 4
v_writelane_b32 v38, s67, 20
s_mov_b32 s68, 0
v_writelane_b32 v38, s68, 36
s_add_u32 s66, s8, 1835008
s_addc_u32 s67, s9, 0
v_writelane_b32 v38, s66, 5
v_writelane_b32 v38, s67, 21
s_mov_b32 s68, 0
v_writelane_b32 v38, s68, 37
s_add_u32 s66, s8, 3670016
s_addc_u32 s67, s9, 0
v_writelane_b32 v38, s66, 6
v_writelane_b32 v38, s67, 22
s_mov_b32 s68, 0
v_writelane_b32 v38, s68, 38
s_add_u32 s66, s8, 5505024
s_addc_u32 s67, s9, 0
v_writelane_b32 v38, s66, 7
v_writelane_b32 v38, s67, 23
s_mov_b32 s68, 0
v_writelane_b32 v38, s68, 39
v_sub_u32 v4, v37, v36
s_cmp_eq_u32 s99, 12288
s_cbranch_scc1 LpolM_%=
s_waitcnt vmcnt(0)
v_readfirstlane_b32 s98, v28
s_nop 3
s_add_u32 s98, s98, 1
s_getpc_b64 s[66:67]
LpcM_%=:
s_sub_u32 s66, s66, LpcM_%=-Lcode0_%=
s_subb_u32 s67, s67, 0
v_min_u32 v39, Lcode1_%=-Lcode0_%=-128, v39
global_load_dword v5, v39, s[66:67]
s_branch LpjM_%=
LpolM_%=:
s_waitcnt vmcnt(0)
v_readfirstlane_b32 s98, v28
s_nop 3
s_add_u32 s98, s98, 1
v_lshrrev_b32 v24, 2, v16
v_and_b32 v25, 12, v24
s_mov_b32 s65, 0x100000
LplM_%=:
global_load_dword v26, v24, s[20:21] sc1
global_load_dword v27, v25, s[22:23] sc1
s_waitcnt vmcnt(0)
v_xor_b32 v26, s98, v26
v_xor_b32 v27, s98, v27
v_or_b32 v26, v26, v27
v_cmp_ne_u32 vcc, 0, v26
s_cbranch_vccz LplokM_%=
s_sub_u32 s65, s65, 1
s_cmp_lg_u32 s65, 0
s_cbranch_scc1 LplM_%=
LplokM_%=:
LpjM_%=:
v_add_u32 v12, 0x4000, v12
v_add_u32 v14, 229376, v14
v_add_u32 v15, 229376, v15
v_add_u32 v12, 0x4000, v12
v_add_u32 v14, 229376, v14
v_add_u32 v15, 229376, v15
v_add_u32 v12, 0x4000, v12
v_add_u32 v14, 229376, v14
v_add_u32 v15, 229376, v15
v_add_u32 v12, 0x4000, v12
v_add_u32 v14, 229376, v14
v_add_u32 v15, 229376, v15
s_barrier
s_nop 1
v_lshrrev_b32 v34, 2, v16
v_and_b32 v34, 124, v34
v_add_u32 v39, 32768, v23
s_and_b32 s72, s99, 4096
v_add_u32 v13, s72, v8
s_sub_u32 s72, 4096, s72
v_add_u32 v35, s72, v8
v_xor_b32 v37, 32, v10
s_barrier
X a[0:3],v13,0
X a[4:7],v13,1024
X a[8:11],v13,2048
X a[12:15],v13,3072
X a[16:19],v35,0
X a[20:23],v35,1024
X a[24:27],v35,2048
X a[28:31],v35,3072
T a[72:73],v9,0
T a[74:75],v9,6144
T a[76:77],v9,1024
T a[78:79],v9,7168
T a[80:81],v32,0
T a[82:83],v32,6144
T a[84:85],v32,1024
T a[86:87],v32,7168
T a[88:89],v10,0
T a[90:91],v10,3072
T a[92:93],v10,6144
T a[94:95],v10,9216
T a[96:97],v37,0
T a[98:99],v37,3072
s_waitcnt lgkmcnt(12)
M v[64:67],a[72:75],a[0:3],0
T a[100:101],v37,6144
M v[68:71],a[72:75],a[4:7],0
T a[102:103],v37,9216
M v[72:75],a[72:75],a[8:11],0
M v[76:79],a[72:75],a[12:15],0
M v[80:83],a[72:75],a[16:19],0
M v[84:87],a[72:75],a[20:23],0
M v[88:91],a[72:75],a[24:27],0
M v[92:95],a[72:75],a[28:31],0
s_waitcnt lgkmcnt(0)
s_barrier
M v[128:131],a[76:79],a[0:3],0
T a[72:73],v9,16384
M v[132:135],a[76:79],a[4:7],0
T a[74:75],v9,22528
M v[136:139],a[76:79],a[8:11],0
X a[32:35],v13,16384
M v[140:143],a[76:79],a[12:15],0
v_add_u32 v12, 0x4000, v12
M v[144:147],a[76:79],a[16:19],0
X a[36:39],v13,17408
v_readlane_b32 s66, v38, 0
M v[148:151],a[76:79],a[20:23],0
v_add_u32 v14, 229376, v14
M v[152:155],a[76:79],a[24:27],0
v_add_u32 v15, 229376, v15
v_readlane_b32 s67, v38, 16
M v[156:159],a[76:79],a[28:31],0
M v[96:99],a[80:83],a[0:3],0
T a[76:77],v9,17408
v_readlane_b32 s68, v38, 32
M v[100:103],a[80:83],a[4:7],0
T a[78:79],v9,23552
M v[104:107],a[80:83],a[8:11],0
X a[40:43],v13,18432
s_nop 1
v_and_b32 v3, s68, v4
M v[108:111],a[80:83],a[12:15],0
M v[112:115],a[80:83],a[16:19],0
X a[44:47],v13,19456
v_add_u32 v3, v36, v3
M v[116:119],a[80:83],a[20:23],0
M v[120:123],a[80:83],a[24:27],0
s_nop 4
global_load_dword v5, v3, s[66:67]
M v[124:127],a[80:83],a[28:31],0
M v[160:163],a[84:87],a[0:3],0
T a[80:81],v32,16384
M v[164:167],a[84:87],a[4:7],0
T a[82:83],v32,22528
M v[168:171],a[84:87],a[8:11],0
X a[48:51],v35,16384
M v[172:175],a[84:87],a[12:15],0
M v[176:179],a[84:87],a[16:19],0
M v[180:183],a[84:87],a[20:23],0
X a[52:55],v35,17408
M v[184:187],a[84:87],a[24:27],0
M v[188:191],a[84:87],a[28:31],0
M v[192:195],a[88:91],a[0:3],0
T a[84:85],v32,17408
M v[196:199],a[88:91],a[4:7],0
T a[86:87],v32,23552
M v[200:203],a[88:91],a[8:11],0
X a[56:59],v35,18432
M v[204:207],a[88:91],a[12:15],0
M v[224:227],a[92:95],a[0:3],0
T a[88:89],v10,16384
M v[228:231],a[92:95],a[4:7],0
T a[90:91],v10,19456
M v[232:235],a[92:95],a[8:11],0
X a[60:63],v35,19456
M v[236:239],a[92:95],a[12:15],0
M v[208:211],a[96:99],a[0:3],0
T a[92:93],v10,22528
M v[212:215],a[96:99],a[4:7],0
T a[94:95],v10,25600
M v[216:219],a[96:99],a[8:11],0
M v[220:223],a[96:99],a[12:15],0
M v[240:243],a[100:103],a[0:3],0
T a[96:97],v37,16384
M v[244:247],a[100:103],a[4:7],0
T a[98:99],v37,19456
M v[248:251],a[100:103],a[8:11],0
M v[252:255],a[100:103],a[12:15],0
s_waitcnt lgkmcnt(4)
M v[64:67],a[72:75],a[32:35],v[64:67]
T a[100:101],v37,22528
M v[68:71],a[72:75],a[36:39],v[68:71]
T a[102:103],v37,25600
M v[72:75],a[72:75],a[40:43],v[72:75]
M v[76:79],a[72:75],a[44:47],v[76:79]
global_load_dwordx2 v[24:25], v23, s[92:93] offset:0
M v[80:83],a[72:75],a[48:51],v[80:83]
M v[84:87],a[72:75],a[52:55],v[84:87]
M v[88:91],a[72:75],a[56:59],v[88:91]
M v[92:95],a[72:75],a[60:63],v[92:95]
s_waitcnt lgkmcnt(0)
s_barrier
M v[128:131],a[76:79],a[32:35],v[128:131]
T a[72:73],v9,32768
M v[132:135],a[76:79],a[36:39],v[132:135]
T a[74:75],v9,38912
M v[136:139],a[76:79],a[40:43],v[136:139]
X a[0:3],v13,32768
global_load_dwordx2 v[26:27], v23, s[92:93] offset:32
M v[140:143],a[76:79],a[44:47],v[140:143]
v_add_u32 v12, 0x4000, v12
M v[144:147],a[76:79],a[48:51],v[144:147]
X a[4:7],v13,33792
M v[148:151],a[76:79],a[52:55],v[148:151]
v_add_u32 v14, 229376, v14
M v[152:155],a[76:79],a[56:59],v[152:155]
v_add_u32 v15, 229376, v15
M v[156:159],a[76:79],a[60:63],v[156:159]
M v[96:99],a[80:83],a[32:35],v[96:99]
T a[76:77],v9,33792
M v[100:103],a[80:83],a[36:39],v[100:103]
T a[78:79],v9,39936
global_load_dwordx2 v[28:29], v23, s[92:93] offset:64
M v[104:107],a[80:83],a[40:43],v[104:107]
X a[8:11],v13,34816
M v[108:111],a[80:83],a[44:47],v[108:111]
M v[112:115],a[80:83],a[48:51],v[112:115]
X a[12:15],v13,35840
M v[116:119],a[80:83],a[52:55],v[116:119]
M v[120:123],a[80:83],a[56:59],v[120:123]
M v[124:127],a[80:83],a[60:63],v[124:127]
M v[160:163],a[84:87],a[32:35],v[160:163]
T a[80:81],v32,32768
M v[164:167],a[84:87],a[36:39],v[164:167]
T a[82:83],v32,38912
global_load_dwordx2 v[30:31], v23, s[92:93] offset:96
M v[168:171],a[84:87],a[40:43],v[168:171]
X a[16:19],v35,32768
M v[172:175],a[84:87],a[44:47],v[172:175]
M v[176:179],a[84:87],a[48:51],v[176:179]
M v[180:183],a[84:87],a[52:55],v[180:183]
X a[20:23],v35,33792
M v[184:187],a[84:87],a[56:59],v[184:187]
M v[188:191],a[84:87],a[60:63],v[188:191]
M v[192:195],a[88:91],a[32:35],v[192:195]
T a[84:85],v32,33792
global_load_dwordx2 v[40:41], v23, s[92:93] offset:128
M v[196:199],a[88:91],a[36:39],v[196:199]
T a[86:87],v32,39936
M v[200:203],a[88:91],a[40:43],v[200:203]
X a[24:27],v35,34816
M v[204:207],a[88:91],a[44:47],v[204:207]
M v[224:227],a[92:95],a[32:35],v[224:227]
T a[88:89],v10,32768
M v[228:231],a[92:95],a[36:39],v[228:231]
T a[90:91],v10,35840
M v[232:235],a[92:95],a[40:43],v[232:235]
X a[28:31],v35,35840
M v[236:239],a[92:95],a[44:47],v[236:239]
global_load_dwordx2 v[42:43], v23, s[92:93] offset:160
M v[208:211],a[96:99],a[32:35],v[208:211]
T a[92:93],v10,38912
M v[212:215],a[96:99],a[36:39],v[212:215]
T a[94:95],v10,41984
M v[216:219],a[96:99],a[40:43],v[216:219]
M v[220:223],a[96:99],a[44:47],v[220:223]
M v[240:243],a[100:103],a[32:35],v[240:243]
T a[96:97],v37,32768
M v[244:247],a[100:103],a[36:39],v[244:247]
T a[98:99],v37,35840
M v[248:251],a[100:103],a[40:43],v[248:251]
M v[252:255],a[100:103],a[44:47],v[252:255]
s_waitcnt lgkmcnt(4)
M v[64:67],a[72:75],a[0:3],v[64:67]
T a[100:101],v37,38912
M v[68:71],a[72:75],a[4:7],v[68:71]
T a[102:103],v37,41984
M v[72:75],a[72:75],a[8:11],v[72:75]
M v[76:79],a[72:75],a[12:15],v[76:79]
global_load_dwordx2 v[44:45], v23, s[92:93] offset:192
M v[80:83],a[72:75],a[16:19],v[80:83]
M v[84:87],a[72:75],a[20:23],v[84:87]
M v[88:91],a[72:75],a[24:27],v[88:91]
M v[92:95],a[72:75],a[28:31],v[92:95]
s_waitcnt lgkmcnt(0)
s_barrier
M v[128:131],a[76:79],a[0:3],v[128:131]
T a[72:73],v9,49152
M v[132:135],a[76:79],a[4:7],v[132:135]
T a[74:75],v9,55296
M v[136:139],a[76:79],a[8:11],v[136:139]
X a[32:35],v13,49152
M v[140:143],a[76:79],a[12:15],v[140:143]
v_add_u32 v12, 0x4000, v12
global_load_dwordx2 v[46:47], v23, s[92:93] offset:224
M v[144:147],a[76:79],a[16:19],v[144:147]
X a[36:39],v13,50176
v_readlane_b32 s66, v38, 1
M v[148:151],a[76:79],a[20:23],v[148:151]
v_add_u32 v14, 229376, v14
M v[152:155],a[76:79],a[24:27],v[152:155]
v_add_u32 v15, 229376, v15
v_readlane_b32 s67, v38, 17
M v[156:159],a[76:79],a[28:31],v[156:159]
M v[96:99],a[80:83],a[0:3],v[96:99]
T a[76:77],v9,50176
v_readlane_b32 s68, v38, 33
M v[100:103],a[80:83],a[4:7],v[100:103]
T a[78:79],v9,56320
M v[104:107],a[80:83],a[8:11],v[104:107]
X a[40:43],v13,51200
s_nop 1
v_and_b32 v3, s68, v4
M v[108:111],a[80:83],a[12:15],v[108:111]
M v[112:115],a[80:83],a[16:19],v[112:115]
X a[44:47],v13,52224
global_load_dwordx2 v[48:49], v39, s[92:93] offset:0
v_add_u32 v3, v36, v3
M v[116:119],a[80:83],a[20:23],v[116:119]
M v[120:123],a[80:83],a[24:27],v[120:123]
s_nop 4
global_load_dword v5, v3, s[66:67]
M v[124:127],a[80:83],a[28:31],v[124:127]
M v[160:163],a[84:87],a[0:3],v[160:163]
T a[80:81],v32,49152
M v[164:167],a[84:87],a[4:7],v[164:167]
T a[82:83],v32,55296
M v[168:171],a[84:87],a[8:11],v[168:171]
X a[48:51],v35,49152
M v[172:175],a[84:87],a[12:15],v[172:175]
M v[176:179],a[84:87],a[16:19],v[176:179]
M v[180:183],a[84:87],a[20:23],v[180:183]
X a[52:55],v35,50176
global_load_dwordx2 v[50:51], v39, s[92:93] offset:32
M v[184:187],a[84:87],a[24:27],v[184:187]
M v[188:191],a[84:87],a[28:31],v[188:191]
M v[192:195],a[88:91],a[0:3],v[192:195]
T a[84:85],v32,50176
M v[196:199],a[88:91],a[4:7],v[196:199]
T a[86:87],v32,56320
M v[200:203],a[88:91],a[8:11],v[200:203]
X a[56:59],v35,51200
M v[204:207],a[88:91],a[12:15],v[204:207]
M v[224:227],a[92:95],a[0:3],v[224:227]
T a[88:89],v10,49152
M v[228:231],a[92:95],a[4:7],v[228:231]
T a[90:91],v10,52224
M v[232:235],a[92:95],a[8:11],v[232:235]
X a[60:63],v35,52224
global_load_dwordx2 v[52:53], v39, s[92:93] offset:64
M v[236:239],a[92:95],a[12:15],v[236:239]
M v[208:211],a[96:99],a[0:3],v[208:211]
T a[92:93],v10,55296
M v[212:215],a[96:99],a[4:7],v[212:215]
T a[94:95],v10,58368
M v[216:219],a[96:99],a[8:11],v[216:219]
M v[220:223],a[96:99],a[12:15],v[220:223]
M v[240:243],a[100:103],a[0:3],v[240:243]
T a[96:97],v37,49152
M v[244:247],a[100:103],a[4:7],v[244:247]
T a[98:99],v37,52224
M v[248:251],a[100:103],a[8:11],v[248:251]
M v[252:255],a[100:103],a[12:15],v[252:255]
s_waitcnt lgkmcnt(4)
M v[64:67],a[72:75],a[32:35],v[64:67]
T a[100:101],v37,55296
M v[68:71],a[72:75],a[36:39],v[68:71]
T a[102:103],v37,58368
M v[72:75],a[72:75],a[40:43],v[72:75]
M v[76:79],a[72:75],a[44:47],v[76:79]
global_load_dwordx2 v[54:55], v39, s[92:93] offset:96
M v[80:83],a[72:75],a[48:51],v[80:83]
M v[84:87],a[72:75],a[52:55],v[84:87]
M v[88:91],a[72:75],a[56:59],v[88:91]
M v[92:95],a[72:75],a[60:63],v[92:95]
s_waitcnt lgkmcnt(0)
s_barrier
M v[128:131],a[76:79],a[32:35],v[128:131]
T a[72:73],v9,0
M v[132:135],a[76:79],a[36:39],v[132:135]
T a[74:75],v9,6144
M v[136:139],a[76:79],a[40:43],v[136:139]
X a[0:3],v13,0
M v[140:143],a[76:79],a[44:47],v[140:143]
v_add_u32 v12, 0x4000, v12
global_load_dwordx2 v[56:57], v39, s[92:93] offset:128
M v[144:147],a[76:79],a[48:51],v[144:147]
X a[4:7],v13,1024
M v[148:151],a[76:79],a[52:55],v[148:151]
v_add_u32 v14, 229376, v14
M v[152:155],a[76:79],a[56:59],v[152:155]
v_add_u32 v15, 229376, v15
M v[156:159],a[76:79],a[60:63],v[156:159]
M v[96:99],a[80:83],a[32:35],v[96:99]
T a[76:77],v9,1024
M v[100:103],a[80:83],a[36:39],v[100:103]
T a[78:79],v9,7168
M v[104:107],a[80:83],a[40:43],v[104:107]
X a[8:11],v13,2048
M v[108:111],a[80:83],a[44:47],v[108:111]
M v[112:115],a[80:83],a[48:51],v[112:115]
X a[12:15],v13,3072
global_load_dwordx2 v[58:59], v39, s[92:93] offset:160
M v[116:119],a[80:83],a[52:55],v[116:119]
M v[120:123],a[80:83],a[56:59],v[120:123]
M v[124:127],a[80:83],a[60:63],v[124:127]
M v[160:163],a[84:87],a[32:35],v[160:163]
T a[80:81],v32,0
M v[164:167],a[84:87],a[36:39],v[164:167]
T a[82:83],v32,6144
M v[168:171],a[84:87],a[40:43],v[168:171]
X a[16:19],v35,0
M v[172:175],a[84:87],a[44:47],v[172:175]
M v[176:179],a[84:87],a[48:51],v[176:179]
M v[180:183],a[84:87],a[52:55],v[180:183]
X a[20:23],v35,1024
global_load_dwordx2 v[60:61], v39, s[92:93] offset:192
M v[184:187],a[84:87],a[56:59],v[184:187]
M v[188:191],a[84:87],a[60:63],v[188:191]
M v[192:195],a[88:91],a[32:35],v[192:195]
T a[84:85],v32,1024
M v[196:199],a[88:91],a[36:39],v[196:199]
T a[86:87],v32,7168
M v[200:203],a[88:91],a[40:43],v[200:203]
X a[24:27],v35,2048
M v[204:207],a[88:91],a[44:47],v[204:207]
M v[224:227],a[92:95],a[32:35],v[224:227]
T a[88:89],v10,0
M v[228:231],a[92:95],a[36:39],v[228:231]
T a[90:91],v10,3072
M v[232:235],a[92:95],a[40:43],v[232:235]
X a[28:31],v35,3072
global_load_dwordx2 v[62:63], v39, s[92:93] offset:224
M v[236:239],a[92:95],a[44:47],v[236:239]
M v[208:211],a[96:99],a[32:35],v[208:211]
T a[92:93],v10,6144
M v[212:215],a[96:99],a[36:39],v[212:215]
T a[94:95],v10,9216
M v[216:219],a[96:99],a[40:43],v[216:219]
M v[220:223],a[96:99],a[44:47],v[220:223]
M v[240:243],a[100:103],a[32:35],v[240:243]
T a[96:97],v37,0
M v[244:247],a[100:103],a[36:39],v[244:247]
T a[98:99],v37,3072
M v[248:251],a[100:103],a[40:43],v[248:251]
M v[252:255],a[100:103],a[44:47],v[252:255]
s_mov_b32 s64, 6
Lp1MP_%=:
s_waitcnt lgkmcnt(4)
M v[64:67],a[72:75],a[0:3],v[64:67]
T a[100:101],v37,6144
M v[68:71],a[72:75],a[4:7],v[68:71]
T a[102:103],v37,9216
M v[72:75],a[72:75],a[8:11],v[72:75]
M v[76:79],a[72:75],a[12:15],v[76:79]
M v[80:83],a[72:75],a[16:19],v[80:83]
M v[84:87],a[72:75],a[20:23],v[84:87]
M v[88:91],a[72:75],a[24:27],v[88:91]
M v[92:95],a[72:75],a[28:31],v[92:95]
s_waitcnt lgkmcnt(0)
s_barrier
M v[128:131],a[76:79],a[0:3],v[128:131]
T a[72:73],v9,16384
M v[132:135],a[76:79],a[4:7],v[132:135]
T a[74:75],v9,22528
M v[136:139],a[76:79],a[8:11],v[136:139]
X a[32:35],v13,16384
M v[140:143],a[76:79],a[12:15],v[140:143]
v_add_u32 v12, 0x4000, v12
M v[144:147],a[76:79],a[16:19],v[144:147]
X a[36:39],v13,17408
M v[148:151],a[76:79],a[20:23],v[148:151]
v_add_u32 v14, 229376, v14
M v[152:155],a[76:79],a[24:27],v[152:155]
v_add_u32 v15, 229376, v15
M v[156:159],a[76:79],a[28:31],v[156:159]
M v[96:99],a[80:83],a[0:3],v[96:99]
T a[76:77],v9,17408
M v[100:103],a[80:83],a[4:7],v[100:103]
T a[78:79],v9,23552
M v[104:107],a[80:83],a[8:11],v[104:107]
X a[40:43],v13,18432
M v[108:111],a[80:83],a[12:15],v[108:111]
M v[112:115],a[80:83],a[16:19],v[112:115]
X a[44:47],v13,19456
M v[116:119],a[80:83],a[20:23],v[116:119]
M v[120:123],a[80:83],a[24:27],v[120:123]
M v[124:127],a[80:83],a[28:31],v[124:127]
M v[160:163],a[84:87],a[0:3],v[160:163]
T a[80:81],v32,16384
M v[164:167],a[84:87],a[4:7],v[164:167]
T a[82:83],v32,22528
M v[168:171],a[84:87],a[8:11],v[168:171]
X a[48:51],v35,16384
M v[172:175],a[84:87],a[12:15],v[172:175]
M v[176:179],a[84:87],a[16:19],v[176:179]
M v[180:183],a[84:87],a[20:23],v[180:183]
X a[52:55],v35,17408
M v[184:187],a[84:87],a[24:27],v[184:187]
M v[188:191],a[84:87],a[28:31],v[188:191]
M v[192:195],a[88:91],a[0:3],v[192:195]
T a[84:85],v32,17408
M v[196:199],a[88:91],a[4:7],v[196:199]
T a[86:87],v32,23552
M v[200:203],a[88:91],a[8:11],v[200:203]
X a[56:59],v35,18432
M v[204:207],a[88:91],a[12:15],v[204:207]
M v[224:227],a[92:95],a[0:3],v[224:227]
T a[88:89],v10,16384
M v[228:231],a[92:95],a[4:7],v[228:231]
T a[90:91],v10,19456
M v[232:235],a[92:95],a[8:11],v[232:235]
X a[60:63],v35,19456
M v[236:239],a[92:95],a[12:15],v[236:239]
M v[208:211],a[96:99],a[0:3],v[208:211]
T a[92:93],v10,22528
M v[212:215],a[96:99],a[4:7],v[212:215]
T a[94:95],v10,25600
M v[216:219],a[96:99],a[8:11],v[216:219]
M v[220:223],a[96:99],a[12:15],v[220:223]
M v[240:243],a[100:103],a[0:3],v[240:243]
T a[96:97],v37,16384
M v[244:247],a[100:103],a[4:7],v[244:247]
T a[98:99],v37,19456
M v[248:251],a[100:103],a[8:11],v[248:251]
M v[252:255],a[100:103],a[12:15],v[252:255]
s_waitcnt lgkmcnt(4)
M v[64:67],a[72:75],a[32:35],v[64:67]
T a[100:101],v37,22528
M v[68:71],a[72:75],a[36:39],v[68:71]
T a[102:103],v37,25600
M v[72:75],a[72:75],a[40:43],v[72:75]
M v[76:79],a[72:75],a[44:47],v[76:79]
M v[80:83],a[72:75],a[48:51],v[80:83]
M v[84:87],a[72:75],a[52:55],v[84:87]
M v[88:91],a[72:75],a[56:59],v[88:91]
M v[92:95],a[72:75],a[60:63],v[92:95]
s_waitcnt lgkmcnt(0)
s_barrier
M v[128:131],a[76:79],a[32:35],v[128:131]
T a[72:73],v9,32768
M v[132:135],a[76:79],a[36:39],v[132:135]
T a[74:75],v9,38912
M v[136:139],a[76:79],a[40:43],v[136:139]
X a[0:3],v13,32768
M v[140:143],a[76:79],a[44:47],v[140:143]
v_add_u32 v12, 0x4000, v12
M v[144:147],a[76:79],a[48:51],v[144:147]
X a[4:7],v13,33792
M v[148:151],a[76:79],a[52:55],v[148:151]
v_add_u32 v14, 229376, v14
M v[152:155],a[76:79],a[56:59],v[152:155]
v_add_u32 v15, 229376, v15
M v[156:159],a[76:79],a[60:63],v[156:159]
M v[96:99],a[80:83],a[32:35],v[96:99]
T a[76:77],v9,33792
M v[100:103],a[80:83],a[36:39],v[100:103]
T a[78:79],v9,39936
M v[104:107],a[80:83],a[40:43],v[104:107]
X a[8:11],v13,34816
M v[108:111],a[80:83],a[44:47],v[108:111]
M v[112:115],a[80:83],a[48:51],v[112:115]
X a[12:15],v13,35840
M v[116:119],a[80:83],a[52:55],v[116:119]
M v[120:123],a[80:83],a[56:59],v[120:123]
M v[124:127],a[80:83],a[60:63],v[124:127]
M v[160:163],a[84:87],a[32:35],v[160:163]
T a[80:81],v32,32768
M v[164:167],a[84:87],a[36:39],v[164:167]
T a[82:83],v32,38912
M v[168:171],a[84:87],a[40:43],v[168:171]
X a[16:19],v35,32768
M v[172:175],a[84:87],a[44:47],v[172:175]
M v[176:179],a[84:87],a[48:51],v[176:179]
M v[180:183],a[84:87],a[52:55],v[180:183]
X a[20:23],v35,33792
M v[184:187],a[84:87],a[56:59],v[184:187]
M v[188:191],a[84:87],a[60:63],v[188:191]
M v[192:195],a[88:91],a[32:35],v[192:195]
T a[84:85],v32,33792
M v[196:199],a[88:91],a[36:39],v[196:199]
T a[86:87],v32,39936
M v[200:203],a[88:91],a[40:43],v[200:203]
X a[24:27],v35,34816
M v[204:207],a[88:91],a[44:47],v[204:207]
M v[224:227],a[92:95],a[32:35],v[224:227]
T a[88:89],v10,32768
M v[228:231],a[92:95],a[36:39],v[228:231]
T a[90:91],v10,35840
M v[232:235],a[92:95],a[40:43],v[232:235]
X a[28:31],v35,35840
M v[236:239],a[92:95],a[44:47],v[236:239]
M v[208:211],a[96:99],a[32:35],v[208:211]
T a[92:93],v10,38912
M v[212:215],a[96:99],a[36:39],v[212:215]
T a[94:95],v10,41984
M v[216:219],a[96:99],a[40:43],v[216:219]
M v[220:223],a[96:99],a[44:47],v[220:223]
M v[240:243],a[100:103],a[32:35],v[240:243]
T a[96:97],v37,32768
M v[244:247],a[100:103],a[36:39],v[244:247]
T a[98:99],v37,35840
M v[248:251],a[100:103],a[40:43],v[248:251]
M v[252:255],a[100:103],a[44:47],v[252:255]
s_waitcnt lgkmcnt(4)
M v[64:67],a[72:75],a[0:3],v[64:67]
T a[100:101],v37,38912
M v[68:71],a[72:75],a[4:7],v[68:71]
T a[102:103],v37,41984
M v[72:75],a[72:75],a[8:11],v[72:75]
M v[76:79],a[72:75],a[12:15],v[76:79]
M v[80:83],a[72:75],a[16:19],v[80:83]
M v[84:87],a[72:75],a[20:23],v[84:87]
M v[88:91],a[72:75],a[24:27],v[88:91]
M v[92:95],a[72:75],a[28:31],v[92:95]
s_waitcnt lgkmcnt(0)
s_barrier
M v[128:131],a[76:79],a[0:3],v[128:131]
T a[72:73],v9,49152
M v[132:135],a[76:79],a[4:7],v[132:135]
T a[74:75],v9,55296
M v[136:139],a[76:79],a[8:11],v[136:139]
X a[32:35],v13,49152
M v[140:143],a[76:79],a[12:15],v[140:143]
v_add_u32 v12, 0x4000, v12
M v[144:147],a[76:79],a[16:19],v[144:147]
X a[36:39],v13,50176
s_sub_u32 s65, 8, s64
M v[148:151],a[76:79],a[20:23],v[148:151]
v_add_u32 v14, 229376, v14
M v[152:155],a[76:79],a[24:27],v[152:155]
v_add_u32 v15, 229376, v15
v_readlane_b32 s66, v38, s65
M v[156:159],a[76:79],a[28:31],v[156:159]
M v[96:99],a[80:83],a[0:3],v[96:99]
T a[76:77],v9,50176
s_add_u32 s65, s65, 16
M v[100:103],a[80:83],a[4:7],v[100:103]
T a[78:79],v9,56320
M v[104:107],a[80:83],a[8:11],v[104:107]
X a[40:43],v13,51200
v_readlane_b32 s67, v38, s65
M v[108:111],a[80:83],a[12:15],v[108:111]
M v[112:115],a[80:83],a[16:19],v[112:115]
X a[44:47],v13,52224
s_add_u32 s65, s65, 16
M v[116:119],a[80:83],a[20:23],v[116:119]
M v[120:123],a[80:83],a[24:27],v[120:123]
v_readlane_b32 s68, v38, s65
M v[124:127],a[80:83],a[28:31],v[124:127]
M v[160:163],a[84:87],a[0:3],v[160:163]
T a[80:81],v32,49152
s_nop 1
v_and_b32 v3, s68, v4
M v[164:167],a[84:87],a[4:7],v[164:167]
T a[82:83],v32,55296
M v[168:171],a[84:87],a[8:11],v[168:171]
X a[48:51],v35,49152
v_add_u32 v3, v36, v3
M v[172:175],a[84:87],a[12:15],v[172:175]
M v[176:179],a[84:87],a[16:19],v[176:179]
s_nop 4
global_load_dword v5, v3, s[66:67]
M v[180:183],a[84:87],a[20:23],v[180:183]
X a[52:55],v35,50176
M v[184:187],a[84:87],a[24:27],v[184:187]
M v[188:191],a[84:87],a[28:31],v[188:191]
M v[192:195],a[88:91],a[0:3],v[192:195]
T a[84:85],v32,50176
M v[196:199],a[88:91],a[4:7],v[196:199]
T a[86:87],v32,56320
M v[200:203],a[88:91],a[8:11],v[200:203]
X a[56:59],v35,51200
M v[204:207],a[88:91],a[12:15],v[204:207]
M v[224:227],a[92:95],a[0:3],v[224:227]
T a[88:89],v10,49152
M v[228:231],a[92:95],a[4:7],v[228:231]
T a[90:91],v10,52224
M v[232:235],a[92:95],a[8:11],v[232:235]
X a[60:63],v35,52224
M v[236:239],a[92:95],a[12:15],v[236:239]
M v[208:211],a[96:99],a[0:3],v[208:211]
T a[92:93],v10,55296
M v[212:215],a[96:99],a[4:7],v[212:215]
T a[94:95],v10,58368
M v[216:219],a[96:99],a[8:11],v[216:219]
M v[220:223],a[96:99],a[12:15],v[220:223]
M v[240:243],a[100:103],a[0:3],v[240:243]
T a[96:97],v37,49152
M v[244:247],a[100:103],a[4:7],v[244:247]
T a[98:99],v37,52224
M v[248:251],a[100:103],a[8:11],v[248:251]
M v[252:255],a[100:103],a[12:15],v[252:255]
s_waitcnt lgkmcnt(4)
M v[64:67],a[72:75],a[32:35],v[64:67]
T a[100:101],v37,55296
M v[68:71],a[72:75],a[36:39],v[68:71]
T a[102:103],v37,58368
M v[72:75],a[72:75],a[40:43],v[72:75]
M v[76:79],a[72:75],a[44:47],v[76:79]
M v[80:83],a[72:75],a[48:51],v[80:83]
M v[84:87],a[72:75],a[52:55],v[84:87]
M v[88:91],a[72:75],a[56:59],v[88:91]
M v[92:95],a[72:75],a[60:63],v[92:95]
s_waitcnt lgkmcnt(0)
s_barrier
M v[128:131],a[76:79],a[32:35],v[128:131]
T a[72:73],v9,0
M v[132:135],a[76:79],a[36:39],v[132:135]
T a[74:75],v9,6144
M v[136:139],a[76:79],a[40:43],v[136:139]
X a[0:3],v13,0
M v[140:143],a[76:79],a[44:47],v[140:143]
v_add_u32 v12, 0x4000, v12
M v[144:147],a[76:79],a[48:51],v[144:147]
X a[4:7],v13,1024
M v[148:151],a[76:79],a[52:55],v[148:151]
v_add_u32 v14, 229376, v14
M v[152:155],a[76:79],a[56:59],v[152:155]
v_add_u32 v15, 229376, v15
M v[156:159],a[76:79],a[60:63],v[156:159]
M v[96:99],a[80:83],a[32:35],v[96:99]
T a[76:77],v9,1024
M v[100:103],a[80:83],a[36:39],v[100:103]
T a[78:79],v9,7168
M v[104:107],a[80:83],a[40:43],v[104:107]
X a[8:11],v13,2048
M v[108:111],a[80:83],a[44:47],v[108:111]
M v[112:115],a[80:83],a[48:51],v[112:115]
X a[12:15],v13,3072
M v[116:119],a[80:83],a[52:55],v[116:119]
M v[120:123],a[80:83],a[56:59],v[120:123]
M v[124:127],a[80:83],a[60:63],v[124:127]
M v[160:163],a[84:87],a[32:35],v[160:163]
T a[80:81],v32,0
M v[164:167],a[84:87],a[36:39],v[164:167]
T a[82:83],v32,6144
M v[168:171],a[84:87],a[40:43],v[168:171]
X a[16:19],v35,0
M v[172:175],a[84:87],a[44:47],v[172:175]
M v[176:179],a[84:87],a[48:51],v[176:179]
M v[180:183],a[84:87],a[52:55],v[180:183]
X a[20:23],v35,1024
M v[184:187],a[84:87],a[56:59],v[184:187]
M v[188:191],a[84:87],a[60:63],v[188:191]
M v[192:195],a[88:91],a[32:35],v[192:195]
T a[84:85],v32,1024
M v[196:199],a[88:91],a[36:39],v[196:199]
T a[86:87],v32,7168
M v[200:203],a[88:91],a[40:43],v[200:203]
X a[24:27],v35,2048
M v[204:207],a[88:91],a[44:47],v[204:207]
M v[224:227],a[92:95],a[32:35],v[224:227]
T a[88:89],v10,0
M v[228:231],a[92:95],a[36:39],v[228:231]
T a[90:91],v10,3072
M v[232:235],a[92:95],a[40:43],v[232:235]
X a[28:31],v35,3072
M v[236:239],a[92:95],a[44:47],v[236:239]
M v[208:211],a[96:99],a[32:35],v[208:211]
T a[92:93],v10,6144
M v[212:215],a[96:99],a[36:39],v[212:215]
T a[94:95],v10,9216
M v[216:219],a[96:99],a[40:43],v[216:219]
M v[220:223],a[96:99],a[44:47],v[220:223]
M v[240:243],a[100:103],a[32:35],v[240:243]
T a[96:97],v37,0
M v[244:247],a[100:103],a[36:39],v[244:247]
T a[98:99],v37,3072
s_sub_u32 s64, s64, 1
M v[248:251],a[100:103],a[40:43],v[248:251]
s_cmp_lg_u32 s64, 0
M v[252:255],a[100:103],a[44:47],v[252:255]
s_cbranch_scc1 Lp1MP_%=
s_branch LmrgM_%=
LrolLM_%=:
s_nop 1
v_xor_b32 v32, 32, v9
v_readfirstlane_b32 s98, v28
s_nop 3
s_add_u32 s98, s98, 1
s_add_u32 m0, s55, 0
s_nop 0
global_load_lds_dwordx4 v14, s[44:45] offset:0
global_load_lds_dwordx4 v14, s[46:47] offset:1024
global_load_lds_dwordx4 v15, s[48:49] offset:2048
s_add_u32 m0, s55, 6144
s_nop 0
global_load_lds_dwordx4 v14, s[10:11] offset:0
global_load_lds_dwordx4 v14, s[12:13] offset:1024
global_load_lds_dwordx4 v15, s[14:15] offset:2048
v_add_u32 v14, 229376, v14
v_add_u32 v15, 229376, v15
s_add_u32 m0, s55, 16384
s_nop 0
global_load_lds_dwordx4 v14, s[44:45] offset:0
global_load_lds_dwordx4 v14, s[46:47] offset:1024
global_load_lds_dwordx4 v15, s[48:49] offset:2048
s_add_u32 m0, s55, 22528
s_nop 0
global_load_lds_dwordx4 v14, s[10:11] offset:0
global_load_lds_dwordx4 v14, s[12:13] offset:1024
global_load_lds_dwordx4 v15, s[14:15] offset:2048
v_add_u32 v14, 229376, v14
v_add_u32 v15, 229376, v15
s_add_u32 m0, s55, 32768
s_nop 0
global_load_lds_dwordx4 v14, s[44:45] offset:0
global_load_lds_dwordx4 v14, s[46:47] offset:1024
global_load_lds_dwordx4 v15, s[48:49] offset:2048
s_add_u32 m0, s55, 38912
s_nop 0
global_load_lds_dwordx4 v14, s[10:11] offset:0
global_load_lds_dwordx4 v14, s[12:13] offset:1024
global_load_lds_dwordx4 v15, s[14:15] offset:2048
v_add_u32 v14, 229376, v14
v_add_u32 v15, 229376, v15
s_add_u32 m0, s55, 49152
s_nop 0
global_load_lds_dwordx4 v14, s[44:45] offset:0
global_load_lds_dwordx4 v14, s[46:47] offset:1024
global_load_lds_dwordx4 v15, s[48:49] offset:2048
s_add_u32 m0, s55, 55296
s_nop 0
global_load_lds_dwordx4 v14, s[10:11] offset:0
global_load_lds_dwordx4 v14, s[12:13] offset:1024
global_load_lds_dwordx4 v15, s[14:15] offset:2048
v_add_u32 v14, 229376, v14
v_add_u32 v15, 229376, v15
s_barrier
s_add_u32 m0, s54, 0
s_nop 0
global_load_lds_dwordx4 v12, s[40:41] offset:0 sc1
global_load_lds_dwordx4 v12, s[40:41] offset:1024 sc1
global_load_lds_dwordx4 v12, s[40:41] offset:2048 sc1
global_load_lds_dwordx4 v12, s[40:41] offset:3072 sc1
s_add_u32 m0, s54, 8192
s_nop 0
global_load_lds_dwordx4 v12, s[8:9] offset:0 sc1
global_load_lds_dwordx4 v12, s[8:9] offset:1024 sc1
global_load_lds_dwordx4 v12, s[8:9] offset:2048 sc1
global_load_lds_dwordx4 v12, s[8:9] offset:3072 sc1
v_add_u32 v12, 0x4000, v12
s_add_u32 m0, s54, 16384
s_nop 0
global_load_lds_dwordx4 v12, s[40:41] offset:0 sc1
global_load_lds_dwordx4 v12, s[40:41] offset:1024 sc1
global_load_lds_dwordx4 v12, s[40:41] offset:2048 sc1
global_load_lds_dwordx4 v12, s[40:41] offset:3072 sc1
s_add_u32 m0, s54, 24576
s_nop 0
global_load_lds_dwordx4 v12, s[8:9] offset:0 sc1
global_load_lds_dwordx4 v12, s[8:9] offset:1024 sc1
global_load_lds_dwordx4 v12, s[8:9] offset:2048 sc1
global_load_lds_dwordx4 v12, s[8:9] offset:3072 sc1
v_add_u32 v12, 0x4000, v12
s_add_u32 m0, s54, 32768
s_nop 0
global_load_lds_dwordx4 v12, s[40:41] offset:0 sc1
global_load_lds_dwordx4 v12, s[40:41] offset:1024 sc1
global_load_lds_dwordx4 v12, s[40:41] offset:2048 sc1
global_load_lds_dwordx4 v12, s[40:41] offset:3072 sc1
s_add_u32 m0, s54, 40960
s_nop 0
global_load_lds_dwordx4 v12, s[8:9] offset:0 sc1
global_load_lds_dwordx4 v12, s[8:9] offset:1024 sc1
global_load_lds_dwordx4 v12, s[8:9] offset:2048 sc1
global_load_lds_dwordx4 v12, s[8:9] offset:3072 sc1
v_add_u32 v12, 0x4000, v12
s_add_u32 m0, s54, 49152
s_nop 0
global_load_lds_dwordx4 v12, s[40:41] offset:0 sc1
global_load_lds_dwordx4 v12, s[40:41] offset:1024 sc1
global_load_lds_dwordx4 v12, s[40:41] offset:2048 sc1
global_load_lds_dwordx4 v12, s[40:41] offset:3072 sc1
s_add_u32 m0, s54, 57344
s_nop 0
global_load_lds_dwordx4 v12, s[8:9] offset:0 sc1
global_load_lds_dwordx4 v12, s[8:9] offset:1024 sc1
global_load_lds_dwordx4 v12, s[8:9] offset:2048 sc1
global_load_lds_dwordx4 v12, s[8:9] offset:3072 sc1
v_add_u32 v12, 0x4000, v12
s_nop 1
v_lshrrev_b32 v34, 2, v16
v_and_b32 v34, 124, v34
v_add_u32 v39, 32768, v23
s_and_b32 s72, s99, 4096
v_add_u32 v13, s72, v8
s_sub_u32 s72, 4096, s72
v_add_u32 v35, s72, v8
v_xor_b32 v37, 32, v10
s_waitcnt vmcnt(24)
s_barrier
X a[0:3],v13,0
X a[4:7],v13,1024
X a[8:11],v13,2048
X a[12:15],v13,3072
X a[16:19],v35,0
X a[20:23],v35,1024
X a[24:27],v35,2048
X a[28:31],v35,3072
T a[72:73],v9,0
T a[74:75],v9,6144
T a[76:77],v9,1024
T a[78:79],v9,7168
T a[80:81],v32,0
T a[82:83],v32,6144
T a[84:85],v32,1024
T a[86:87],v32,7168
T a[88:89],v10,0
T a[90:91],v10,3072
T a[92:93],v10,6144
T a[94:95],v10,9216
T a[96:97],v37,0
T a[98:99],v37,3072
s_waitcnt lgkmcnt(12)
M v[64:67],a[72:75],a[0:3],0
T a[100:101],v37,6144
M v[68:71],a[72:75],a[4:7],0
T a[102:103],v37,9216
M v[72:75],a[72:75],a[8:11],0
M v[76:79],a[72:75],a[12:15],0
M v[80:83],a[72:75],a[16:19],0
M v[84:87],a[72:75],a[20:23],0
M v[88:91],a[72:75],a[24:27],0
M v[92:95],a[72:75],a[28:31],0
s_waitcnt vmcnt(16) lgkmcnt(0)
s_barrier
M v[128:131],a[76:79],a[0:3],0
T a[72:73],v9,16384
s_add_u32 m0, s54, 0
M v[132:135],a[76:79],a[4:7],0
T a[74:75],v9,22528
global_load_lds_dwordx4 v12, s[40:41] offset:0 sc1
M v[136:139],a[76:79],a[8:11],0
X a[32:35],v13,16384
M v[140:143],a[76:79],a[12:15],0
global_load_lds_dwordx4 v12, s[40:41] offset:1024 sc1
M v[144:147],a[76:79],a[16:19],0
X a[36:39],v13,17408
M v[148:151],a[76:79],a[20:23],0
M v[152:155],a[76:79],a[24:27],0
global_load_lds_dwordx4 v12, s[40:41] offset:2048 sc1
M v[156:159],a[76:79],a[28:31],0
M v[96:99],a[80:83],a[0:3],0
T a[76:77],v9,17408
M v[100:103],a[80:83],a[4:7],0
T a[78:79],v9,23552
M v[104:107],a[80:83],a[8:11],0
X a[40:43],v13,18432
M v[108:111],a[80:83],a[12:15],0
global_load_lds_dwordx4 v12, s[40:41] offset:3072 sc1
M v[112:115],a[80:83],a[16:19],0
X a[44:47],v13,19456
M v[116:119],a[80:83],a[20:23],0
s_add_u32 m0, s54, 8192
M v[120:123],a[80:83],a[24:27],0
global_load_lds_dwordx4 v12, s[8:9] offset:0 sc1
M v[124:127],a[80:83],a[28:31],0
global_load_lds_dwordx4 v12, s[8:9] offset:1024 sc1
M v[160:163],a[84:87],a[0:3],0
T a[80:81],v32,16384
M v[164:167],a[84:87],a[4:7],0
T a[82:83],v32,22528
M v[168:171],a[84:87],a[8:11],0
X a[48:51],v35,16384
M v[172:175],a[84:87],a[12:15],0
global_load_lds_dwordx4 v12, s[8:9] offset:2048 sc1
M v[176:179],a[84:87],a[16:19],0
global_load_lds_dwordx4 v12, s[8:9] offset:3072 sc1
M v[180:183],a[84:87],a[20:23],0
X a[52:55],v35,17408
M v[184:187],a[84:87],a[24:27],0
v_add_u32 v12, 0x4000, v12
M v[188:191],a[84:87],a[28:31],0
s_add_u32 m0, s55, 0
M v[192:195],a[88:91],a[0:3],0
T a[84:85],v32,17408
M v[196:199],a[88:91],a[4:7],0
T a[86:87],v32,23552
M v[200:203],a[88:91],a[8:11],0
X a[56:59],v35,18432
M v[204:207],a[88:91],a[12:15],0
global_load_lds_dwordx4 v14, s[44:45] offset:0
M v[224:227],a[92:95],a[0:3],0
T a[88:89],v10,16384
M v[228:231],a[92:95],a[4:7],0
T a[90:91],v10,19456
M v[232:235],a[92:95],a[8:11],0
X a[60:63],v35,19456
M v[236:239],a[92:95],a[12:15],0
global_load_lds_dwordx4 v14, s[46:47] offset:1024
M v[208:211],a[96:99],a[0:3],0
T a[92:93],v10,22528
M v[212:215],a[96:99],a[4:7],0
T a[94:95],v10,25600
M v[216:219],a[96:99],a[8:11],0
global_load_lds_dwordx4 v15, s[48:49] offset:2048
M v[220:223],a[96:99],a[12:15],0
s_add_u32 m0, s55, 6144
M v[240:243],a[100:103],a[0:3],0
T a[96:97],v37,16384
M v[244:247],a[100:103],a[4:7],0
T a[98:99],v37,19456
M v[248:251],a[100:103],a[8:11],0
global_load_lds_dwordx4 v14, s[10:11] offset:0
M v[252:255],a[100:103],a[12:15],0
global_load_lds_dwordx4 v14, s[12:13] offset:1024
global_load_lds_dwordx4 v15, s[14:15] offset:2048
v_add_u32 v14, 229376, v14
v_add_u32 v15, 229376, v15
s_waitcnt lgkmcnt(4)
M v[64:67],a[72:75],a[32:35],v[64:67]
T a[100:101],v37,22528
M v[68:71],a[72:75],a[36:39],v[68:71]
T a[102:103],v37,25600
M v[72:75],a[72:75],a[40:43],v[72:75]
M v[76:79],a[72:75],a[44:47],v[76:79]
global_load_dwordx2 v[24:25], v23, s[92:93] offset:0
M v[80:83],a[72:75],a[48:51],v[80:83]
M v[84:87],a[72:75],a[52:55],v[84:87]
M v[88:91],a[72:75],a[56:59],v[88:91]
M v[92:95],a[72:75],a[60:63],v[92:95]
s_waitcnt vmcnt(23) lgkmcnt(0)
s_barrier
M v[128:131],a[76:79],a[32:35],v[128:131]
T a[72:73],v9,32768
s_add_u32 m0, s54, 16384
M v[132:135],a[76:79],a[36:39],v[132:135]
T a[74:75],v9,38912
global_load_lds_dwordx4 v12, s[40:41] offset:0 sc1
M v[136:139],a[76:79],a[40:43],v[136:139]
X a[0:3],v13,32768
global_load_dwordx2 v[26:27], v23, s[92:93] offset:32
M v[140:143],a[76:79],a[44:47],v[140:143]
global_load_lds_dwordx4 v12, s[40:41] offset:1024 sc1
M v[144:147],a[76:79],a[48:51],v[144:147]
X a[4:7],v13,33792
M v[148:151],a[76:79],a[52:55],v[148:151]
M v[152:155],a[76:79],a[56:59],v[152:155]
global_load_lds_dwordx4 v12, s[40:41] offset:2048 sc1
M v[156:159],a[76:79],a[60:63],v[156:159]
M v[96:99],a[80:83],a[32:35],v[96:99]
T a[76:77],v9,33792
M v[100:103],a[80:83],a[36:39],v[100:103]
T a[78:79],v9,39936
global_load_dwordx2 v[28:29], v23, s[92:93] offset:64
M v[104:107],a[80:83],a[40:43],v[104:107]
X a[8:11],v13,34816
M v[108:111],a[80:83],a[44:47],v[108:111]
global_load_lds_dwordx4 v12, s[40:41] offset:3072 sc1
M v[112:115],a[80:83],a[48:51],v[112:115]
X a[12:15],v13,35840
M v[116:119],a[80:83],a[52:55],v[116:119]
s_add_u32 m0, s54, 24576
M v[120:123],a[80:83],a[56:59],v[120:123]
global_load_lds_dwordx4 v12, s[8:9] offset:0 sc1
M v[124:127],a[80:83],a[60:63],v[124:127]
global_load_lds_dwordx4 v12, s[8:9] offset:1024 sc1
M v[160:163],a[84:87],a[32:35],v[160:163]
T a[80:81],v32,32768
M v[164:167],a[84:87],a[36:39],v[164:167]
T a[82:83],v32,38912
global_load_dwordx2 v[30:31], v23, s[92:93] offset:96
M v[168:171],a[84:87],a[40:43],v[168:171]
X a[16:19],v35,32768
M v[172:175],a[84:87],a[44:47],v[172:175]
global_load_lds_dwordx4 v12, s[8:9] offset:2048 sc1
M v[176:179],a[84:87],a[48:51],v[176:179]
global_load_lds_dwordx4 v12, s[8:9] offset:3072 sc1
M v[180:183],a[84:87],a[52:55],v[180:183]
X a[20:23],v35,33792
M v[184:187],a[84:87],a[56:59],v[184:187]
v_add_u32 v12, 0x4000, v12
M v[188:191],a[84:87],a[60:63],v[188:191]
s_add_u32 m0, s55, 16384
M v[192:195],a[88:91],a[32:35],v[192:195]
T a[84:85],v32,33792
global_load_dwordx2 v[40:41], v23, s[92:93] offset:128
M v[196:199],a[88:91],a[36:39],v[196:199]
T a[86:87],v32,39936
M v[200:203],a[88:91],a[40:43],v[200:203]
X a[24:27],v35,34816
M v[204:207],a[88:91],a[44:47],v[204:207]
global_load_lds_dwordx4 v14, s[44:45] offset:0
M v[224:227],a[92:95],a[32:35],v[224:227]
T a[88:89],v10,32768
M v[228:231],a[92:95],a[36:39],v[228:231]
T a[90:91],v10,35840
M v[232:235],a[92:95],a[40:43],v[232:235]
X a[28:31],v35,35840
M v[236:239],a[92:95],a[44:47],v[236:239]
global_load_lds_dwordx4 v14, s[46:47] offset:1024
global_load_dwordx2 v[42:43], v23, s[92:93] offset:160
M v[208:211],a[96:99],a[32:35],v[208:211]
T a[92:93],v10,38912
M v[212:215],a[96:99],a[36:39],v[212:215]
T a[94:95],v10,41984
M v[216:219],a[96:99],a[40:43],v[216:219]
global_load_lds_dwordx4 v15, s[48:49] offset:2048
M v[220:223],a[96:99],a[44:47],v[220:223]
s_add_u32 m0, s55, 22528
M v[240:243],a[100:103],a[32:35],v[240:243]
T a[96:97],v37,32768
M v[244:247],a[100:103],a[36:39],v[244:247]
T a[98:99],v37,35840
M v[248:251],a[100:103],a[40:43],v[248:251]
global_load_lds_dwordx4 v14, s[10:11] offset:0
M v[252:255],a[100:103],a[44:47],v[252:255]
global_load_lds_dwordx4 v14, s[12:13] offset:1024
global_load_lds_dwordx4 v15, s[14:15] offset:2048
v_add_u32 v14, 229376, v14
v_add_u32 v15, 229376, v15
s_waitcnt lgkmcnt(4)
M v[64:67],a[72:75],a[0:3],v[64:67]
T a[100:101],v37,38912
M v[68:71],a[72:75],a[4:7],v[68:71]
T a[102:103],v37,41984
M v[72:75],a[72:75],a[8:11],v[72:75]
M v[76:79],a[72:75],a[12:15],v[76:79]
global_load_dwordx2 v[44:45], v23, s[92:93] offset:192
M v[80:83],a[72:75],a[16:19],v[80:83]
M v[84:87],a[72:75],a[20:23],v[84:87]
M v[88:91],a[72:75],a[24:27],v[88:91]
M v[92:95],a[72:75],a[28:31],v[92:95]
s_waitcnt vmcnt(35) lgkmcnt(0)
s_barrier
M v[128:131],a[76:79],a[0:3],v[128:131]
T a[72:73],v9,49152
s_add_u32 m0, s54, 32768
M v[132:135],a[76:79],a[4:7],v[132:135]
T a[74:75],v9,55296
global_load_lds_dwordx4 v12, s[40:41] offset:0 sc1
M v[136:139],a[76:79],a[8:11],v[136:139]
X a[32:35],v13,49152
M v[140:143],a[76:79],a[12:15],v[140:143]
global_load_lds_dwordx4 v12, s[40:41] offset:1024 sc1
global_load_dwordx2 v[46:47], v23, s[92:93] offset:224
M v[144:147],a[76:79],a[16:19],v[144:147]
X a[36:39],v13,50176
M v[148:151],a[76:79],a[20:23],v[148:151]
M v[152:155],a[76:79],a[24:27],v[152:155]
global_load_lds_dwordx4 v12, s[40:41] offset:2048 sc1
M v[156:159],a[76:79],a[28:31],v[156:159]
M v[96:99],a[80:83],a[0:3],v[96:99]
T a[76:77],v9,50176
M v[100:103],a[80:83],a[4:7],v[100:103]
T a[78:79],v9,56320
M v[104:107],a[80:83],a[8:11],v[104:107]
X a[40:43],v13,51200
M v[108:111],a[80:83],a[12:15],v[108:111]
global_load_lds_dwordx4 v12, s[40:41] offset:3072 sc1
M v[112:115],a[80:83],a[16:19],v[112:115]
X a[44:47],v13,52224
global_load_dwordx2 v[48:49], v39, s[92:93] offset:0
M v[116:119],a[80:83],a[20:23],v[116:119]
s_add_u32 m0, s54, 40960
M v[120:123],a[80:83],a[24:27],v[120:123]
global_load_lds_dwordx4 v12, s[8:9] offset:0 sc1
M v[124:127],a[80:83],a[28:31],v[124:127]
global_load_lds_dwordx4 v12, s[8:9] offset:1024 sc1
M v[160:163],a[84:87],a[0:3],v[160:163]
T a[80:81],v32,49152
M v[164:167],a[84:87],a[4:7],v[164:167]
T a[82:83],v32,55296
M v[168:171],a[84:87],a[8:11],v[168:171]
X a[48:51],v35,49152
M v[172:175],a[84:87],a[12:15],v[172:175]
global_load_lds_dwordx4 v12, s[8:9] offset:2048 sc1
M v[176:179],a[84:87],a[16:19],v[176:179]
global_load_lds_dwordx4 v12, s[8:9] offset:3072 sc1
M v[180:183],a[84:87],a[20:23],v[180:183]
X a[52:55],v35,50176
global_load_dwordx2 v[50:51], v39, s[92:93] offset:32
M v[184:187],a[84:87],a[24:27],v[184:187]
v_add_u32 v12, 0x4000, v12
M v[188:191],a[84:87],a[28:31],v[188:191]
s_add_u32 m0, s55, 32768
M v[192:195],a[88:91],a[0:3],v[192:195]
T a[84:85],v32,50176
M v[196:199],a[88:91],a[4:7],v[196:199]
T a[86:87],v32,56320
M v[200:203],a[88:91],a[8:11],v[200:203]
X a[56:59],v35,51200
M v[204:207],a[88:91],a[12:15],v[204:207]
global_load_lds_dwordx4 v14, s[44:45] offset:0
M v[224:227],a[92:95],a[0:3],v[224:227]
T a[88:89],v10,49152
M v[228:231],a[92:95],a[4:7],v[228:231]
T a[90:91],v10,52224
M v[232:235],a[92:95],a[8:11],v[232:235]
X a[60:63],v35,52224
global_load_dwordx2 v[52:53], v39, s[92:93] offset:64
M v[236:239],a[92:95],a[12:15],v[236:239]
global_load_lds_dwordx4 v14, s[46:47] offset:1024
M v[208:211],a[96:99],a[0:3],v[208:211]
T a[92:93],v10,55296
M v[212:215],a[96:99],a[4:7],v[212:215]
T a[94:95],v10,58368
M v[216:219],a[96:99],a[8:11],v[216:219]
global_load_lds_dwordx4 v15, s[48:49] offset:2048
M v[220:223],a[96:99],a[12:15],v[220:223]
s_add_u32 m0, s55, 38912
M v[240:243],a[100:103],a[0:3],v[240:243]
T a[96:97],v37,49152
M v[244:247],a[100:103],a[4:7],v[244:247]
T a[98:99],v37,52224
M v[248:251],a[100:103],a[8:11],v[248:251]
global_load_lds_dwordx4 v14, s[10:11] offset:0
M v[252:255],a[100:103],a[12:15],v[252:255]
global_load_lds_dwordx4 v14, s[12:13] offset:1024
global_load_lds_dwordx4 v15, s[14:15] offset:2048
v_add_u32 v14, 229376, v14
v_add_u32 v15, 229376, v15
s_waitcnt lgkmcnt(4)
M v[64:67],a[72:75],a[32:35],v[64:67]
T a[100:101],v37,55296
M v[68:71],a[72:75],a[36:39],v[68:71]
T a[102:103],v37,58368
M v[72:75],a[72:75],a[40:43],v[72:75]
M v[76:79],a[72:75],a[44:47],v[76:79]
global_load_dwordx2 v[54:55], v39, s[92:93] offset:96
M v[80:83],a[72:75],a[48:51],v[80:83]
M v[84:87],a[72:75],a[52:55],v[84:87]
M v[88:91],a[72:75],a[56:59],v[88:91]
M v[92:95],a[72:75],a[60:63],v[92:95]
s_waitcnt vmcnt(40) lgkmcnt(0)
s_barrier
M v[128:131],a[76:79],a[32:35],v[128:131]
T a[72:73],v9,0
s_add_u32 m0, s54, 49152
M v[132:135],a[76:79],a[36:39],v[132:135]
T a[74:75],v9,6144
global_load_lds_dwordx4 v12, s[40:41] offset:0 sc1
M v[136:139],a[76:79],a[40:43],v[136:139]
X a[0:3],v13,0
M v[140:143],a[76:79],a[44:47],v[140:143]
global_load_lds_dwordx4 v12, s[40:41] offset:1024 sc1
global_load_dwordx2 v[56:57], v39, s[92:93] offset:128
M v[144:147],a[76:79],a[48:51],v[144:147]
X a[4:7],v13,1024
M v[148:151],a[76:79],a[52:55],v[148:151]
M v[152:155],a[76:79],a[56:59],v[152:155]
global_load_lds_dwordx4 v12, s[40:41] offset:2048 sc1
M v[156:159],a[76:79],a[60:63],v[156:159]
M v[96:99],a[80:83],a[32:35],v[96:99]
T a[76:77],v9,1024
M v[100:103],a[80:83],a[36:39],v[100:103]
T a[78:79],v9,7168
M v[104:107],a[80:83],a[40:43],v[104:107]
X a[8:11],v13,2048
M v[108:111],a[80:83],a[44:47],v[108:111]
global_load_lds_dwordx4 v12, s[40:41] offset:3072 sc1
M v[112:115],a[80:83],a[48:51],v[112:115]
X a[12:15],v13,3072
global_load_dwordx2 v[58:59], v39, s[92:93] offset:160
M v[116:119],a[80:83],a[52:55],v[116:119]
s_add_u32 m0, s54, 57344
M v[120:123],a[80:83],a[56:59],v[120:123]
global_load_lds_dwordx4 v12, s[8:9] offset:0 sc1
M v[124:127],a[80:83],a[60:63],v[124:127]
global_load_lds_dwordx4 v12, s[8:9] offset:1024 sc1
M v[160:163],a[84:87],a[32:35],v[160:163]
T a[80:81],v32,0
M v[164:167],a[84:87],a[36:39],v[164:167]
T a[82:83],v32,6144
M v[168:171],a[84:87],a[40:43],v[168:171]
X a[16:19],v35,0
M v[172:175],a[84:87],a[44:47],v[172:175]
global_load_lds_dwordx4 v12, s[8:9] offset:2048 sc1
M v[176:179],a[84:87],a[48:51],v[176:179]
global_load_lds_dwordx4 v12, s[8:9] offset:3072 sc1
M v[180:183],a[84:87],a[52:55],v[180:183]
X a[20:23],v35,1024
global_load_dwordx2 v[60:61], v39, s[92:93] offset:192
M v[184:187],a[84:87],a[56:59],v[184:187]
v_add_u32 v12, 0x4000, v12
M v[188:191],a[84:87],a[60:63],v[188:191]
s_add_u32 m0, s55, 49152
M v[192:195],a[88:91],a[32:35],v[192:195]
T a[84:85],v32,1024
M v[196:199],a[88:91],a[36:39],v[196:199]
T a[86:87],v32,7168
M v[200:203],a[88:91],a[40:43],v[200:203]
X a[24:27],v35,2048
M v[204:207],a[88:91],a[44:47],v[204:207]
global_load_lds_dwordx4 v14, s[44:45] offset:0
M v[224:227],a[92:95],a[32:35],v[224:227]
T a[88:89],v10,0
M v[228:231],a[92:95],a[36:39],v[228:231]
T a[90:91],v10,3072
M v[232:235],a[92:95],a[40:43],v[232:235]
X a[28:31],v35,3072
global_load_dwordx2 v[62:63], v39, s[92:93] offset:224
M v[236:239],a[92:95],a[44:47],v[236:239]
global_load_lds_dwordx4 v14, s[46:47] offset:1024
M v[208:211],a[96:99],a[32:35],v[208:211]
T a[92:93],v10,6144
M v[212:215],a[96:99],a[36:39],v[212:215]
T a[94:95],v10,9216
M v[216:219],a[96:99],a[40:43],v[216:219]
global_load_lds_dwordx4 v15, s[48:49] offset:2048
M v[220:223],a[96:99],a[44:47],v[220:223]
s_add_u32 m0, s55, 55296
M v[240:243],a[100:103],a[32:35],v[240:243]
T a[96:97],v37,0
M v[244:247],a[100:103],a[36:39],v[244:247]
T a[98:99],v37,3072
M v[248:251],a[100:103],a[40:43],v[248:251]
global_load_lds_dwordx4 v14, s[10:11] offset:0
M v[252:255],a[100:103],a[44:47],v[252:255]
global_load_lds_dwordx4 v14, s[12:13] offset:1024
global_load_lds_dwordx4 v15, s[14:15] offset:2048
v_add_u32 v14, 229376, v14
v_add_u32 v15, 229376, v15
s_mov_b32 s64, 6
Lp1ML_%=:
s_waitcnt lgkmcnt(4)
M v[64:67],a[72:75],a[0:3],v[64:67]
T a[100:101],v37,6144
M v[68:71],a[72:75],a[4:7],v[68:71]
T a[102:103],v37,9216
M v[72:75],a[72:75],a[8:11],v[72:75]
M v[76:79],a[72:75],a[12:15],v[76:79]
M v[80:83],a[72:75],a[16:19],v[80:83]
M v[84:87],a[72:75],a[20:23],v[84:87]
M v[88:91],a[72:75],a[24:27],v[88:91]
M v[92:95],a[72:75],a[28:31],v[92:95]
s_waitcnt vmcnt(28) lgkmcnt(0)
s_barrier
M v[128:131],a[76:79],a[0:3],v[128:131]
T a[72:73],v9,16384
s_add_u32 m0, s54, 0
M v[132:135],a[76:79],a[4:7],v[132:135]
T a[74:75],v9,22528
global_load_lds_dwordx4 v12, s[40:41] offset:0 sc1
M v[136:139],a[76:79],a[8:11],v[136:139]
X a[32:35],v13,16384
M v[140:143],a[76:79],a[12:15],v[140:143]
global_load_lds_dwordx4 v12, s[40:41] offset:1024 sc1
M v[144:147],a[76:79],a[16:19],v[144:147]
X a[36:39],v13,17408
M v[148:151],a[76:79],a[20:23],v[148:151]
M v[152:155],a[76:79],a[24:27],v[152:155]
global_load_lds_dwordx4 v12, s[40:41] offset:2048 sc1
M v[156:159],a[76:79],a[28:31],v[156:159]
M v[96:99],a[80:83],a[0:3],v[96:99]
T a[76:77],v9,17408
M v[100:103],a[80:83],a[4:7],v[100:103]
T a[78:79],v9,23552
M v[104:107],a[80:83],a[8:11],v[104:107]
X a[40:43],v13,18432
M v[108:111],a[80:83],a[12:15],v[108:111]
global_load_lds_dwordx4 v12, s[40:41] offset:3072 sc1
M v[112:115],a[80:83],a[16:19],v[112:115]
X a[44:47],v13,19456
M v[116:119],a[80:83],a[20:23],v[116:119]
s_add_u32 m0, s54, 8192
M v[120:123],a[80:83],a[24:27],v[120:123]
global_load_lds_dwordx4 v12, s[8:9] offset:0 sc1
M v[124:127],a[80:83],a[28:31],v[124:127]
global_load_lds_dwordx4 v12, s[8:9] offset:1024 sc1
M v[160:163],a[84:87],a[0:3],v[160:163]
T a[80:81],v32,16384
M v[164:167],a[84:87],a[4:7],v[164:167]
T a[82:83],v32,22528
M v[168:171],a[84:87],a[8:11],v[168:171]
X a[48:51],v35,16384
M v[172:175],a[84:87],a[12:15],v[172:175]
global_load_lds_dwordx4 v12, s[8:9] offset:2048 sc1
M v[176:179],a[84:87],a[16:19],v[176:179]
global_load_lds_dwordx4 v12, s[8:9] offset:3072 sc1
M v[180:183],a[84:87],a[20:23],v[180:183]
X a[52:55],v35,17408
M v[184:187],a[84:87],a[24:27],v[184:187]
v_add_u32 v12, 0x4000, v12
M v[188:191],a[84:87],a[28:31],v[188:191]
s_add_u32 m0, s55, 0
M v[192:195],a[88:91],a[0:3],v[192:195]
T a[84:85],v32,17408
M v[196:199],a[88:91],a[4:7],v[196:199]
T a[86:87],v32,23552
M v[200:203],a[88:91],a[8:11],v[200:203]
X a[56:59],v35,18432
M v[204:207],a[88:91],a[12:15],v[204:207]
global_load_lds_dwordx4 v14, s[44:45] offset:0
M v[224:227],a[92:95],a[0:3],v[224:227]
T a[88:89],v10,16384
M v[228:231],a[92:95],a[4:7],v[228:231]
T a[90:91],v10,19456
M v[232:235],a[92:95],a[8:11],v[232:235]
X a[60:63],v35,19456
M v[236:239],a[92:95],a[12:15],v[236:239]
global_load_lds_dwordx4 v14, s[46:47] offset:1024
M v[208:211],a[96:99],a[0:3],v[208:211]
T a[92:93],v10,22528
M v[212:215],a[96:99],a[4:7],v[212:215]
T a[94:95],v10,25600
M v[216:219],a[96:99],a[8:11],v[216:219]
global_load_lds_dwordx4 v15, s[48:49] offset:2048
M v[220:223],a[96:99],a[12:15],v[220:223]
s_add_u32 m0, s55, 6144
M v[240:243],a[100:103],a[0:3],v[240:243]
T a[96:97],v37,16384
M v[244:247],a[100:103],a[4:7],v[244:247]
T a[98:99],v37,19456
M v[248:251],a[100:103],a[8:11],v[248:251]
global_load_lds_dwordx4 v14, s[10:11] offset:0
M v[252:255],a[100:103],a[12:15],v[252:255]
global_load_lds_dwordx4 v14, s[12:13] offset:1024
global_load_lds_dwordx4 v15, s[14:15] offset:2048
v_add_u32 v14, 229376, v14
v_add_u32 v15, 229376, v15
s_waitcnt lgkmcnt(4)
M v[64:67],a[72:75],a[32:35],v[64:67]
T a[100:101],v37,22528
M v[68:71],a[72:75],a[36:39],v[68:71]
T a[102:103],v37,25600
M v[72:75],a[72:75],a[40:43],v[72:75]
M v[76:79],a[72:75],a[44:47],v[76:79]
M v[80:83],a[72:75],a[48:51],v[80:83]
M v[84:87],a[72:75],a[52:55],v[84:87]
M v[88:91],a[72:75],a[56:59],v[88:91]
M v[92:95],a[72:75],a[60:63],v[92:95]
s_waitcnt vmcnt(28) lgkmcnt(0)
s_barrier
M v[128:131],a[76:79],a[32:35],v[128:131]
T a[72:73],v9,32768
s_add_u32 m0, s54, 16384
M v[132:135],a[76:79],a[36:39],v[132:135]
T a[74:75],v9,38912
global_load_lds_dwordx4 v12, s[40:41] offset:0 sc1
M v[136:139],a[76:79],a[40:43],v[136:139]
X a[0:3],v13,32768
M v[140:143],a[76:79],a[44:47],v[140:143]
global_load_lds_dwordx4 v12, s[40:41] offset:1024 sc1
M v[144:147],a[76:79],a[48:51],v[144:147]
X a[4:7],v13,33792
M v[148:151],a[76:79],a[52:55],v[148:151]
M v[152:155],a[76:79],a[56:59],v[152:155]
global_load_lds_dwordx4 v12, s[40:41] offset:2048 sc1
M v[156:159],a[76:79],a[60:63],v[156:159]
M v[96:99],a[80:83],a[32:35],v[96:99]
T a[76:77],v9,33792
M v[100:103],a[80:83],a[36:39],v[100:103]
T a[78:79],v9,39936
M v[104:107],a[80:83],a[40:43],v[104:107]
X a[8:11],v13,34816
M v[108:111],a[80:83],a[44:47],v[108:111]
global_load_lds_dwordx4 v12, s[40:41] offset:3072 sc1
M v[112:115],a[80:83],a[48:51],v[112:115]
X a[12:15],v13,35840
M v[116:119],a[80:83],a[52:55],v[116:119]
s_add_u32 m0, s54, 24576
M v[120:123],a[80:83],a[56:59],v[120:123]
global_load_lds_dwordx4 v12, s[8:9] offset:0 sc1
M v[124:127],a[80:83],a[60:63],v[124:127]
global_load_lds_dwordx4 v12, s[8:9] offset:1024 sc1
M v[160:163],a[84:87],a[32:35],v[160:163]
T a[80:81],v32,32768
M v[164:167],a[84:87],a[36:39],v[164:167]
T a[82:83],v32,38912
M v[168:171],a[84:87],a[40:43],v[168:171]
X a[16:19],v35,32768
M v[172:175],a[84:87],a[44:47],v[172:175]
global_load_lds_dwordx4 v12, s[8:9] offset:2048 sc1
M v[176:179],a[84:87],a[48:51],v[176:179]
global_load_lds_dwordx4 v12, s[8:9] offset:3072 sc1
M v[180:183],a[84:87],a[52:55],v[180:183]
X a[20:23],v35,33792
M v[184:187],a[84:87],a[56:59],v[184:187]
v_add_u32 v12, 0x4000, v12
M v[188:191],a[84:87],a[60:63],v[188:191]
s_add_u32 m0, s55, 16384
M v[192:195],a[88:91],a[32:35],v[192:195]
T a[84:85],v32,33792
M v[196:199],a[88:91],a[36:39],v[196:199]
T a[86:87],v32,39936
M v[200:203],a[88:91],a[40:43],v[200:203]
X a[24:27],v35,34816
M v[204:207],a[88:91],a[44:47],v[204:207]
global_load_lds_dwordx4 v14, s[44:45] offset:0
M v[224:227],a[92:95],a[32:35],v[224:227]
T a[88:89],v10,32768
M v[228:231],a[92:95],a[36:39],v[228:231]
T a[90:91],v10,35840
M v[232:235],a[92:95],a[40:43],v[232:235]
X a[28:31],v35,35840
M v[236:239],a[92:95],a[44:47],v[236:239]
global_load_lds_dwordx4 v14, s[46:47] offset:1024
M v[208:211],a[96:99],a[32:35],v[208:211]
T a[92:93],v10,38912
M v[212:215],a[96:99],a[36:39],v[212:215]
T a[94:95],v10,41984
M v[216:219],a[96:99],a[40:43],v[216:219]
global_load_lds_dwordx4 v15, s[48:49] offset:2048
M v[220:223],a[96:99],a[44:47],v[220:223]
s_add_u32 m0, s55, 22528
M v[240:243],a[100:103],a[32:35],v[240:243]
T a[96:97],v37,32768
M v[244:247],a[100:103],a[36:39],v[244:247]
T a[98:99],v37,35840
M v[248:251],a[100:103],a[40:43],v[248:251]
global_load_lds_dwordx4 v14, s[10:11] offset:0
M v[252:255],a[100:103],a[44:47],v[252:255]
global_load_lds_dwordx4 v14, s[12:13] offset:1024
global_load_lds_dwordx4 v15, s[14:15] offset:2048
v_add_u32 v14, 229376, v14
v_add_u32 v15, 229376, v15
s_waitcnt lgkmcnt(4)
M v[64:67],a[72:75],a[0:3],v[64:67]
T a[100:101],v37,38912
M v[68:71],a[72:75],a[4:7],v[68:71]
T a[102:103],v37,41984
M v[72:75],a[72:75],a[8:11],v[72:75]
M v[76:79],a[72:75],a[12:15],v[76:79]
M v[80:83],a[72:75],a[16:19],v[80:83]
M v[84:87],a[72:75],a[20:23],v[84:87]
M v[88:91],a[72:75],a[24:27],v[88:91]
M v[92:95],a[72:75],a[28:31],v[92:95]
s_waitcnt vmcnt(28) lgkmcnt(0)
s_barrier
M v[128:131],a[76:79],a[0:3],v[128:131]
T a[72:73],v9,49152
s_add_u32 m0, s54, 32768
M v[132:135],a[76:79],a[4:7],v[132:135]
T a[74:75],v9,55296
global_load_lds_dwordx4 v12, s[40:41] offset:0 sc1
M v[136:139],a[76:79],a[8:11],v[136:139]
X a[32:35],v13,49152
M v[140:143],a[76:79],a[12:15],v[140:143]
global_load_lds_dwordx4 v12, s[40:41] offset:1024 sc1
M v[144:147],a[76:79],a[16:19],v[144:147]
X a[36:39],v13,50176
M v[148:151],a[76:79],a[20:23],v[148:151]
M v[152:155],a[76:79],a[24:27],v[152:155]
global_load_lds_dwordx4 v12, s[40:41] offset:2048 sc1
M v[156:159],a[76:79],a[28:31],v[156:159]
M v[96:99],a[80:83],a[0:3],v[96:99]
T a[76:77],v9,50176
M v[100:103],a[80:83],a[4:7],v[100:103]
T a[78:79],v9,56320
M v[104:107],a[80:83],a[8:11],v[104:107]
X a[40:43],v13,51200
M v[108:111],a[80:83],a[12:15],v[108:111]
global_load_lds_dwordx4 v12, s[40:41] offset:3072 sc1
M v[112:115],a[80:83],a[16:19],v[112:115]
X a[44:47],v13,52224
M v[116:119],a[80:83],a[20:23],v[116:119]
s_add_u32 m0, s54, 40960
M v[120:123],a[80:83],a[24:27],v[120:123]
global_load_lds_dwordx4 v12, s[8:9] offset:0 sc1
M v[124:127],a[80:83],a[28:31],v[124:127]
global_load_lds_dwordx4 v12, s[8:9] offset:1024 sc1
M v[160:163],a[84:87],a[0:3],v[160:163]
T a[80:81],v32,49152
M v[164:167],a[84:87],a[4:7],v[164:167]
T a[82:83],v32,55296
M v[168:171],a[84:87],a[8:11],v[168:171]
X a[48:51],v35,49152
M v[172:175],a[84:87],a[12:15],v[172:175]
global_load_lds_dwordx4 v12, s[8:9] offset:2048 sc1
M v[176:179],a[84:87],a[16:19],v[176:179]
global_load_lds_dwordx4 v12, s[8:9] offset:3072 sc1
M v[180:183],a[84:87],a[20:23],v[180:183]
X a[52:55],v35,50176
M v[184:187],a[84:87],a[24:27],v[184:187]
v_add_u32 v12, 0x4000, v12
M v[188:191],a[84:87],a[28:31],v[188:191]
s_add_u32 m0, s55, 32768
M v[192:195],a[88:91],a[0:3],v[192:195]
T a[84:85],v32,50176
M v[196:199],a[88:91],a[4:7],v[196:199]
T a[86:87],v32,56320
M v[200:203],a[88:91],a[8:11],v[200:203]
X a[56:59],v35,51200
M v[204:207],a[88:91],a[12:15],v[204:207]
global_load_lds_dwordx4 v14, s[44:45] offset:0
M v[224:227],a[92:95],a[0:3],v[224:227]
T a[88:89],v10,49152
M v[228:231],a[92:95],a[4:7],v[228:231]
T a[90:91],v10,52224
M v[232:235],a[92:95],a[8:11],v[232:235]
X a[60:63],v35,52224
M v[236:239],a[92:95],a[12:15],v[236:239]
global_load_lds_dwordx4 v14, s[46:47] offset:1024
M v[208:211],a[96:99],a[0:3],v[208:211]
T a[92:93],v10,55296
M v[212:215],a[96:99],a[4:7],v[212:215]
T a[94:95],v10,58368
M v[216:219],a[96:99],a[8:11],v[216:219]
global_load_lds_dwordx4 v15, s[48:49] offset:2048
M v[220:223],a[96:99],a[12:15],v[220:223]
s_add_u32 m0, s55, 38912
M v[240:243],a[100:103],a[0:3],v[240:243]
T a[96:97],v37,49152
M v[244:247],a[100:103],a[4:7],v[244:247]
T a[98:99],v37,52224
M v[248:251],a[100:103],a[8:11],v[248:251]
global_load_lds_dwordx4 v14, s[10:11] offset:0
M v[252:255],a[100:103],a[12:15],v[252:255]
global_load_lds_dwordx4 v14, s[12:13] offset:1024
global_load_lds_dwordx4 v15, s[14:15] offset:2048
v_add_u32 v14, 229376, v14
v_add_u32 v15, 229376, v15
s_waitcnt lgkmcnt(4)
M v[64:67],a[72:75],a[32:35],v[64:67]
T a[100:101],v37,55296
M v[68:71],a[72:75],a[36:39],v[68:71]
T a[102:103],v37,58368
M v[72:75],a[72:75],a[40:43],v[72:75]
M v[76:79],a[72:75],a[44:47],v[76:79]
M v[80:83],a[72:75],a[48:51],v[80:83]
M v[84:87],a[72:75],a[52:55],v[84:87]
M v[88:91],a[72:75],a[56:59],v[88:91]
M v[92:95],a[72:75],a[60:63],v[92:95]
s_waitcnt vmcnt(28) lgkmcnt(0)
s_barrier
M v[128:131],a[76:79],a[32:35],v[128:131]
T a[72:73],v9,0
s_add_u32 m0, s54, 49152
M v[132:135],a[76:79],a[36:39],v[132:135]
T a[74:75],v9,6144
global_load_lds_dwordx4 v12, s[40:41] offset:0 sc1
M v[136:139],a[76:79],a[40:43],v[136:139]
X a[0:3],v13,0
M v[140:143],a[76:79],a[44:47],v[140:143]
global_load_lds_dwordx4 v12, s[40:41] offset:1024 sc1
M v[144:147],a[76:79],a[48:51],v[144:147]
X a[4:7],v13,1024
M v[148:151],a[76:79],a[52:55],v[148:151]
M v[152:155],a[76:79],a[56:59],v[152:155]
global_load_lds_dwordx4 v12, s[40:41] offset:2048 sc1
M v[156:159],a[76:79],a[60:63],v[156:159]
M v[96:99],a[80:83],a[32:35],v[96:99]
T a[76:77],v9,1024
M v[100:103],a[80:83],a[36:39],v[100:103]
T a[78:79],v9,7168
M v[104:107],a[80:83],a[40:43],v[104:107]
X a[8:11],v13,2048
M v[108:111],a[80:83],a[44:47],v[108:111]
global_load_lds_dwordx4 v12, s[40:41] offset:3072 sc1
M v[112:115],a[80:83],a[48:51],v[112:115]
X a[12:15],v13,3072
M v[116:119],a[80:83],a[52:55],v[116:119]
s_add_u32 m0, s54, 57344
M v[120:123],a[80:83],a[56:59],v[120:123]
global_load_lds_dwordx4 v12, s[8:9] offset:0 sc1
M v[124:127],a[80:83],a[60:63],v[124:127]
global_load_lds_dwordx4 v12, s[8:9] offset:1024 sc1
M v[160:163],a[84:87],a[32:35],v[160:163]
T a[80:81],v32,0
M v[164:167],a[84:87],a[36:39],v[164:167]
T a[82:83],v32,6144
M v[168:171],a[84:87],a[40:43],v[168:171]
X a[16:19],v35,0
M v[172:175],a[84:87],a[44:47],v[172:175]
global_load_lds_dwordx4 v12, s[8:9] offset:2048 sc1
M v[176:179],a[84:87],a[48:51],v[176:179]
global_load_lds_dwordx4 v12, s[8:9] offset:3072 sc1
M v[180:183],a[84:87],a[52:55],v[180:183]
X a[20:23],v35,1024
M v[184:187],a[84:87],a[56:59],v[184:187]
v_add_u32 v12, 0x4000, v12
M v[188:191],a[84:87],a[60:63],v[188:191]
s_add_u32 m0, s55, 49152
M v[192:195],a[88:91],a[32:35],v[192:195]
T a[84:85],v32,1024
M v[196:199],a[88:91],a[36:39],v[196:199]
T a[86:87],v32,7168
M v[200:203],a[88:91],a[40:43],v[200:203]
X a[24:27],v35,2048
M v[204:207],a[88:91],a[44:47],v[204:207]
global_load_lds_dwordx4 v14, s[44:45] offset:0
M v[224:227],a[92:95],a[32:35],v[224:227]
T a[88:89],v10,0
M v[228:231],a[92:95],a[36:39],v[228:231]
T a[90:91],v10,3072
M v[232:235],a[92:95],a[40:43],v[232:235]
X a[28:31],v35,3072
M v[236:239],a[92:95],a[44:47],v[236:239]
global_load_lds_dwordx4 v14, s[46:47] offset:1024
M v[208:211],a[96:99],a[32:35],v[208:211]
T a[92:93],v10,6144
M v[212:215],a[96:99],a[36:39],v[212:215]
T a[94:95],v10,9216
M v[216:219],a[96:99],a[40:43],v[216:219]
global_load_lds_dwordx4 v15, s[48:49] offset:2048
M v[220:223],a[96:99],a[44:47],v[220:223]
s_add_u32 m0, s55, 55296
M v[240:243],a[100:103],a[32:35],v[240:243]
T a[96:97],v37,0
M v[244:247],a[100:103],a[36:39],v[244:247]
T a[98:99],v37,3072
s_sub_u32 s64, s64, 1
M v[248:251],a[100:103],a[40:43],v[248:251]
global_load_lds_dwordx4 v14, s[10:11] offset:0
s_cmp_lg_u32 s64, 0
M v[252:255],a[100:103],a[44:47],v[252:255]
global_load_lds_dwordx4 v14, s[12:13] offset:1024
global_load_lds_dwordx4 v15, s[14:15] offset:2048
v_add_u32 v14, 229376, v14
v_add_u32 v15, 229376, v15
s_cbranch_scc1 Lp1ML_%=
LmrgM_%=:
v_mov_b32 v36, s57
v_mov_b32 v38, 0
s_waitcnt lgkmcnt(4)
v_add_u32 v33, s79, v16
M v[64:67],a[72:75],a[0:3],v[64:67]
T a[100:101],v37,6144
v_subrev_u32 v12, 524288, v12
M v[68:71],a[72:75],a[4:7],v[68:71]
T a[102:103],v37,9216
s_add_u32 s68, s58, 147456
s_addc_u32 s69, s59, 0
M v[72:75],a[72:75],a[8:11],v[72:75]
s_lshr_b32 s65, s99, 2
s_mov_b32 s73, 294912
M v[76:79],a[72:75],a[12:15],v[76:79]
s_sub_u32 s55, s55, s65
s_and_b32 s72, s99, 4096
s_cselect_b32 s72, 0xfff28000, s73
s_cselect_b32 s73, -1, 0
M v[80:83],a[72:75],a[16:19],v[80:83]
M v[84:87],a[72:75],a[20:23],v[84:87]
M v[88:91],a[72:75],a[24:27],v[88:91]
M v[92:95],a[72:75],a[28:31],v[92:95]
s_waitcnt vmcnt(28) lgkmcnt(0)
s_barrier
M v[128:131],a[76:79],a[0:3],v[128:131]
T a[72:73],v9,16384
s_add_u32 m0, s54, 131072
M v[132:135],a[76:79],a[4:7],v[132:135]
T a[74:75],v9,22528
global_load_lds_dwordx4 v12, s[40:41] offset:0 sc1
M v[136:139],a[76:79],a[8:11],v[136:139]
X a[32:35],v13,16384
M v[140:143],a[76:79],a[12:15],v[140:143]
M v[144:147],a[76:79],a[16:19],v[144:147]
X a[36:39],v13,17408
M v[148:151],a[76:79],a[20:23],v[148:151]
global_load_lds_dwordx4 v12, s[40:41] offset:1024 sc1
M v[152:155],a[76:79],a[24:27],v[152:155]
M v[156:159],a[76:79],a[28:31],v[156:159]
global_load_lds_dwordx4 v12, s[40:41] offset:2048 sc1
M v[96:99],a[80:83],a[0:3],v[96:99]
T a[76:77],v9,17408
M v[100:103],a[80:83],a[4:7],v[100:103]
T a[78:79],v9,23552
M v[104:107],a[80:83],a[8:11],v[104:107]
X a[40:43],v13,18432
M v[108:111],a[80:83],a[12:15],v[108:111]
global_load_lds_dwordx4 v12, s[40:41] offset:3072 sc1
v_add_u32 v12, 0x4000, v12
M v[112:115],a[80:83],a[16:19],v[112:115]
X a[44:47],v13,19456
M v[116:119],a[80:83],a[20:23],v[116:119]
s_add_u32 m0, s55, 0
M v[120:123],a[80:83],a[24:27],v[120:123]
global_load_lds_dwordx4 v11, s[50:51] offset:0
M v[124:127],a[80:83],a[28:31],v[124:127]
M v[160:163],a[84:87],a[0:3],v[160:163]
T a[80:81],v32,16384
M v[164:167],a[84:87],a[4:7],v[164:167]
T a[82:83],v32,22528
M v[168:171],a[84:87],a[8:11],v[168:171]
X a[48:51],v35,16384
M v[172:175],a[84:87],a[12:15],v[172:175]
global_load_lds_dwordx4 v11, s[52:53] offset:1024
v_add_u32 v11, 229376, v11
M v[176:179],a[84:87],a[16:19],v[176:179]
s_add_u32 m0, s55, 8192
M v[180:183],a[84:87],a[20:23],v[180:183]
X a[52:55],v35,17408
M v[184:187],a[84:87],a[24:27],v[184:187]
global_load_lds_dwordx4 v11, s[50:51] offset:0
M v[188:191],a[84:87],a[28:31],v[188:191]
global_load_lds_dwordx4 v11, s[52:53] offset:1024
v_add_u32 v11, 229376, v11
M v[192:195],a[88:91],a[0:3],v[192:195]
T a[84:85],v32,17408
M v[196:199],a[88:91],a[4:7],v[196:199]
T a[86:87],v32,23552
M v[200:203],a[88:91],a[8:11],v[200:203]
X a[56:59],v35,18432
M v[204:207],a[88:91],a[12:15],v[204:207]
s_add_u32 m0, s54, 147456
M v[224:227],a[92:95],a[0:3],v[224:227]
T a[88:89],v10,16384
M v[228:231],a[92:95],a[4:7],v[228:231]
T a[90:91],v10,19456
M v[232:235],a[92:95],a[8:11],v[232:235]
X a[60:63],v35,19456
M v[236:239],a[92:95],a[12:15],v[236:239]
global_load_lds_dwordx4 v12, s[40:41] offset:0 sc1
M v[208:211],a[96:99],a[0:3],v[208:211]
T a[92:93],v10,22528
M v[212:215],a[96:99],a[4:7],v[212:215]
T a[94:95],v10,25600
M v[216:219],a[96:99],a[8:11],v[216:219]
global_load_lds_dwordx4 v12, s[40:41] offset:1024 sc1
M v[220:223],a[96:99],a[12:15],v[220:223]
global_load_lds_dwordx4 v12, s[40:41] offset:2048 sc1
M v[240:243],a[100:103],a[0:3],v[240:243]
T a[96:97],v37,16384
M v[244:247],a[100:103],a[4:7],v[244:247]
T a[98:99],v37,19456
M v[248:251],a[100:103],a[8:11],v[248:251]
global_load_lds_dwordx4 v12, s[40:41] offset:3072 sc1
M v[252:255],a[100:103],a[12:15],v[252:255]
v_add_u32 v12, 0x4000, v12
s_waitcnt lgkmcnt(4)
M v[64:67],a[72:75],a[32:35],v[64:67]
T a[100:101],v37,22528
M v[68:71],a[72:75],a[36:39],v[68:71]
T a[102:103],v37,25600
M v[72:75],a[72:75],a[40:43],v[72:75]
M v[76:79],a[72:75],a[44:47],v[76:79]
M v[80:83],a[72:75],a[48:51],v[80:83]
M v[84:87],a[72:75],a[52:55],v[84:87]
M v[88:91],a[72:75],a[56:59],v[88:91]
M v[92:95],a[72:75],a[60:63],v[92:95]
s_waitcnt vmcnt(26) lgkmcnt(0)
s_barrier
M v[128:131],a[76:79],a[32:35],v[128:131]
T a[72:73],v9,32768
s_add_u32 m0, s54, 0
M v[132:135],a[76:79],a[36:39],v[132:135]
T a[74:75],v9,38912
global_load_lds_dwordx4 v12, s[40:41] offset:0 sc1
M v[136:139],a[76:79],a[40:43],v[136:139]
X a[0:3],v13,32768
M v[140:143],a[76:79],a[44:47],v[140:143]
M v[144:147],a[76:79],a[48:51],v[144:147]
X a[4:7],v13,33792
M v[148:151],a[76:79],a[52:55],v[148:151]
global_load_lds_dwordx4 v12, s[40:41] offset:1024 sc1
M v[152:155],a[76:79],a[56:59],v[152:155]
M v[156:159],a[76:79],a[60:63],v[156:159]
global_load_lds_dwordx4 v12, s[40:41] offset:2048 sc1
M v[96:99],a[80:83],a[32:35],v[96:99]
T a[76:77],v9,33792
M v[100:103],a[80:83],a[36:39],v[100:103]
T a[78:79],v9,39936
M v[104:107],a[80:83],a[40:43],v[104:107]
X a[8:11],v13,34816
M v[108:111],a[80:83],a[44:47],v[108:111]
global_load_lds_dwordx4 v12, s[40:41] offset:3072 sc1
v_add_u32 v12, 0x4000, v12
M v[112:115],a[80:83],a[48:51],v[112:115]
X a[12:15],v13,35840
M v[116:119],a[80:83],a[52:55],v[116:119]
s_add_u32 m0, s55, 16384
M v[120:123],a[80:83],a[56:59],v[120:123]
global_load_lds_dwordx4 v11, s[50:51] offset:0
M v[124:127],a[80:83],a[60:63],v[124:127]
M v[160:163],a[84:87],a[32:35],v[160:163]
T a[80:81],v32,32768
M v[164:167],a[84:87],a[36:39],v[164:167]
T a[82:83],v32,38912
M v[168:171],a[84:87],a[40:43],v[168:171]
X a[16:19],v35,32768
M v[172:175],a[84:87],a[44:47],v[172:175]
global_load_lds_dwordx4 v11, s[52:53] offset:1024
v_add_u32 v11, 229376, v11
M v[176:179],a[84:87],a[48:51],v[176:179]
s_add_u32 m0, s55, 24576
M v[180:183],a[84:87],a[52:55],v[180:183]
X a[20:23],v35,33792
M v[184:187],a[84:87],a[56:59],v[184:187]
global_load_lds_dwordx4 v11, s[50:51] offset:0
M v[188:191],a[84:87],a[60:63],v[188:191]
global_load_lds_dwordx4 v11, s[52:53] offset:1024
v_add_u32 v11, 229376, v11
M v[192:195],a[88:91],a[32:35],v[192:195]
T a[84:85],v32,33792
M v[196:199],a[88:91],a[36:39],v[196:199]
T a[86:87],v32,39936
M v[200:203],a[88:91],a[40:43],v[200:203]
X a[24:27],v35,34816
M v[204:207],a[88:91],a[44:47],v[204:207]
s_add_u32 m0, s54, 16384
M v[224:227],a[92:95],a[32:35],v[224:227]
T a[88:89],v10,32768
M v[228:231],a[92:95],a[36:39],v[228:231]
T a[90:91],v10,35840
M v[232:235],a[92:95],a[40:43],v[232:235]
X a[28:31],v35,35840
M v[236:239],a[92:95],a[44:47],v[236:239]
global_load_lds_dwordx4 v12, s[40:41] offset:0 sc1
M v[208:211],a[96:99],a[32:35],v[208:211]
T a[92:93],v10,38912
M v[212:215],a[96:99],a[36:39],v[212:215]
T a[94:95],v10,41984
M v[216:219],a[96:99],a[40:43],v[216:219]
global_load_lds_dwordx4 v12, s[40:41] offset:1024 sc1
M v[220:223],a[96:99],a[44:47],v[220:223]
global_load_lds_dwordx4 v12, s[40:41] offset:2048 sc1
M v[240:243],a[100:103],a[32:35],v[240:243]
T a[96:97],v37,32768
M v[244:247],a[100:103],a[36:39],v[244:247]
T a[98:99],v37,35840
M v[248:251],a[100:103],a[40:43],v[248:251]
global_load_lds_dwordx4 v12, s[40:41] offset:3072 sc1
M v[252:255],a[100:103],a[44:47],v[252:255]
v_add_u32 v12, 0x4000, v12
s_waitcnt lgkmcnt(4)
M v[64:67],a[72:75],a[0:3],v[64:67]
T a[100:101],v37,38912
M v[68:71],a[72:75],a[4:7],v[68:71]
T a[102:103],v37,41984
M v[72:75],a[72:75],a[8:11],v[72:75]
M v[76:79],a[72:75],a[12:15],v[76:79]
M v[80:83],a[72:75],a[16:19],v[80:83]
M v[84:87],a[72:75],a[20:23],v[84:87]
M v[88:91],a[72:75],a[24:27],v[88:91]
M v[92:95],a[72:75],a[28:31],v[92:95]
s_waitcnt vmcnt(24) lgkmcnt(0)
s_barrier
M v[128:131],a[76:79],a[0:3],v[128:131]
T a[72:73],v9,49152
s_add_u32 m0, s54, 32768
M v[132:135],a[76:79],a[4:7],v[132:135]
T a[74:75],v9,55296
global_load_lds_dwordx4 v12, s[40:41] offset:0 sc1
M v[136:139],a[76:79],a[8:11],v[136:139]
X a[32:35],v13,49152
M v[140:143],a[76:79],a[12:15],v[140:143]
M v[144:147],a[76:79],a[16:19],v[144:147]
X a[36:39],v13,50176
M v[148:151],a[76:79],a[20:23],v[148:151]
global_load_lds_dwordx4 v12, s[40:41] offset:1024 sc1
M v[152:155],a[76:79],a[24:27],v[152:155]
M v[156:159],a[76:79],a[28:31],v[156:159]
M v[96:99],a[80:83],a[0:3],v[96:99]
T a[76:77],v9,50176
M v[100:103],a[80:83],a[4:7],v[100:103]
T a[78:79],v9,56320
M v[104:107],a[80:83],a[8:11],v[104:107]
X a[40:43],v13,51200
M v[108:111],a[80:83],a[12:15],v[108:111]
global_load_lds_dwordx4 v12, s[40:41] offset:2048 sc1
M v[112:115],a[80:83],a[16:19],v[112:115]
X a[44:47],v13,52224
M v[116:119],a[80:83],a[20:23],v[116:119]
global_load_lds_dwordx4 v12, s[40:41] offset:3072 sc1
v_add_u32 v12, 0x4000, v12
M v[120:123],a[80:83],a[24:27],v[120:123]
M v[124:127],a[80:83],a[28:31],v[124:127]
M v[160:163],a[84:87],a[0:3],v[160:163]
T a[80:81],v32,49152
M v[164:167],a[84:87],a[4:7],v[164:167]
T a[82:83],v32,55296
M v[168:171],a[84:87],a[8:11],v[168:171]
X a[48:51],v35,49152
M v[172:175],a[84:87],a[12:15],v[172:175]
s_add_u32 m0, s55, 32768
M v[176:179],a[84:87],a[16:19],v[176:179]
global_load_lds_dwordx4 v11, s[50:51] offset:0
M v[180:183],a[84:87],a[20:23],v[180:183]
X a[52:55],v35,50176
M v[184:187],a[84:87],a[24:27],v[184:187]
global_load_lds_dwordx4 v11, s[52:53] offset:1024
v_add_u32 v11, 229376, v11
M v[188:191],a[84:87],a[28:31],v[188:191]
M v[192:195],a[88:91],a[0:3],v[192:195]
T a[84:85],v32,50176
M v[196:199],a[88:91],a[4:7],v[196:199]
T a[86:87],v32,56320
M v[200:203],a[88:91],a[8:11],v[200:203]
X a[56:59],v35,51200
M v[204:207],a[88:91],a[12:15],v[204:207]
s_add_u32 m0, s55, 40960
M v[224:227],a[92:95],a[0:3],v[224:227]
T a[88:89],v10,49152
M v[228:231],a[92:95],a[4:7],v[228:231]
T a[90:91],v10,52224
M v[232:235],a[92:95],a[8:11],v[232:235]
X a[60:63],v35,52224
M v[236:239],a[92:95],a[12:15],v[236:239]
global_load_lds_dwordx4 v11, s[50:51] offset:0
M v[208:211],a[96:99],a[0:3],v[208:211]
T a[92:93],v10,55296
M v[212:215],a[96:99],a[4:7],v[212:215]
T a[94:95],v10,58368
M v[216:219],a[96:99],a[8:11],v[216:219]
global_load_lds_dwordx4 v11, s[52:53] offset:1024
M v[220:223],a[96:99],a[12:15],v[220:223]
v_add_u32 v11, 229376, v11
M v[240:243],a[100:103],a[0:3],v[240:243]
T a[96:97],v37,49152
M v[244:247],a[100:103],a[4:7],v[244:247]
T a[98:99],v37,52224
M v[248:251],a[100:103],a[8:11],v[248:251]
M v[252:255],a[100:103],a[12:15],v[252:255]
s_waitcnt lgkmcnt(4)
M v[64:67],a[72:75],a[32:35],v[64:67]
T a[100:101],v37,55296
M v[68:71],a[72:75],a[36:39],v[68:71]
T a[102:103],v37,58368
M v[72:75],a[72:75],a[40:43],v[72:75]
v_lshrrev_b32 v10, 9, v16
M v[76:79],a[72:75],a[44:47],v[76:79]
v_lshlrev_b32 v10, 10, v10
M v[80:83],a[72:75],a[48:51],v[80:83]
v_sub_u32 v9, v9, v10
M v[84:87],a[72:75],a[52:55],v[84:87]
v_xor_b32 v32, 32, v9
M v[88:91],a[72:75],a[56:59],v[88:91]
v_add_u32 v13, 131072, v8
M v[92:95],a[72:75],a[60:63],v[92:95]
s_waitcnt vmcnt(26) lgkmcnt(0)
s_barrier
M v[128:131],a[76:79],a[32:35],v[128:131]
T a[72:73],v9,0
s_add_u32 m0, s54, 49152
M v[132:135],a[76:79],a[36:39],v[132:135]
T a[74:75],v9,4096
global_load_lds_dwordx4 v12, s[40:41] offset:0 sc1
M v[136:139],a[76:79],a[40:43],v[136:139]
X a[0:3],v13,0
M v[140:143],a[76:79],a[44:47],v[140:143]
M v[144:147],a[76:79],a[48:51],v[144:147]
X a[4:7],v13,1024
M v[148:151],a[76:79],a[52:55],v[148:151]
M v[152:155],a[76:79],a[56:59],v[152:155]
M v[156:159],a[76:79],a[60:63],v[156:159]
M v[96:99],a[80:83],a[32:35],v[96:99]
T a[76:77],v9,1024
M v[100:103],a[80:83],a[36:39],v[100:103]
T a[78:79],v9,5120
M v[104:107],a[80:83],a[40:43],v[104:107]
X a[8:11],v13,2048
M v[108:111],a[80:83],a[44:47],v[108:111]
global_load_lds_dwordx4 v12, s[40:41] offset:1024 sc1
M v[112:115],a[80:83],a[48:51],v[112:115]
X a[12:15],v13,3072
M v[116:119],a[80:83],a[52:55],v[116:119]
M v[120:123],a[80:83],a[56:59],v[120:123]
M v[124:127],a[80:83],a[60:63],v[124:127]
M v[160:163],a[84:87],a[32:35],v[160:163]
T a[80:81],v32,0
M v[164:167],a[84:87],a[36:39],v[164:167]
T a[82:83],v32,4096
M v[168:171],a[84:87],a[40:43],v[168:171]
X a[16:19],v13,4096
M v[172:175],a[84:87],a[44:47],v[172:175]
global_load_lds_dwordx4 v12, s[40:41] offset:2048 sc1
M v[176:179],a[84:87],a[48:51],v[176:179]
M v[180:183],a[84:87],a[52:55],v[180:183]
X a[20:23],v13,5120
M v[184:187],a[84:87],a[56:59],v[184:187]
M v[188:191],a[84:87],a[60:63],v[188:191]
M v[192:195],a[88:91],a[32:35],v[192:195]
M v[196:199],a[88:91],a[36:39],v[196:199]
X a[24:27],v13,6144
M v[200:203],a[88:91],a[40:43],v[200:203]
M v[204:207],a[88:91],a[44:47],v[204:207]
M v[224:227],a[92:95],a[32:35],v[224:227]
global_load_lds_dwordx4 v12, s[40:41] offset:3072 sc1
M v[228:231],a[92:95],a[36:39],v[228:231]
v_add_u32 v12, 0x4000, v12
M v[232:235],a[92:95],a[40:43],v[232:235]
X a[28:31],v13,7168
M v[236:239],a[92:95],a[44:47],v[236:239]
M v[208:211],a[96:99],a[32:35],v[208:211]
M v[212:215],a[96:99],a[36:39],v[212:215]
M v[216:219],a[96:99],a[40:43],v[216:219]
M v[220:223],a[96:99],a[44:47],v[220:223]
M v[240:243],a[100:103],a[32:35],v[240:243]
M v[244:247],a[100:103],a[36:39],v[244:247]
M v[248:251],a[100:103],a[40:43],v[248:251]
M v[252:255],a[100:103],a[44:47],v[252:255]
s_waitcnt lgkmcnt(0)
M a[128:131],a[72:75],a[0:3],0
T a[84:85],v32,1024
M a[132:135],a[72:75],a[4:7],0
T a[86:87],v32,5120
M a[136:139],a[72:75],a[8:11],0
M a[140:143],a[72:75],a[12:15],0
v_mul_f32 v64, v64, v128
v_mul_f32 v68, v68, v132
M a[144:147],a[72:75],a[16:19],0
v_mul_f32 v65, v65, v129
v_mul_f32 v69, v69, v133
M a[148:151],a[72:75],a[20:23],0
v_mul_f32 v66, v66, v130
v_mul_f32 v70, v70, v134
M a[152:155],a[72:75],a[24:27],0
v_mul_f32 v67, v67, v131
v_mul_f32 v71, v71, v135
M a[156:159],a[72:75],a[28:31],0
s_waitcnt vmcnt(24) lgkmcnt(0)
s_barrier
M a[192:195],a[76:79],a[0:3],0
T a[72:73],v9,8192
s_add_u32 m0, s54, 131072
M a[196:199],a[76:79],a[4:7],0
T a[74:75],v9,12288
global_load_lds_dwordx4 v12, s[40:41] offset:0 sc1
M a[200:203],a[76:79],a[8:11],0
X a[32:35],v13,16384
v_mul_f32 v96, v96, v160
M a[204:207],a[76:79],a[12:15],0
X a[36:39],v13,17408
v_mul_f32 v100, v100, v164
M a[208:211],a[76:79],a[16:19],0
X a[40:43],v13,18432
v_mul_f32 v97, v97, v161
M a[212:215],a[76:79],a[20:23],0
global_load_lds_dwordx4 v12, s[40:41] offset:1024 sc1
v_mul_f32 v101, v101, v165
M a[216:219],a[76:79],a[24:27],0
X a[44:47],v13,19456
v_mul_f32 v98, v98, v162
M a[220:223],a[76:79],a[28:31],0
global_load_lds_dwordx4 v12, s[40:41] offset:2048 sc1
v_mul_f32 v102, v102, v166
M a[160:163],a[80:83],a[0:3],0
T a[76:77],v9,9216
v_mul_f32 v99, v99, v163
M a[164:167],a[80:83],a[4:7],0
T a[78:79],v9,13312
v_mul_f32 v103, v103, v167
M a[168:171],a[80:83],a[8:11],0
X a[48:51],v13,20480
v_cvt_pk_bf16_f32 v64, v64, v65
M a[172:175],a[80:83],a[12:15],0
X a[52:55],v13,21504
v_cvt_pk_bf16_f32 v68, v68, v69
M a[176:179],a[80:83],a[16:19],0
X a[56:59],v13,22528
v_cvt_pk_bf16_f32 v65, v66, v67
M a[180:183],a[80:83],a[20:23],0
global_load_lds_dwordx4 v12, s[40:41] offset:3072 sc1
v_cvt_pk_bf16_f32 v69, v70, v71
M a[184:187],a[80:83],a[24:27],0
X a[60:63],v13,23552
v_cvt_pk_bf16_f32 v66, v96, v97
M a[188:191],a[80:83],a[28:31],0
v_add_u32 v12, 0x4000, v12
v_cvt_pk_bf16_f32 v70, v100, v101
M a[224:227],a[84:87],a[0:3],0
T a[80:81],v32,8192
v_cvt_pk_bf16_f32 v67, v98, v99
M a[228:231],a[84:87],a[4:7],0
T a[82:83],v32,12288
v_cvt_pk_bf16_f32 v71, v102, v103
M a[232:235],a[84:87],a[8:11],0
s_add_u32 m0, s55, 0
global_store_dwordx4 v16, v[64:67], s[58:59] offset:2048 sc1
M a[236:239],a[84:87],a[12:15],0
global_load_lds_dwordx4 v11, s[50:51] offset:0
global_store_dwordx4 v16, v[68:71], s[68:69] offset:2048 sc1
M a[240:243],a[84:87],a[16:19],0
global_load_lds_dwordx4 v11, s[52:53] offset:1024
v_mul_f32 v192, v192, v224
M a[244:247],a[84:87],a[20:23],0
v_add_u32 v11, 229376, v11
v_mul_f32 v196, v196, v228
M a[248:251],a[84:87],a[24:27],0
v_mul_f32 v193, v193, v225
v_mul_f32 v197, v197, v229
M a[252:255],a[84:87],a[28:31],0
s_waitcnt lgkmcnt(2)
v_mul_f32 v194, v194, v226
M a[128:131],a[72:75],a[32:35],a[128:131]
T a[84:85],v32,9216
v_mul_f32 v198, v198, v230
M a[132:135],a[72:75],a[36:39],a[132:135]
T a[86:87],v32,13312
v_mul_f32 v195, v195, v227
M a[136:139],a[72:75],a[40:43],a[136:139]
v_mul_f32 v199, v199, v231
v_mul_f32 v208, v208, v240
M a[140:143],a[72:75],a[44:47],a[140:143]
v_mul_f32 v212, v212, v244
v_mul_f32 v209, v209, v241
M a[144:147],a[72:75],a[48:51],a[144:147]
v_mul_f32 v213, v213, v245
v_mul_f32 v210, v210, v242
M a[148:151],a[72:75],a[52:55],a[148:151]
v_mul_f32 v214, v214, v246
v_mul_f32 v211, v211, v243
M a[152:155],a[72:75],a[56:59],a[152:155]
v_mul_f32 v215, v215, v247
v_cvt_pk_bf16_f32 v192, v192, v193
M a[156:159],a[72:75],a[60:63],a[156:159]
s_waitcnt vmcnt(26) lgkmcnt(0)
s_barrier
M a[192:195],a[76:79],a[32:35],a[192:195]
T a[72:73],v9,16384
s_add_u32 m0, s54, 147456
M a[196:199],a[76:79],a[36:39],a[196:199]
T a[74:75],v9,20480
global_load_lds_dwordx4 v12, s[40:41] offset:0 sc1
M a[200:203],a[76:79],a[40:43],a[200:203]
X a[0:3],v8,0
v_cvt_pk_bf16_f32 v196, v196, v197
M a[204:207],a[76:79],a[44:47],a[204:207]
X a[4:7],v8,1024
v_cvt_pk_bf16_f32 v193, v194, v195
M a[208:211],a[76:79],a[48:51],a[208:211]
X a[8:11],v8,2048
v_cvt_pk_bf16_f32 v197, v198, v199
M a[212:215],a[76:79],a[52:55],a[212:215]
global_load_lds_dwordx4 v12, s[40:41] offset:1024 sc1
v_cvt_pk_bf16_f32 v194, v208, v209
M a[216:219],a[76:79],a[56:59],a[216:219]
X a[12:15],v8,3072
v_cvt_pk_bf16_f32 v198, v212, v213
M a[220:223],a[76:79],a[60:63],a[220:223]
global_load_lds_dwordx4 v12, s[40:41] offset:2048 sc1
v_cvt_pk_bf16_f32 v195, v210, v211
M a[160:163],a[80:83],a[32:35],a[160:163]
T a[76:77],v9,17408
v_cvt_pk_bf16_f32 v199, v214, v215
M a[164:167],a[80:83],a[36:39],a[164:167]
T a[78:79],v9,21504
global_store_dwordx4 v33, v[192:195], s[58:59] sc1
M a[168:171],a[80:83],a[40:43],a[168:171]
X a[16:19],v8,4096
global_store_dwordx4 v33, v[196:199], s[68:69] sc1
M a[172:175],a[80:83],a[44:47],a[172:175]
X a[20:23],v8,5120
s_add_u32 s58, s58, 294912
s_addc_u32 s59, s59, 0
M a[176:179],a[80:83],a[48:51],a[176:179]
X a[24:27],v8,6144
v_mul_f32 v72, v72, v136
M a[180:183],a[80:83],a[52:55],a[180:183]
global_load_lds_dwordx4 v12, s[40:41] offset:3072 sc1
v_mul_f32 v73, v73, v137
M a[184:187],a[80:83],a[56:59],a[184:187]
X a[28:31],v8,7168
v_mul_f32 v74, v74, v138
M a[188:191],a[80:83],a[60:63],a[188:191]
v_add_u32 v12, 0x4000, v12
v_mul_f32 v75, v75, v139
M a[224:227],a[84:87],a[32:35],a[224:227]
T a[80:81],v32,16384
v_mul_f32 v104, v104, v168
M a[228:231],a[84:87],a[36:39],a[228:231]
T a[82:83],v32,20480
v_mul_f32 v105, v105, v169
M a[232:235],a[84:87],a[40:43],a[232:235]
s_add_u32 m0, s55, 8192
v_mul_f32 v106, v106, v170
M a[236:239],a[84:87],a[44:47],a[236:239]
global_load_lds_dwordx4 v11, s[50:51] offset:0
v_mul_f32 v107, v107, v171
M a[240:243],a[84:87],a[48:51],a[240:243]
global_load_lds_dwordx4 v11, s[52:53] offset:1024
v_cvt_pk_bf16_f32 v72, v72, v73
M a[244:247],a[84:87],a[52:55],a[244:247]
v_add_u32 v11, 229376, v11
v_cvt_pk_bf16_f32 v73, v74, v75
M a[248:251],a[84:87],a[56:59],a[248:251]
s_add_u32 s68, s68, 294912
s_addc_u32 s69, s69, 0
M a[252:255],a[84:87],a[60:63],a[252:255]
s_waitcnt lgkmcnt(2)
v_mul_f32 v76, v76, v140
M a[128:131],a[72:75],a[0:3],a[128:131]
T a[84:85],v32,17408
v_mul_f32 v77, v77, v141
M a[132:135],a[72:75],a[4:7],a[132:135]
T a[86:87],v32,21504
v_mul_f32 v78, v78, v142
M a[136:139],a[72:75],a[8:11],a[136:139]
v_mul_f32 v79, v79, v143
v_mul_f32 v108, v108, v172
M a[140:143],a[72:75],a[12:15],a[140:143]
v_mul_f32 v109, v109, v173
v_mul_f32 v110, v110, v174
M a[144:147],a[72:75],a[16:19],a[144:147]
v_mul_f32 v111, v111, v175
v_cvt_pk_bf16_f32 v76, v76, v77
M a[148:151],a[72:75],a[20:23],a[148:151]
v_cvt_pk_bf16_f32 v77, v78, v79
v_cvt_pk_bf16_f32 v74, v104, v105
M a[152:155],a[72:75],a[24:27],a[152:155]
v_cvt_pk_bf16_f32 v78, v108, v109
v_cvt_pk_bf16_f32 v75, v106, v107
M a[156:159],a[72:75],a[28:31],a[156:159]
s_waitcnt vmcnt(28) lgkmcnt(0)
s_barrier
M a[192:195],a[76:79],a[0:3],a[192:195]
T a[72:73],v9,24576
s_add_u32 m0, s54, 0
M a[196:199],a[76:79],a[4:7],a[196:199]
T a[74:75],v9,28672
global_load_lds_dwordx4 v12, s[40:41] offset:0 sc1
M a[200:203],a[76:79],a[8:11],a[200:203]
X a[32:35],v8,16384
v_cvt_pk_bf16_f32 v79, v110, v111
M a[204:207],a[76:79],a[12:15],a[204:207]
X a[36:39],v8,17408
global_store_dwordx4 v16, v[72:75], s[58:59] offset:2048 sc1
M a[208:211],a[76:79],a[16:19],a[208:211]
X a[40:43],v8,18432
global_store_dwordx4 v16, v[76:79], s[68:69] offset:2048 sc1
M a[212:215],a[76:79],a[20:23],a[212:215]
global_load_lds_dwordx4 v12, s[40:41] offset:1024 sc1
v_mul_f32 v200, v200, v232
M a[216:219],a[76:79],a[24:27],a[216:219]
X a[44:47],v8,19456
v_mul_f32 v204, v204, v236
M a[220:223],a[76:79],a[28:31],a[220:223]
global_load_lds_dwordx4 v12, s[40:41] offset:2048 sc1
v_mul_f32 v201, v201, v233
M a[160:163],a[80:83],a[0:3],a[160:163]
T a[76:77],v9,25600
v_mul_f32 v205, v205, v237
M a[164:167],a[80:83],a[4:7],a[164:167]
T a[78:79],v9,29696
v_mul_f32 v202, v202, v234
M a[168:171],a[80:83],a[8:11],a[168:171]
X a[48:51],v8,20480
v_mul_f32 v206, v206, v238
M a[172:175],a[80:83],a[12:15],a[172:175]
X a[52:55],v8,21504
v_mul_f32 v203, v203, v235
M a[176:179],a[80:83],a[16:19],a[176:179]
X a[56:59],v8,22528
v_mul_f32 v207, v207, v239
M a[180:183],a[80:83],a[20:23],a[180:183]
global_load_lds_dwordx4 v12, s[40:41] offset:3072 sc1
v_mul_f32 v216, v216, v248
M a[184:187],a[80:83],a[24:27],a[184:187]
X a[60:63],v8,23552
v_mul_f32 v220, v220, v252
M a[188:191],a[80:83],a[28:31],a[188:191]
v_add_u32 v12, 0x4000, v12
v_mul_f32 v217, v217, v249
M a[224:227],a[84:87],a[0:3],a[224:227]
T a[80:81],v32,24576
v_mul_f32 v221, v221, v253
M a[228:231],a[84:87],a[4:7],a[228:231]
T a[82:83],v32,28672
v_mul_f32 v218, v218, v250
M a[232:235],a[84:87],a[8:11],a[232:235]
s_add_u32 m0, s55, 16384
v_mul_f32 v222, v222, v254
M a[236:239],a[84:87],a[12:15],a[236:239]
global_load_lds_dwordx4 v11, s[50:51] offset:0
v_mul_f32 v219, v219, v251
M a[240:243],a[84:87],a[16:19],a[240:243]
global_load_lds_dwordx4 v11, s[52:53] offset:1024
v_mul_f32 v223, v223, v255
M a[244:247],a[84:87],a[20:23],a[244:247]
v_add_u32 v11, 229376, v11
v_cvt_pk_bf16_f32 v200, v200, v201
M a[248:251],a[84:87],a[24:27],a[248:251]
v_cvt_pk_bf16_f32 v204, v204, v205
v_cvt_pk_bf16_f32 v201, v202, v203
M a[252:255],a[84:87],a[28:31],a[252:255]
s_waitcnt lgkmcnt(2)
v_cvt_pk_bf16_f32 v205, v206, v207
M a[128:131],a[72:75],a[32:35],a[128:131]
T a[84:85],v32,25600
v_cvt_pk_bf16_f32 v202, v216, v217
M a[132:135],a[72:75],a[36:39],a[132:135]
T a[86:87],v32,29696
v_cvt_pk_bf16_f32 v206, v220, v221
M a[136:139],a[72:75],a[40:43],a[136:139]
v_cvt_pk_bf16_f32 v203, v218, v219
v_cvt_pk_bf16_f32 v207, v222, v223
M a[140:143],a[72:75],a[44:47],a[140:143]
global_store_dwordx4 v33, v[200:203], s[58:59] sc1
global_store_dwordx4 v33, v[204:207], s[68:69] sc1
M a[144:147],a[72:75],a[48:51],a[144:147]
s_add_u32 s58, s58, s72
s_addc_u32 s59, s59, s73
M a[148:151],a[72:75],a[52:55],a[148:151]
s_add_u32 s68, s68, s72
s_addc_u32 s69, s69, s73
M a[152:155],a[72:75],a[56:59],a[152:155]
v_mul_f32 v80, v80, v144
v_mul_f32 v84, v84, v148
M a[156:159],a[72:75],a[60:63],a[156:159]
s_waitcnt vmcnt(32) lgkmcnt(0)
s_barrier
M a[192:195],a[76:79],a[32:35],a[192:195]
T a[72:73],v9,32768
s_add_u32 m0, s54, 16384
M a[196:199],a[76:79],a[36:39],a[196:199]
T a[74:75],v9,36864
global_load_lds_dwordx4 v12, s[40:41] offset:0 sc1
M a[200:203],a[76:79],a[40:43],a[200:203]
X a[0:3],v8,32768
v_mul_f32 v81, v81, v145
M a[204:207],a[76:79],a[44:47],a[204:207]
X a[4:7],v8,33792
v_mul_f32 v85, v85, v149
M a[208:211],a[76:79],a[48:51],a[208:211]
X a[8:11],v8,34816
v_mul_f32 v82, v82, v146
M a[212:215],a[76:79],a[52:55],a[212:215]
global_load_lds_dwordx4 v12, s[40:41] offset:1024 sc1
v_mul_f32 v86, v86, v150
M a[216:219],a[76:79],a[56:59],a[216:219]
X a[12:15],v8,35840
v_mul_f32 v83, v83, v147
M a[220:223],a[76:79],a[60:63],a[220:223]
global_load_lds_dwordx4 v12, s[40:41] offset:2048 sc1
v_mul_f32 v87, v87, v151
M a[160:163],a[80:83],a[32:35],a[160:163]
T a[76:77],v9,33792
v_mul_f32 v112, v112, v176
M a[164:167],a[80:83],a[36:39],a[164:167]
T a[78:79],v9,37888
v_mul_f32 v116, v116, v180
M a[168:171],a[80:83],a[40:43],a[168:171]
X a[16:19],v8,36864
v_mul_f32 v113, v113, v177
M a[172:175],a[80:83],a[44:47],a[172:175]
X a[20:23],v8,37888
v_mul_f32 v117, v117, v181
M a[176:179],a[80:83],a[48:51],a[176:179]
X a[24:27],v8,38912
v_mul_f32 v114, v114, v178
M a[180:183],a[80:83],a[52:55],a[180:183]
global_load_lds_dwordx4 v12, s[40:41] offset:3072 sc1
v_mul_f32 v118, v118, v182
M a[184:187],a[80:83],a[56:59],a[184:187]
X a[28:31],v8,39936
v_mul_f32 v115, v115, v179
M a[188:191],a[80:83],a[60:63],a[188:191]
v_add_u32 v12, 0x4000, v12
v_mul_f32 v119, v119, v183
M a[224:227],a[84:87],a[32:35],a[224:227]
T a[80:81],v32,32768
v_cvt_pk_bf16_f32 v80, v80, v81
M a[228:231],a[84:87],a[36:39],a[228:231]
T a[82:83],v32,36864
v_cvt_pk_bf16_f32 v84, v84, v85
M a[232:235],a[84:87],a[40:43],a[232:235]
s_add_u32 m0, s55, 24576
v_cvt_pk_bf16_f32 v81, v82, v83
M a[236:239],a[84:87],a[44:47],a[236:239]
global_load_lds_dwordx4 v11, s[50:51] offset:0
v_cvt_pk_bf16_f32 v85, v86, v87
M a[240:243],a[84:87],a[48:51],a[240:243]
global_load_lds_dwordx4 v11, s[52:53] offset:1024
v_cvt_pk_bf16_f32 v82, v112, v113
M a[244:247],a[84:87],a[52:55],a[244:247]
v_add_u32 v11, 229376, v11
v_cvt_pk_bf16_f32 v86, v116, v117
M a[248:251],a[84:87],a[56:59],a[248:251]
v_cvt_pk_bf16_f32 v83, v114, v115
v_cvt_pk_bf16_f32 v87, v118, v119
M a[252:255],a[84:87],a[60:63],a[252:255]
s_waitcnt lgkmcnt(2)
global_store_dwordx4 v16, v[80:83], s[58:59] offset:2048 sc1
M a[128:131],a[72:75],a[0:3],a[128:131]
T a[84:85],v32,33792
global_store_dwordx4 v16, v[84:87], s[68:69] offset:2048 sc1
M a[132:135],a[72:75],a[4:7],a[132:135]
T a[86:87],v32,37888
s_add_u32 s58, s58, 294912
s_addc_u32 s59, s59, 0
M a[136:139],a[72:75],a[8:11],a[136:139]
s_add_u32 s68, s68, 294912
s_addc_u32 s69, s69, 0
M a[140:143],a[72:75],a[12:15],a[140:143]
v_mul_f32 v88, v88, v152
v_mul_f32 v92, v92, v156
M a[144:147],a[72:75],a[16:19],a[144:147]
v_mul_f32 v89, v89, v153
v_mul_f32 v93, v93, v157
M a[148:151],a[72:75],a[20:23],a[148:151]
v_mul_f32 v90, v90, v154
v_mul_f32 v94, v94, v158
M a[152:155],a[72:75],a[24:27],a[152:155]
v_mul_f32 v91, v91, v155
v_mul_f32 v95, v95, v159
M a[156:159],a[72:75],a[28:31],a[156:159]
s_waitcnt vmcnt(34) lgkmcnt(0)
s_barrier
M a[192:195],a[76:79],a[0:3],a[192:195]
T a[72:73],v9,40960
s_add_u32 m0, s54, 32768
M a[196:199],a[76:79],a[4:7],a[196:199]
T a[74:75],v9,45056
global_load_lds_dwordx4 v12, s[40:41] offset:0 sc1
M a[200:203],a[76:79],a[8:11],a[200:203]
X a[32:35],v8,49152
v_mul_f32 v120, v120, v184
M a[204:207],a[76:79],a[12:15],a[204:207]
X a[36:39],v8,50176
v_mul_f32 v124, v124, v188
M a[208:211],a[76:79],a[16:19],a[208:211]
X a[40:43],v8,51200
v_mul_f32 v121, v121, v185
M a[212:215],a[76:79],a[20:23],a[212:215]
global_load_lds_dwordx4 v12, s[40:41] offset:1024 sc1
v_mul_f32 v125, v125, v189
M a[216:219],a[76:79],a[24:27],a[216:219]
X a[44:47],v8,52224
v_mul_f32 v122, v122, v186
M a[220:223],a[76:79],a[28:31],a[220:223]
global_load_lds_dwordx4 v12, s[40:41] offset:2048 sc1
v_mul_f32 v126, v126, v190
M a[160:163],a[80:83],a[0:3],a[160:163]
T a[76:77],v9,41984
v_mul_f32 v123, v123, v187
M a[164:167],a[80:83],a[4:7],a[164:167]
T a[78:79],v9,46080
v_mul_f32 v127, v127, v191
M a[168:171],a[80:83],a[8:11],a[168:171]
X a[48:51],v8,53248
v_cvt_pk_bf16_f32 v88, v88, v89
M a[172:175],a[80:83],a[12:15],a[172:175]
X a[52:55],v8,54272
v_cvt_pk_bf16_f32 v92, v92, v93
M a[176:179],a[80:83],a[16:19],a[176:179]
X a[56:59],v8,55296
v_cvt_pk_bf16_f32 v89, v90, v91
M a[180:183],a[80:83],a[20:23],a[180:183]
global_load_lds_dwordx4 v12, s[40:41] offset:3072 sc1
v_cvt_pk_bf16_f32 v93, v94, v95
M a[184:187],a[80:83],a[24:27],a[184:187]
X a[60:63],v8,56320
v_cvt_pk_bf16_f32 v90, v120, v121
M a[188:191],a[80:83],a[28:31],a[188:191]
v_add_u32 v12, 0x4000, v12
v_cvt_pk_bf16_f32 v94, v124, v125
M a[224:227],a[84:87],a[0:3],a[224:227]
T a[80:81],v32,40960
v_cvt_pk_bf16_f32 v91, v122, v123
M a[228:231],a[84:87],a[4:7],a[228:231]
T a[82:83],v32,45056
v_cvt_pk_bf16_f32 v95, v126, v127
M a[232:235],a[84:87],a[8:11],a[232:235]
s_add_u32 m0, s55, 32768
global_store_dwordx4 v16, v[88:91], s[58:59] offset:2048 sc1
M a[236:239],a[84:87],a[12:15],a[236:239]
global_load_lds_dwordx4 v11, s[50:51] offset:0
global_store_dwordx4 v16, v[92:95], s[68:69] offset:2048 sc1
M a[240:243],a[84:87],a[16:19],a[240:243]
global_load_lds_dwordx4 v11, s[52:53] offset:1024
s_waitcnt vmcnt(63)
M a[244:247],a[84:87],a[20:23],a[244:247]
v_add_u32 v11, 229376, v11
v_lshlrev_b32 v64, 16, v24
M a[248:251],a[84:87],a[24:27],a[248:251]
v_and_b32 v65, 0xffff0000, v24
v_lshlrev_b32 v66, 16, v25
M a[252:255],a[84:87],a[28:31],a[252:255]
s_waitcnt lgkmcnt(2)
v_and_b32 v67, 0xffff0000, v25
M a[128:131],a[72:75],a[32:35],a[128:131]
T a[84:85],v32,41984
v_lshlrev_b32 v68, 16, v26
M a[132:135],a[72:75],a[36:39],a[132:135]
T a[86:87],v32,46080
v_and_b32 v69, 0xffff0000, v26
M a[136:139],a[72:75],a[40:43],a[136:139]
v_lshlrev_b32 v70, 16, v27
v_and_b32 v71, 0xffff0000, v27
M a[140:143],a[72:75],a[44:47],a[140:143]
v_lshlrev_b32 v72, 16, v28
v_and_b32 v73, 0xffff0000, v28
M a[144:147],a[72:75],a[48:51],a[144:147]
v_lshlrev_b32 v74, 16, v29
v_and_b32 v75, 0xffff0000, v29
M a[148:151],a[72:75],a[52:55],a[148:151]
v_lshlrev_b32 v76, 16, v30
v_and_b32 v77, 0xffff0000, v30
M a[152:155],a[72:75],a[56:59],a[152:155]
v_lshlrev_b32 v78, 16, v31
v_and_b32 v79, 0xffff0000, v31
M a[156:159],a[72:75],a[60:63],a[156:159]
s_waitcnt vmcnt(34) lgkmcnt(0)
s_barrier
M a[192:195],a[76:79],a[32:35],a[192:195]
T a[72:73],v9,0
s_add_u32 m0, s54, 49152
M a[196:199],a[76:79],a[36:39],a[196:199]
T a[74:75],v9,4096
global_load_lds_dwordx4 v12, s[40:41] offset:0 sc1
M a[200:203],a[76:79],a[40:43],a[200:203]
X a[0:3],v13,0
v_lshlrev_b32 v80, 16, v40
M a[204:207],a[76:79],a[44:47],a[204:207]
X a[4:7],v13,1024
v_and_b32 v81, 0xffff0000, v40
M a[208:211],a[76:79],a[48:51],a[208:211]
X a[8:11],v13,2048
v_lshlrev_b32 v82, 16, v41
M a[212:215],a[76:79],a[52:55],a[212:215]
global_load_lds_dwordx4 v12, s[40:41] offset:1024 sc1
v_and_b32 v83, 0xffff0000, v41
M a[216:219],a[76:79],a[56:59],a[216:219]
X a[12:15],v13,3072
v_lshlrev_b32 v84, 16, v42
M a[220:223],a[76:79],a[60:63],a[220:223]
global_load_lds_dwordx4 v12, s[40:41] offset:2048 sc1
v_and_b32 v85, 0xffff0000, v42
M a[160:163],a[80:83],a[32:35],a[160:163]
T a[76:77],v9,1024
v_lshlrev_b32 v86, 16, v43
M a[164:167],a[80:83],a[36:39],a[164:167]
T a[78:79],v9,5120
v_and_b32 v87, 0xffff0000, v43
M a[168:171],a[80:83],a[40:43],a[168:171]
X a[16:19],v13,4096
v_lshlrev_b32 v88, 16, v44
M a[172:175],a[80:83],a[44:47],a[172:175]
X a[20:23],v13,5120
v_and_b32 v89, 0xffff0000, v44
M a[176:179],a[80:83],a[48:51],a[176:179]
X a[24:27],v13,6144
v_lshlrev_b32 v90, 16, v45
M a[180:183],a[80:83],a[52:55],a[180:183]
global_load_lds_dwordx4 v12, s[40:41] offset:3072 sc1
v_and_b32 v91, 0xffff0000, v45
M a[184:187],a[80:83],a[56:59],a[184:187]
X a[28:31],v13,7168
v_lshlrev_b32 v92, 16, v46
M a[188:191],a[80:83],a[60:63],a[188:191]
v_add_u32 v12, 0x4000, v12
v_and_b32 v93, 0xffff0000, v46
M a[224:227],a[84:87],a[32:35],a[224:227]
T a[80:81],v32,0
v_lshlrev_b32 v94, 16, v47
M a[228:231],a[84:87],a[36:39],a[228:231]
T a[82:83],v32,4096
v_and_b32 v95, 0xffff0000, v47
M a[232:235],a[84:87],a[40:43],a[232:235]
s_add_u32 m0, s55, 40960
v_lshlrev_b32 v96, 16, v48
M a[236:239],a[84:87],a[44:47],a[236:239]
global_load_lds_dwordx4 v11, s[50:51] offset:0
v_and_b32 v97, 0xffff0000, v48
M a[240:243],a[84:87],a[48:51],a[240:243]
global_load_lds_dwordx4 v11, s[52:53] offset:1024
v_lshlrev_b32 v98, 16, v49
M a[244:247],a[84:87],a[52:55],a[244:247]
v_add_u32 v11, 229376, v11
v_and_b32 v99, 0xffff0000, v49
M a[248:251],a[84:87],a[56:59],a[248:251]
v_lshlrev_b32 v100, 16, v50
v_and_b32 v101, 0xffff0000, v50
M a[252:255],a[84:87],a[60:63],a[252:255]
s_waitcnt lgkmcnt(2)
v_lshlrev_b32 v102, 16, v51
M a[128:131],a[72:75],a[0:3],a[128:131]
T a[84:85],v32,1024
v_and_b32 v103, 0xffff0000, v51
M a[132:135],a[72:75],a[4:7],a[132:135]
T a[86:87],v32,5120
v_lshlrev_b32 v104, 16, v52
M a[136:139],a[72:75],a[8:11],a[136:139]
v_and_b32 v105, 0xffff0000, v52
v_lshlrev_b32 v106, 16, v53
M a[140:143],a[72:75],a[12:15],a[140:143]
v_and_b32 v107, 0xffff0000, v53
v_lshlrev_b32 v108, 16, v54
M a[144:147],a[72:75],a[16:19],a[144:147]
v_and_b32 v109, 0xffff0000, v54
v_lshlrev_b32 v110, 16, v55
M a[148:151],a[72:75],a[20:23],a[148:151]
v_and_b32 v111, 0xffff0000, v55
v_lshlrev_b32 v112, 16, v56
M a[152:155],a[72:75],a[24:27],a[152:155]
v_and_b32 v113, 0xffff0000, v56
v_lshlrev_b32 v114, 16, v57
M a[156:159],a[72:75],a[28:31],a[156:159]
s_waitcnt vmcnt(32) lgkmcnt(0)
s_barrier
M a[192:195],a[76:79],a[0:3],a[192:195]
T a[72:73],v9,8192
s_add_u32 m0, s54, 131072
M a[196:199],a[76:79],a[4:7],a[196:199]
T a[74:75],v9,12288
global_load_lds_dwordx4 v12, s[40:41] offset:0 sc1
M a[200:203],a[76:79],a[8:11],a[200:203]
X a[32:35],v13,16384
v_and_b32 v115, 0xffff0000, v57
M a[204:207],a[76:79],a[12:15],a[204:207]
X a[36:39],v13,17408
v_lshlrev_b32 v116, 16, v58
M a[208:211],a[76:79],a[16:19],a[208:211]
X a[40:43],v13,18432
v_and_b32 v117, 0xffff0000, v58
M a[212:215],a[76:79],a[20:23],a[212:215]
global_load_lds_dwordx4 v12, s[40:41] offset:1024 sc1
v_lshlrev_b32 v118, 16, v59
M a[216:219],a[76:79],a[24:27],a[216:219]
X a[44:47],v13,19456
v_and_b32 v119, 0xffff0000, v59
M a[220:223],a[76:79],a[28:31],a[220:223]
global_load_lds_dwordx4 v12, s[40:41] offset:2048 sc1
v_lshlrev_b32 v120, 16, v60
M a[160:163],a[80:83],a[0:3],a[160:163]
T a[76:77],v9,9216
v_and_b32 v121, 0xffff0000, v60
M a[164:167],a[80:83],a[4:7],a[164:167]
T a[78:79],v9,13312
v_lshlrev_b32 v122, 16, v61
M a[168:171],a[80:83],a[8:11],a[168:171]
X a[48:51],v13,20480
v_and_b32 v123, 0xffff0000, v61
M a[172:175],a[80:83],a[12:15],a[172:175]
X a[52:55],v13,21504
v_lshlrev_b32 v124, 16, v62
M a[176:179],a[80:83],a[16:19],a[176:179]
X a[56:59],v13,22528
v_and_b32 v125, 0xffff0000, v62
M a[180:183],a[80:83],a[20:23],a[180:183]
global_load_lds_dwordx4 v12, s[40:41] offset:3072 sc1
v_lshlrev_b32 v126, 16, v63
M a[184:187],a[80:83],a[24:27],a[184:187]
X a[60:63],v13,23552
v_and_b32 v127, 0xffff0000, v63
M a[188:191],a[80:83],a[28:31],a[188:191]
v_add_u32 v12, 0x4000, v12
M a[224:227],a[84:87],a[0:3],a[224:227]
T a[80:81],v32,8192
M a[228:231],a[84:87],a[4:7],a[228:231]
T a[82:83],v32,12288
M a[232:235],a[84:87],a[8:11],a[232:235]
s_add_u32 m0, s55, 0
M a[236:239],a[84:87],a[12:15],a[236:239]
global_load_lds_dwordx4 v11, s[50:51] offset:0
M a[240:243],a[84:87],a[16:19],a[240:243]
global_load_lds_dwordx4 v11, s[52:53] offset:1024
M a[244:247],a[84:87],a[20:23],a[244:247]
v_add_u32 v11, 229376, v11
M a[248:251],a[84:87],a[24:27],a[248:251]
M a[252:255],a[84:87],a[28:31],a[252:255]
s_waitcnt lgkmcnt(2)
M a[128:131],a[72:75],a[32:35],a[128:131]
T a[84:85],v32,9216
M a[132:135],a[72:75],a[36:39],a[132:135]
T a[86:87],v32,13312
M a[136:139],a[72:75],a[40:43],a[136:139]
M a[140:143],a[72:75],a[44:47],a[140:143]
M a[144:147],a[72:75],a[48:51],a[144:147]
M a[148:151],a[72:75],a[52:55],a[148:151]
M a[152:155],a[72:75],a[56:59],a[152:155]
M a[156:159],a[72:75],a[60:63],a[156:159]
s_waitcnt vmcnt(30) lgkmcnt(0)
s_barrier
M a[192:195],a[76:79],a[32:35],a[192:195]
T a[72:73],v9,16384
s_add_u32 m0, s54, 147456
M a[196:199],a[76:79],a[36:39],a[196:199]
T a[74:75],v9,20480
global_load_lds_dwordx4 v12, s[40:41] offset:0 sc1
M a[200:203],a[76:79],a[40:43],a[200:203]
X a[0:3],v8,0
M a[204:207],a[76:79],a[44:47],a[204:207]
X a[4:7],v8,1024
M a[208:211],a[76:79],a[48:51],a[208:211]
X a[8:11],v8,2048
M a[212:215],a[76:79],a[52:55],a[212:215]
global_load_lds_dwordx4 v12, s[40:41] offset:1024 sc1
M a[216:219],a[76:79],a[56:59],a[216:219]
X a[12:15],v8,3072
M a[220:223],a[76:79],a[60:63],a[220:223]
global_load_lds_dwordx4 v12, s[40:41] offset:2048 sc1
M a[160:163],a[80:83],a[32:35],a[160:163]
T a[76:77],v9,17408
M a[164:167],a[80:83],a[36:39],a[164:167]
T a[78:79],v9,21504
M a[168:171],a[80:83],a[40:43],a[168:171]
X a[16:19],v8,4096
M a[172:175],a[80:83],a[44:47],a[172:175]
X a[20:23],v8,5120
M a[176:179],a[80:83],a[48:51],a[176:179]
X a[24:27],v8,6144
M a[180:183],a[80:83],a[52:55],a[180:183]
global_load_lds_dwordx4 v12, s[40:41] offset:3072 sc1
M a[184:187],a[80:83],a[56:59],a[184:187]
X a[28:31],v8,7168
M a[188:191],a[80:83],a[60:63],a[188:191]
v_add_u32 v12, 0x4000, v12
M a[224:227],a[84:87],a[32:35],a[224:227]
T a[80:81],v32,16384
M a[228:231],a[84:87],a[36:39],a[228:231]
T a[82:83],v32,20480
M a[232:235],a[84:87],a[40:43],a[232:235]
s_add_u32 m0, s55, 8192
M a[236:239],a[84:87],a[44:47],a[236:239]
global_load_lds_dwordx4 v11, s[50:51] offset:0
M a[240:243],a[84:87],a[48:51],a[240:243]
global_load_lds_dwordx4 v11, s[52:53] offset:1024
M a[244:247],a[84:87],a[52:55],a[244:247]
v_add_u32 v11, 229376, v11
M a[248:251],a[84:87],a[56:59],a[248:251]
M a[252:255],a[84:87],a[60:63],a[252:255]
s_waitcnt lgkmcnt(2)
M a[128:131],a[72:75],a[0:3],a[128:131]
T a[84:85],v32,17408
M a[132:135],a[72:75],a[4:7],a[132:135]
T a[86:87],v32,21504
M a[136:139],a[72:75],a[8:11],a[136:139]
M a[140:143],a[72:75],a[12:15],a[140:143]
M a[144:147],a[72:75],a[16:19],a[144:147]
M a[148:151],a[72:75],a[20:23],a[148:151]
M a[152:155],a[72:75],a[24:27],a[152:155]
M a[156:159],a[72:75],a[28:31],a[156:159]
s_waitcnt vmcnt(28) lgkmcnt(0)
s_barrier
M a[192:195],a[76:79],a[0:3],a[192:195]
T a[72:73],v9,24576
s_add_u32 m0, s54, 0
M a[196:199],a[76:79],a[4:7],a[196:199]
T a[74:75],v9,28672
global_load_lds_dwordx4 v12, s[40:41] offset:0 sc1
M a[200:203],a[76:79],a[8:11],a[200:203]
X a[32:35],v8,16384
M a[204:207],a[76:79],a[12:15],a[204:207]
X a[36:39],v8,17408
M a[208:211],a[76:79],a[16:19],a[208:211]
X a[40:43],v8,18432
M a[212:215],a[76:79],a[20:23],a[212:215]
global_load_lds_dwordx4 v12, s[40:41] offset:1024 sc1
M a[216:219],a[76:79],a[24:27],a[216:219]
X a[44:47],v8,19456
M a[220:223],a[76:79],a[28:31],a[220:223]
global_load_lds_dwordx4 v12, s[40:41] offset:2048 sc1
M a[160:163],a[80:83],a[0:3],a[160:163]
T a[76:77],v9,25600
M a[164:167],a[80:83],a[4:7],a[164:167]
T a[78:79],v9,29696
M a[168:171],a[80:83],a[8:11],a[168:171]
X a[48:51],v8,20480
M a[172:175],a[80:83],a[12:15],a[172:175]
X a[52:55],v8,21504
M a[176:179],a[80:83],a[16:19],a[176:179]
X a[56:59],v8,22528
M a[180:183],a[80:83],a[20:23],a[180:183]
global_load_lds_dwordx4 v12, s[40:41] offset:3072 sc1
M a[184:187],a[80:83],a[24:27],a[184:187]
X a[60:63],v8,23552
M a[188:191],a[80:83],a[28:31],a[188:191]
v_add_u32 v12, 0x4000, v12
M a[224:227],a[84:87],a[0:3],a[224:227]
T a[80:81],v32,24576
M a[228:231],a[84:87],a[4:7],a[228:231]
T a[82:83],v32,28672
M a[232:235],a[84:87],a[8:11],a[232:235]
s_add_u32 m0, s55, 16384
M a[236:239],a[84:87],a[12:15],a[236:239]
global_load_lds_dwordx4 v11, s[50:51] offset:0
M a[240:243],a[84:87],a[16:19],a[240:243]
global_load_lds_dwordx4 v11, s[52:53] offset:1024
M a[244:247],a[84:87],a[20:23],a[244:247]
v_add_u32 v11, 229376, v11
M a[248:251],a[84:87],a[24:27],a[248:251]
M a[252:255],a[84:87],a[28:31],a[252:255]
s_waitcnt lgkmcnt(2)
M a[128:131],a[72:75],a[32:35],a[128:131]
T a[84:85],v32,25600
M a[132:135],a[72:75],a[36:39],a[132:135]
T a[86:87],v32,29696
M a[136:139],a[72:75],a[40:43],a[136:139]
M a[140:143],a[72:75],a[44:47],a[140:143]
M a[144:147],a[72:75],a[48:51],a[144:147]
M a[148:151],a[72:75],a[52:55],a[148:151]
M a[152:155],a[72:75],a[56:59],a[152:155]
M a[156:159],a[72:75],a[60:63],a[156:159]
s_waitcnt vmcnt(24) lgkmcnt(0)
s_barrier
M a[192:195],a[76:79],a[32:35],a[192:195]
T a[72:73],v9,32768
s_add_u32 m0, s54, 16384
M a[196:199],a[76:79],a[36:39],a[196:199]
T a[74:75],v9,36864
global_load_lds_dwordx4 v12, s[40:41] offset:0 sc1
M a[200:203],a[76:79],a[40:43],a[200:203]
X a[0:3],v8,32768
M a[204:207],a[76:79],a[44:47],a[204:207]
X a[4:7],v8,33792
M a[208:211],a[76:79],a[48:51],a[208:211]
X a[8:11],v8,34816
M a[212:215],a[76:79],a[52:55],a[212:215]
global_load_lds_dwordx4 v12, s[40:41] offset:1024 sc1
M a[216:219],a[76:79],a[56:59],a[216:219]
X a[12:15],v8,35840
M a[220:223],a[76:79],a[60:63],a[220:223]
global_load_lds_dwordx4 v12, s[40:41] offset:2048 sc1
M a[160:163],a[80:83],a[32:35],a[160:163]
T a[76:77],v9,33792
M a[164:167],a[80:83],a[36:39],a[164:167]
T a[78:79],v9,37888
M a[168:171],a[80:83],a[40:43],a[168:171]
X a[16:19],v8,36864
M a[172:175],a[80:83],a[44:47],a[172:175]
X a[20:23],v8,37888
M a[176:179],a[80:83],a[48:51],a[176:179]
X a[24:27],v8,38912
M a[180:183],a[80:83],a[52:55],a[180:183]
global_load_lds_dwordx4 v12, s[40:41] offset:3072 sc1
M a[184:187],a[80:83],a[56:59],a[184:187]
X a[28:31],v8,39936
M a[188:191],a[80:83],a[60:63],a[188:191]
v_add_u32 v12, 0x4000, v12
M a[224:227],a[84:87],a[32:35],a[224:227]
T a[80:81],v32,32768
M a[228:231],a[84:87],a[36:39],a[228:231]
T a[82:83],v32,36864
M a[232:235],a[84:87],a[40:43],a[232:235]
s_add_u32 m0, s55, 24576
M a[236:239],a[84:87],a[44:47],a[236:239]
global_load_lds_dwordx4 v11, s[50:51] offset:0
M a[240:243],a[84:87],a[48:51],a[240:243]
global_load_lds_dwordx4 v11, s[52:53] offset:1024
M a[244:247],a[84:87],a[52:55],a[244:247]
v_add_u32 v11, 229376, v11
M a[248:251],a[84:87],a[56:59],a[248:251]
M a[252:255],a[84:87],a[60:63],a[252:255]
s_waitcnt lgkmcnt(2)
M a[128:131],a[72:75],a[0:3],a[128:131]
T a[84:85],v32,33792
M a[132:135],a[72:75],a[4:7],a[132:135]
T a[86:87],v32,37888
M a[136:139],a[72:75],a[8:11],a[136:139]
M a[140:143],a[72:75],a[12:15],a[140:143]
M a[144:147],a[72:75],a[16:19],a[144:147]
M a[148:151],a[72:75],a[20:23],a[148:151]
M a[152:155],a[72:75],a[24:27],a[152:155]
M a[156:159],a[72:75],a[28:31],a[156:159]
s_waitcnt vmcnt(24) lgkmcnt(0)
s_barrier
M a[192:195],a[76:79],a[0:3],a[192:195]
T a[72:73],v9,40960
s_add_u32 m0, s54, 32768
M a[196:199],a[76:79],a[4:7],a[196:199]
T a[74:75],v9,45056
global_load_lds_dwordx4 v12, s[40:41] offset:0 sc1
M a[200:203],a[76:79],a[8:11],a[200:203]
X a[32:35],v8,49152
M a[204:207],a[76:79],a[12:15],a[204:207]
X a[36:39],v8,50176
M a[208:211],a[76:79],a[16:19],a[208:211]
X a[40:43],v8,51200
M a[212:215],a[76:79],a[20:23],a[212:215]
global_load_lds_dwordx4 v12, s[40:41] offset:1024 sc1
M a[216:219],a[76:79],a[24:27],a[216:219]
X a[44:47],v8,52224
M a[220:223],a[76:79],a[28:31],a[220:223]
global_load_lds_dwordx4 v12, s[40:41] offset:2048 sc1
M a[160:163],a[80:83],a[0:3],a[160:163]
T a[76:77],v9,41984
M a[164:167],a[80:83],a[4:7],a[164:167]
T a[78:79],v9,46080
M a[168:171],a[80:83],a[8:11],a[168:171]
X a[48:51],v8,53248
M a[172:175],a[80:83],a[12:15],a[172:175]
X a[52:55],v8,54272
M a[176:179],a[80:83],a[16:19],a[176:179]
X a[56:59],v8,55296
M a[180:183],a[80:83],a[20:23],a[180:183]
global_load_lds_dwordx4 v12, s[40:41] offset:3072 sc1
M a[184:187],a[80:83],a[24:27],a[184:187]
X a[60:63],v8,56320
M a[188:191],a[80:83],a[28:31],a[188:191]
v_add_u32 v12, 0x4000, v12
M a[224:227],a[84:87],a[0:3],a[224:227]
T a[80:81],v32,40960
M a[228:231],a[84:87],a[4:7],a[228:231]
T a[82:83],v32,45056
M a[232:235],a[84:87],a[8:11],a[232:235]
s_add_u32 m0, s55, 32768
M a[236:239],a[84:87],a[12:15],a[236:239]
global_load_lds_dwordx4 v11, s[50:51] offset:0
M a[240:243],a[84:87],a[16:19],a[240:243]
global_load_lds_dwordx4 v11, s[52:53] offset:1024
M a[244:247],a[84:87],a[20:23],a[244:247]
v_add_u32 v11, 229376, v11
M a[248:251],a[84:87],a[24:27],a[248:251]
M a[252:255],a[84:87],a[28:31],a[252:255]
s_waitcnt lgkmcnt(2)
M a[128:131],a[72:75],a[32:35],a[128:131]
T a[84:85],v32,41984
M a[132:135],a[72:75],a[36:39],a[132:135]
T a[86:87],v32,46080
M a[136:139],a[72:75],a[40:43],a[136:139]
M a[140:143],a[72:75],a[44:47],a[140:143]
M a[144:147],a[72:75],a[48:51],a[144:147]
M a[148:151],a[72:75],a[52:55],a[148:151]
M a[152:155],a[72:75],a[56:59],a[152:155]
M a[156:159],a[72:75],a[60:63],a[156:159]
s_waitcnt vmcnt(24) lgkmcnt(0)
s_barrier
M a[192:195],a[76:79],a[32:35],a[192:195]
T a[72:73],v9,0
s_add_u32 m0, s54, 49152
M a[196:199],a[76:79],a[36:39],a[196:199]
T a[74:75],v9,4096
global_load_lds_dwordx4 v12, s[40:41] offset:0 sc1
M a[200:203],a[76:79],a[40:43],a[200:203]
X a[0:3],v13,0
M a[204:207],a[76:79],a[44:47],a[204:207]
X a[4:7],v13,1024
M a[208:211],a[76:79],a[48:51],a[208:211]
X a[8:11],v13,2048
M a[212:215],a[76:79],a[52:55],a[212:215]
global_load_lds_dwordx4 v12, s[40:41] offset:1024 sc1
M a[216:219],a[76:79],a[56:59],a[216:219]
X a[12:15],v13,3072
M a[220:223],a[76:79],a[60:63],a[220:223]
global_load_lds_dwordx4 v12, s[40:41] offset:2048 sc1
M a[160:163],a[80:83],a[32:35],a[160:163]
T a[76:77],v9,1024
M a[164:167],a[80:83],a[36:39],a[164:167]
T a[78:79],v9,5120
M a[168:171],a[80:83],a[40:43],a[168:171]
X a[16:19],v13,4096
M a[172:175],a[80:83],a[44:47],a[172:175]
X a[20:23],v13,5120
M a[176:179],a[80:83],a[48:51],a[176:179]
X a[24:27],v13,6144
M a[180:183],a[80:83],a[52:55],a[180:183]
global_load_lds_dwordx4 v12, s[40:41] offset:3072 sc1
M a[184:187],a[80:83],a[56:59],a[184:187]
X a[28:31],v13,7168
M a[188:191],a[80:83],a[60:63],a[188:191]
v_add_u32 v12, 0x4000, v12
M a[224:227],a[84:87],a[32:35],a[224:227]
T a[80:81],v32,0
M a[228:231],a[84:87],a[36:39],a[228:231]
T a[82:83],v32,4096
M a[232:235],a[84:87],a[40:43],a[232:235]
s_add_u32 m0, s55, 40960
M a[236:239],a[84:87],a[44:47],a[236:239]
global_load_lds_dwordx4 v11, s[50:51] offset:0
M a[240:243],a[84:87],a[48:51],a[240:243]
global_load_lds_dwordx4 v11, s[52:53] offset:1024
M a[244:247],a[84:87],a[52:55],a[244:247]
v_add_u32 v11, 229376, v11
M a[248:251],a[84:87],a[56:59],a[248:251]
M a[252:255],a[84:87],a[60:63],a[252:255]
s_waitcnt lgkmcnt(2)
M a[128:131],a[72:75],a[0:3],a[128:131]
T a[84:85],v32,1024
M a[132:135],a[72:75],a[4:7],a[132:135]
T a[86:87],v32,5120
M a[136:139],a[72:75],a[8:11],a[136:139]
M a[140:143],a[72:75],a[12:15],a[140:143]
M a[144:147],a[72:75],a[16:19],a[144:147]
M a[148:151],a[72:75],a[20:23],a[148:151]
M a[152:155],a[72:75],a[24:27],a[152:155]
M a[156:159],a[72:75],a[28:31],a[156:159]
s_waitcnt vmcnt(24) lgkmcnt(0)
s_barrier
M a[192:195],a[76:79],a[0:3],a[192:195]
T a[72:73],v9,8192
s_add_u32 m0, s54, 131072
M a[196:199],a[76:79],a[4:7],a[196:199]
T a[74:75],v9,12288
global_load_lds_dwordx4 v12, s[40:41] offset:0 sc1
M a[200:203],a[76:79],a[8:11],a[200:203]
X a[32:35],v13,16384
M a[204:207],a[76:79],a[12:15],a[204:207]
X a[36:39],v13,17408
M a[208:211],a[76:79],a[16:19],a[208:211]
X a[40:43],v13,18432
M a[212:215],a[76:79],a[20:23],a[212:215]
global_load_lds_dwordx4 v12, s[40:41] offset:1024 sc1
M a[216:219],a[76:79],a[24:27],a[216:219]
X a[44:47],v13,19456
M a[220:223],a[76:79],a[28:31],a[220:223]
global_load_lds_dwordx4 v12, s[40:41] offset:2048 sc1
M a[160:163],a[80:83],a[0:3],a[160:163]
T a[76:77],v9,9216
M a[164:167],a[80:83],a[4:7],a[164:167]
T a[78:79],v9,13312
M a[168:171],a[80:83],a[8:11],a[168:171]
X a[48:51],v13,20480
M a[172:175],a[80:83],a[12:15],a[172:175]
X a[52:55],v13,21504
M a[176:179],a[80:83],a[16:19],a[176:179]
X a[56:59],v13,22528
M a[180:183],a[80:83],a[20:23],a[180:183]
global_load_lds_dwordx4 v12, s[40:41] offset:3072 sc1
M a[184:187],a[80:83],a[24:27],a[184:187]
X a[60:63],v13,23552
M a[188:191],a[80:83],a[28:31],a[188:191]
v_add_u32 v12, 0x4000, v12
M a[224:227],a[84:87],a[0:3],a[224:227]
T a[80:81],v32,8192
M a[228:231],a[84:87],a[4:7],a[228:231]
T a[82:83],v32,12288
M a[232:235],a[84:87],a[8:11],a[232:235]
s_add_u32 m0, s55, 0
M a[236:239],a[84:87],a[12:15],a[236:239]
global_load_lds_dwordx4 v11, s[50:51] offset:0
M a[240:243],a[84:87],a[16:19],a[240:243]
global_load_lds_dwordx4 v11, s[52:53] offset:1024
M a[244:247],a[84:87],a[20:23],a[244:247]
v_add_u32 v11, 229376, v11
M a[248:251],a[84:87],a[24:27],a[248:251]
M a[252:255],a[84:87],a[28:31],a[252:255]
s_waitcnt lgkmcnt(2)
M a[128:131],a[72:75],a[32:35],a[128:131]
T a[84:85],v32,9216
M a[132:135],a[72:75],a[36:39],a[132:135]
T a[86:87],v32,13312
M a[136:139],a[72:75],a[40:43],a[136:139]
M a[140:143],a[72:75],a[44:47],a[140:143]
M a[144:147],a[72:75],a[48:51],a[144:147]
M a[148:151],a[72:75],a[52:55],a[148:151]
M a[152:155],a[72:75],a[56:59],a[152:155]
M a[156:159],a[72:75],a[60:63],a[156:159]
s_waitcnt vmcnt(24) lgkmcnt(0)
s_barrier
s_cmp_lg_u32 s99, 0
s_cbranch_scc1 Lnf1M_%=
s_mov_b64 exec, 1
v_mov_b32 v37, s98
global_store_dword v36, v37, s[96:97] offset:0 sc1
s_mov_b64 exec, -1
Lnf1M_%=:
s_nop 0
M a[192:195],a[76:79],a[32:35],a[192:195]
T a[72:73],v9,16384
s_add_u32 m0, s54, 147456
M a[196:199],a[76:79],a[36:39],a[196:199]
T a[74:75],v9,20480
global_load_lds_dwordx4 v12, s[40:41] offset:0 sc1
M a[200:203],a[76:79],a[40:43],a[200:203]
X a[0:3],v8,0
M a[204:207],a[76:79],a[44:47],a[204:207]
X a[4:7],v8,1024
M a[208:211],a[76:79],a[48:51],a[208:211]
X a[8:11],v8,2048
M a[212:215],a[76:79],a[52:55],a[212:215]
global_load_lds_dwordx4 v12, s[40:41] offset:1024 sc1
M a[216:219],a[76:79],a[56:59],a[216:219]
X a[12:15],v8,3072
M a[220:223],a[76:79],a[60:63],a[220:223]
global_load_lds_dwordx4 v12, s[40:41] offset:2048 sc1
M a[160:163],a[80:83],a[32:35],a[160:163]
T a[76:77],v9,17408
M a[164:167],a[80:83],a[36:39],a[164:167]
T a[78:79],v9,21504
M a[168:171],a[80:83],a[40:43],a[168:171]
X a[16:19],v8,4096
M a[172:175],a[80:83],a[44:47],a[172:175]
X a[20:23],v8,5120
M a[176:179],a[80:83],a[48:51],a[176:179]
X a[24:27],v8,6144
M a[180:183],a[80:83],a[52:55],a[180:183]
global_load_lds_dwordx4 v12, s[40:41] offset:3072 sc1
M a[184:187],a[80:83],a[56:59],a[184:187]
X a[28:31],v8,7168
M a[188:191],a[80:83],a[60:63],a[188:191]
v_add_u32 v12, 0x4000, v12
M a[224:227],a[84:87],a[32:35],a[224:227]
T a[80:81],v32,16384
M a[228:231],a[84:87],a[36:39],a[228:231]
T a[82:83],v32,20480
M a[232:235],a[84:87],a[40:43],a[232:235]
s_add_u32 m0, s55, 8192
M a[236:239],a[84:87],a[44:47],a[236:239]
global_load_lds_dwordx4 v11, s[50:51] offset:0
M a[240:243],a[84:87],a[48:51],a[240:243]
global_load_lds_dwordx4 v11, s[52:53] offset:1024
M a[244:247],a[84:87],a[52:55],a[244:247]
v_add_u32 v11, 229376, v11
M a[248:251],a[84:87],a[56:59],a[248:251]
M a[252:255],a[84:87],a[60:63],a[252:255]
s_waitcnt lgkmcnt(2)
M a[128:131],a[72:75],a[0:3],a[128:131]
T a[84:85],v32,17408
M a[132:135],a[72:75],a[4:7],a[132:135]
T a[86:87],v32,21504
M a[136:139],a[72:75],a[8:11],a[136:139]
M a[140:143],a[72:75],a[12:15],a[140:143]
M a[144:147],a[72:75],a[16:19],a[144:147]
M a[148:151],a[72:75],a[20:23],a[148:151]
M a[152:155],a[72:75],a[24:27],a[152:155]
M a[156:159],a[72:75],a[28:31],a[156:159]
s_waitcnt vmcnt(24) lgkmcnt(0)
s_barrier
M a[192:195],a[76:79],a[0:3],a[192:195]
T a[72:73],v9,24576
s_add_u32 m0, s54, 0
M a[196:199],a[76:79],a[4:7],a[196:199]
T a[74:75],v9,28672
global_load_lds_dwordx4 v12, s[40:41] offset:0 sc1
M a[200:203],a[76:79],a[8:11],a[200:203]
X a[32:35],v8,16384
M a[204:207],a[76:79],a[12:15],a[204:207]
X a[36:39],v8,17408
M a[208:211],a[76:79],a[16:19],a[208:211]
X a[40:43],v8,18432
M a[212:215],a[76:79],a[20:23],a[212:215]
global_load_lds_dwordx4 v12, s[40:41] offset:1024 sc1
M a[216:219],a[76:79],a[24:27],a[216:219]
X a[44:47],v8,19456
M a[220:223],a[76:79],a[28:31],a[220:223]
global_load_lds_dwordx4 v12, s[40:41] offset:2048 sc1
M a[160:163],a[80:83],a[0:3],a[160:163]
T a[76:77],v9,25600
M a[164:167],a[80:83],a[4:7],a[164:167]
T a[78:79],v9,29696
M a[168:171],a[80:83],a[8:11],a[168:171]
X a[48:51],v8,20480
M a[172:175],a[80:83],a[12:15],a[172:175]
X a[52:55],v8,21504
M a[176:179],a[80:83],a[16:19],a[176:179]
X a[56:59],v8,22528
M a[180:183],a[80:83],a[20:23],a[180:183]
global_load_lds_dwordx4 v12, s[40:41] offset:3072 sc1
M a[184:187],a[80:83],a[24:27],a[184:187]
X a[60:63],v8,23552
M a[188:191],a[80:83],a[28:31],a[188:191]
v_add_u32 v12, 0x4000, v12
M a[224:227],a[84:87],a[0:3],a[224:227]
T a[80:81],v32,24576
M a[228:231],a[84:87],a[4:7],a[228:231]
T a[82:83],v32,28672
M a[232:235],a[84:87],a[8:11],a[232:235]
s_add_u32 m0, s55, 16384
M a[236:239],a[84:87],a[12:15],a[236:239]
global_load_lds_dwordx4 v11, s[50:51] offset:0
M a[240:243],a[84:87],a[16:19],a[240:243]
global_load_lds_dwordx4 v11, s[52:53] offset:1024
M a[244:247],a[84:87],a[20:23],a[244:247]
v_add_u32 v11, 229376, v11
M a[248:251],a[84:87],a[24:27],a[248:251]
M a[252:255],a[84:87],a[28:31],a[252:255]
s_waitcnt lgkmcnt(2)
M a[128:131],a[72:75],a[32:35],a[128:131]
T a[84:85],v32,25600
M a[132:135],a[72:75],a[36:39],a[132:135]
T a[86:87],v32,29696
M a[136:139],a[72:75],a[40:43],a[136:139]
M a[140:143],a[72:75],a[44:47],a[140:143]
M a[144:147],a[72:75],a[48:51],a[144:147]
M a[148:151],a[72:75],a[52:55],a[148:151]
M a[152:155],a[72:75],a[56:59],a[152:155]
M a[156:159],a[72:75],a[60:63],a[156:159]
s_waitcnt vmcnt(24) lgkmcnt(0)
s_barrier
M a[192:195],a[76:79],a[32:35],a[192:195]
T a[72:73],v9,32768
s_add_u32 m0, s54, 16384
M a[196:199],a[76:79],a[36:39],a[196:199]
T a[74:75],v9,36864
global_load_lds_dwordx4 v12, s[40:41] offset:0 sc1
M a[200:203],a[76:79],a[40:43],a[200:203]
X a[0:3],v8,32768
M a[204:207],a[76:79],a[44:47],a[204:207]
X a[4:7],v8,33792
M a[208:211],a[76:79],a[48:51],a[208:211]
X a[8:11],v8,34816
M a[212:215],a[76:79],a[52:55],a[212:215]
global_load_lds_dwordx4 v12, s[40:41] offset:1024 sc1
M a[216:219],a[76:79],a[56:59],a[216:219]
X a[12:15],v8,35840
M a[220:223],a[76:79],a[60:63],a[220:223]
global_load_lds_dwordx4 v12, s[40:41] offset:2048 sc1
M a[160:163],a[80:83],a[32:35],a[160:163]
T a[76:77],v9,33792
M a[164:167],a[80:83],a[36:39],a[164:167]
T a[78:79],v9,37888
M a[168:171],a[80:83],a[40:43],a[168:171]
X a[16:19],v8,36864
M a[172:175],a[80:83],a[44:47],a[172:175]
X a[20:23],v8,37888
M a[176:179],a[80:83],a[48:51],a[176:179]
X a[24:27],v8,38912
M a[180:183],a[80:83],a[52:55],a[180:183]
global_load_lds_dwordx4 v12, s[40:41] offset:3072 sc1
M a[184:187],a[80:83],a[56:59],a[184:187]
X a[28:31],v8,39936
M a[188:191],a[80:83],a[60:63],a[188:191]
v_add_u32 v12, 0x4000, v12
M a[224:227],a[84:87],a[32:35],a[224:227]
T a[80:81],v32,32768
M a[228:231],a[84:87],a[36:39],a[228:231]
T a[82:83],v32,36864
M a[232:235],a[84:87],a[40:43],a[232:235]
s_add_u32 m0, s55, 24576
M a[236:239],a[84:87],a[44:47],a[236:239]
global_load_lds_dwordx4 v11, s[50:51] offset:0
M a[240:243],a[84:87],a[48:51],a[240:243]
global_load_lds_dwordx4 v11, s[52:53] offset:1024
M a[244:247],a[84:87],a[52:55],a[244:247]
v_add_u32 v11, 229376, v11
M a[248:251],a[84:87],a[56:59],a[248:251]
M a[252:255],a[84:87],a[60:63],a[252:255]
s_waitcnt lgkmcnt(2)
M a[128:131],a[72:75],a[0:3],a[128:131]
T a[84:85],v32,33792
M a[132:135],a[72:75],a[4:7],a[132:135]
T a[86:87],v32,37888
M a[136:139],a[72:75],a[8:11],a[136:139]
M a[140:143],a[72:75],a[12:15],a[140:143]
M a[144:147],a[72:75],a[16:19],a[144:147]
M a[148:151],a[72:75],a[20:23],a[148:151]
M a[152:155],a[72:75],a[24:27],a[152:155]
M a[156:159],a[72:75],a[28:31],a[156:159]
s_waitcnt vmcnt(24) lgkmcnt(0)
s_barrier
M a[192:195],a[76:79],a[0:3],a[192:195]
T a[72:73],v9,40960
s_add_u32 m0, s54, 32768
M a[196:199],a[76:79],a[4:7],a[196:199]
T a[74:75],v9,45056
global_load_lds_dwordx4 v12, s[40:41] offset:0 sc1
M a[200:203],a[76:79],a[8:11],a[200:203]
X a[32:35],v8,49152
M a[204:207],a[76:79],a[12:15],a[204:207]
X a[36:39],v8,50176
M a[208:211],a[76:79],a[16:19],a[208:211]
X a[40:43],v8,51200
M a[212:215],a[76:79],a[20:23],a[212:215]
global_load_lds_dwordx4 v12, s[40:41] offset:1024 sc1
M a[216:219],a[76:79],a[24:27],a[216:219]
X a[44:47],v8,52224
M a[220:223],a[76:79],a[28:31],a[220:223]
global_load_lds_dwordx4 v12, s[40:41] offset:2048 sc1
M a[160:163],a[80:83],a[0:3],a[160:163]
T a[76:77],v9,41984
M a[164:167],a[80:83],a[4:7],a[164:167]
T a[78:79],v9,46080
M a[168:171],a[80:83],a[8:11],a[168:171]
X a[48:51],v8,53248
M a[172:175],a[80:83],a[12:15],a[172:175]
X a[52:55],v8,54272
M a[176:179],a[80:83],a[16:19],a[176:179]
X a[56:59],v8,55296
M a[180:183],a[80:83],a[20:23],a[180:183]
global_load_lds_dwordx4 v12, s[40:41] offset:3072 sc1
M a[184:187],a[80:83],a[24:27],a[184:187]
X a[60:63],v8,56320
M a[188:191],a[80:83],a[28:31],a[188:191]
v_add_u32 v12, 0x4000, v12
M a[224:227],a[84:87],a[0:3],a[224:227]
T a[80:81],v32,40960
M a[228:231],a[84:87],a[4:7],a[228:231]
T a[82:83],v32,45056
M a[232:235],a[84:87],a[8:11],a[232:235]
s_add_u32 m0, s55, 32768
M a[236:239],a[84:87],a[12:15],a[236:239]
global_load_lds_dwordx4 v11, s[50:51] offset:0
M a[240:243],a[84:87],a[16:19],a[240:243]
global_load_lds_dwordx4 v11, s[52:53] offset:1024
M a[244:247],a[84:87],a[20:23],a[244:247]
v_add_u32 v11, 229376, v11
M a[248:251],a[84:87],a[24:27],a[248:251]
M a[252:255],a[84:87],a[28:31],a[252:255]
s_waitcnt lgkmcnt(2)
M a[128:131],a[72:75],a[32:35],a[128:131]
T a[84:85],v32,41984
M a[132:135],a[72:75],a[36:39],a[132:135]
T a[86:87],v32,46080
M a[136:139],a[72:75],a[40:43],a[136:139]
M a[140:143],a[72:75],a[44:47],a[140:143]
M a[144:147],a[72:75],a[48:51],a[144:147]
M a[148:151],a[72:75],a[52:55],a[148:151]
M a[152:155],a[72:75],a[56:59],a[152:155]
M a[156:159],a[72:75],a[60:63],a[156:159]
s_waitcnt vmcnt(24) lgkmcnt(0)
s_barrier
M a[192:195],a[76:79],a[32:35],a[192:195]
T a[72:73],v9,0
s_add_u32 m0, s54, 49152
M a[196:199],a[76:79],a[36:39],a[196:199]
T a[74:75],v9,4096
global_load_lds_dwordx4 v12, s[40:41] offset:0 sc1
M a[200:203],a[76:79],a[40:43],a[200:203]
X a[0:3],v13,0
M a[204:207],a[76:79],a[44:47],a[204:207]
X a[4:7],v13,1024
M a[208:211],a[76:79],a[48:51],a[208:211]
X a[8:11],v13,2048
M a[212:215],a[76:79],a[52:55],a[212:215]
global_load_lds_dwordx4 v12, s[40:41] offset:1024 sc1
M a[216:219],a[76:79],a[56:59],a[216:219]
X a[12:15],v13,3072
M a[220:223],a[76:79],a[60:63],a[220:223]
global_load_lds_dwordx4 v12, s[40:41] offset:2048 sc1
M a[160:163],a[80:83],a[32:35],a[160:163]
T a[76:77],v9,1024
M a[164:167],a[80:83],a[36:39],a[164:167]
T a[78:79],v9,5120
M a[168:171],a[80:83],a[40:43],a[168:171]
X a[16:19],v13,4096
M a[172:175],a[80:83],a[44:47],a[172:175]
X a[20:23],v13,5120
M a[176:179],a[80:83],a[48:51],a[176:179]
X a[24:27],v13,6144
M a[180:183],a[80:83],a[52:55],a[180:183]
global_load_lds_dwordx4 v12, s[40:41] offset:3072 sc1
M a[184:187],a[80:83],a[56:59],a[184:187]
X a[28:31],v13,7168
M a[188:191],a[80:83],a[60:63],a[188:191]
v_add_u32 v12, 0x4000, v12
M a[224:227],a[84:87],a[32:35],a[224:227]
T a[80:81],v32,0
M a[228:231],a[84:87],a[36:39],a[228:231]
T a[82:83],v32,4096
M a[232:235],a[84:87],a[40:43],a[232:235]
s_add_u32 m0, s55, 40960
M a[236:239],a[84:87],a[44:47],a[236:239]
global_load_lds_dwordx4 v11, s[50:51] offset:0
M a[240:243],a[84:87],a[48:51],a[240:243]
global_load_lds_dwordx4 v11, s[52:53] offset:1024
M a[244:247],a[84:87],a[52:55],a[244:247]
v_add_u32 v11, 229376, v11
M a[248:251],a[84:87],a[56:59],a[248:251]
M a[252:255],a[84:87],a[60:63],a[252:255]
s_waitcnt lgkmcnt(2)
M a[128:131],a[72:75],a[0:3],a[128:131]
T a[84:85],v32,1024
M a[132:135],a[72:75],a[4:7],a[132:135]
T a[86:87],v32,5120
M a[136:139],a[72:75],a[8:11],a[136:139]
M a[140:143],a[72:75],a[12:15],a[140:143]
M a[144:147],a[72:75],a[16:19],a[144:147]
M a[148:151],a[72:75],a[20:23],a[148:151]
M a[152:155],a[72:75],a[24:27],a[152:155]
M a[156:159],a[72:75],a[28:31],a[156:159]
s_waitcnt vmcnt(24) lgkmcnt(0)
s_barrier
M a[192:195],a[76:79],a[0:3],a[192:195]
T a[72:73],v9,8192
s_add_u32 m0, s54, 131072
M a[196:199],a[76:79],a[4:7],a[196:199]
T a[74:75],v9,12288
global_load_lds_dwordx4 v12, s[40:41] offset:0 sc1
M a[200:203],a[76:79],a[8:11],a[200:203]
X a[32:35],v13,16384
M a[204:207],a[76:79],a[12:15],a[204:207]
X a[36:39],v13,17408
M a[208:211],a[76:79],a[16:19],a[208:211]
X a[40:43],v13,18432
M a[212:215],a[76:79],a[20:23],a[212:215]
global_load_lds_dwordx4 v12, s[40:41] offset:1024 sc1
M a[216:219],a[76:79],a[24:27],a[216:219]
X a[44:47],v13,19456
M a[220:223],a[76:79],a[28:31],a[220:223]
global_load_lds_dwordx4 v12, s[40:41] offset:2048 sc1
M a[160:163],a[80:83],a[0:3],a[160:163]
T a[76:77],v9,9216
M a[164:167],a[80:83],a[4:7],a[164:167]
T a[78:79],v9,13312
M a[168:171],a[80:83],a[8:11],a[168:171]
X a[48:51],v13,20480
M a[172:175],a[80:83],a[12:15],a[172:175]
X a[52:55],v13,21504
M a[176:179],a[80:83],a[16:19],a[176:179]
X a[56:59],v13,22528
M a[180:183],a[80:83],a[20:23],a[180:183]
global_load_lds_dwordx4 v12, s[40:41] offset:3072 sc1
M a[184:187],a[80:83],a[24:27],a[184:187]
X a[60:63],v13,23552
M a[188:191],a[80:83],a[28:31],a[188:191]
v_add_u32 v12, 0x4000, v12
M a[224:227],a[84:87],a[0:3],a[224:227]
T a[80:81],v32,8192
M a[228:231],a[84:87],a[4:7],a[228:231]
T a[82:83],v32,12288
M a[232:235],a[84:87],a[8:11],a[232:235]
s_add_u32 m0, s55, 0
M a[236:239],a[84:87],a[12:15],a[236:239]
global_load_lds_dwordx4 v11, s[50:51] offset:0
M a[240:243],a[84:87],a[16:19],a[240:243]
global_load_lds_dwordx4 v11, s[52:53] offset:1024
M a[244:247],a[84:87],a[20:23],a[244:247]
v_add_u32 v11, 229376, v11
M a[248:251],a[84:87],a[24:27],a[248:251]
M a[252:255],a[84:87],a[28:31],a[252:255]
s_waitcnt lgkmcnt(2)
M a[128:131],a[72:75],a[32:35],a[128:131]
T a[84:85],v32,9216
M a[132:135],a[72:75],a[36:39],a[132:135]
T a[86:87],v32,13312
M a[136:139],a[72:75],a[40:43],a[136:139]
M a[140:143],a[72:75],a[44:47],a[140:143]
M a[144:147],a[72:75],a[48:51],a[144:147]
M a[148:151],a[72:75],a[52:55],a[148:151]
M a[152:155],a[72:75],a[56:59],a[152:155]
M a[156:159],a[72:75],a[60:63],a[156:159]
s_waitcnt vmcnt(24) lgkmcnt(0)
s_barrier
M a[192:195],a[76:79],a[32:35],a[192:195]
T a[72:73],v9,16384
s_add_u32 m0, s54, 147456
M a[196:199],a[76:79],a[36:39],a[196:199]
T a[74:75],v9,20480
global_load_lds_dwordx4 v12, s[40:41] offset:0 sc1
M a[200:203],a[76:79],a[40:43],a[200:203]
X a[0:3],v8,0
M a[204:207],a[76:79],a[44:47],a[204:207]
X a[4:7],v8,1024
M a[208:211],a[76:79],a[48:51],a[208:211]
X a[8:11],v8,2048
M a[212:215],a[76:79],a[52:55],a[212:215]
global_load_lds_dwordx4 v12, s[40:41] offset:1024 sc1
M a[216:219],a[76:79],a[56:59],a[216:219]
X a[12:15],v8,3072
M a[220:223],a[76:79],a[60:63],a[220:223]
global_load_lds_dwordx4 v12, s[40:41] offset:2048 sc1
M a[160:163],a[80:83],a[32:35],a[160:163]
T a[76:77],v9,17408
M a[164:167],a[80:83],a[36:39],a[164:167]
T a[78:79],v9,21504
M a[168:171],a[80:83],a[40:43],a[168:171]
X a[16:19],v8,4096
M a[172:175],a[80:83],a[44:47],a[172:175]
X a[20:23],v8,5120
M a[176:179],a[80:83],a[48:51],a[176:179]
X a[24:27],v8,6144
M a[180:183],a[80:83],a[52:55],a[180:183]
global_load_lds_dwordx4 v12, s[40:41] offset:3072 sc1
M a[184:187],a[80:83],a[56:59],a[184:187]
X a[28:31],v8,7168
M a[188:191],a[80:83],a[60:63],a[188:191]
v_add_u32 v12, 0x4000, v12
M a[224:227],a[84:87],a[32:35],a[224:227]
T a[80:81],v32,16384
M a[228:231],a[84:87],a[36:39],a[228:231]
T a[82:83],v32,20480
M a[232:235],a[84:87],a[40:43],a[232:235]
s_add_u32 m0, s55, 8192
M a[236:239],a[84:87],a[44:47],a[236:239]
global_load_lds_dwordx4 v11, s[50:51] offset:0
M a[240:243],a[84:87],a[48:51],a[240:243]
global_load_lds_dwordx4 v11, s[52:53] offset:1024
M a[244:247],a[84:87],a[52:55],a[244:247]
v_add_u32 v11, 229376, v11
M a[248:251],a[84:87],a[56:59],a[248:251]
M a[252:255],a[84:87],a[60:63],a[252:255]
s_waitcnt lgkmcnt(2)
M a[128:131],a[72:75],a[0:3],a[128:131]
T a[84:85],v32,17408
M a[132:135],a[72:75],a[4:7],a[132:135]
T a[86:87],v32,21504
M a[136:139],a[72:75],a[8:11],a[136:139]
M a[140:143],a[72:75],a[12:15],a[140:143]
M a[144:147],a[72:75],a[16:19],a[144:147]
M a[148:151],a[72:75],a[20:23],a[148:151]
M a[152:155],a[72:75],a[24:27],a[152:155]
M a[156:159],a[72:75],a[28:31],a[156:159]
s_waitcnt vmcnt(24) lgkmcnt(0)
s_barrier
M a[192:195],a[76:79],a[0:3],a[192:195]
T a[72:73],v9,24576
s_add_u32 m0, s54, 0
M a[196:199],a[76:79],a[4:7],a[196:199]
T a[74:75],v9,28672
global_load_lds_dwordx4 v12, s[40:41] offset:0 sc1
M a[200:203],a[76:79],a[8:11],a[200:203]
X a[32:35],v8,16384
M a[204:207],a[76:79],a[12:15],a[204:207]
X a[36:39],v8,17408
M a[208:211],a[76:79],a[16:19],a[208:211]
X a[40:43],v8,18432
M a[212:215],a[76:79],a[20:23],a[212:215]
global_load_lds_dwordx4 v12, s[40:41] offset:1024 sc1
M a[216:219],a[76:79],a[24:27],a[216:219]
X a[44:47],v8,19456
M a[220:223],a[76:79],a[28:31],a[220:223]
global_load_lds_dwordx4 v12, s[40:41] offset:2048 sc1
M a[160:163],a[80:83],a[0:3],a[160:163]
T a[76:77],v9,25600
M a[164:167],a[80:83],a[4:7],a[164:167]
T a[78:79],v9,29696
M a[168:171],a[80:83],a[8:11],a[168:171]
X a[48:51],v8,20480
M a[172:175],a[80:83],a[12:15],a[172:175]
X a[52:55],v8,21504
M a[176:179],a[80:83],a[16:19],a[176:179]
X a[56:59],v8,22528
M a[180:183],a[80:83],a[20:23],a[180:183]
global_load_lds_dwordx4 v12, s[40:41] offset:3072 sc1
M a[184:187],a[80:83],a[24:27],a[184:187]
X a[60:63],v8,23552
M a[188:191],a[80:83],a[28:31],a[188:191]
v_add_u32 v12, 0x4000, v12
M a[224:227],a[84:87],a[0:3],a[224:227]
T a[80:81],v32,24576
M a[228:231],a[84:87],a[4:7],a[228:231]
T a[82:83],v32,28672
M a[232:235],a[84:87],a[8:11],a[232:235]
s_add_u32 m0, s55, 16384
M a[236:239],a[84:87],a[12:15],a[236:239]
global_load_lds_dwordx4 v11, s[50:51] offset:0
M a[240:243],a[84:87],a[16:19],a[240:243]
global_load_lds_dwordx4 v11, s[52:53] offset:1024
M a[244:247],a[84:87],a[20:23],a[244:247]
v_add_u32 v11, 229376, v11
M a[248:251],a[84:87],a[24:27],a[248:251]
M a[252:255],a[84:87],a[28:31],a[252:255]
s_waitcnt lgkmcnt(2)
M a[128:131],a[72:75],a[32:35],a[128:131]
T a[84:85],v32,25600
M a[132:135],a[72:75],a[36:39],a[132:135]
T a[86:87],v32,29696
M a[136:139],a[72:75],a[40:43],a[136:139]
M a[140:143],a[72:75],a[44:47],a[140:143]
M a[144:147],a[72:75],a[48:51],a[144:147]
M a[148:151],a[72:75],a[52:55],a[148:151]
M a[152:155],a[72:75],a[56:59],a[152:155]
M a[156:159],a[72:75],a[60:63],a[156:159]
s_waitcnt vmcnt(24) lgkmcnt(0)
s_barrier
M a[192:195],a[76:79],a[32:35],a[192:195]
T a[72:73],v9,32768
s_add_u32 m0, s54, 16384
M a[196:199],a[76:79],a[36:39],a[196:199]
T a[74:75],v9,36864
global_load_lds_dwordx4 v12, s[40:41] offset:0 sc1
M a[200:203],a[76:79],a[40:43],a[200:203]
X a[0:3],v8,32768
M a[204:207],a[76:79],a[44:47],a[204:207]
X a[4:7],v8,33792
M a[208:211],a[76:79],a[48:51],a[208:211]
X a[8:11],v8,34816
M a[212:215],a[76:79],a[52:55],a[212:215]
global_load_lds_dwordx4 v12, s[40:41] offset:1024 sc1
M a[216:219],a[76:79],a[56:59],a[216:219]
X a[12:15],v8,35840
M a[220:223],a[76:79],a[60:63],a[220:223]
global_load_lds_dwordx4 v12, s[40:41] offset:2048 sc1
M a[160:163],a[80:83],a[32:35],a[160:163]
T a[76:77],v9,33792
M a[164:167],a[80:83],a[36:39],a[164:167]
T a[78:79],v9,37888
M a[168:171],a[80:83],a[40:43],a[168:171]
X a[16:19],v8,36864
M a[172:175],a[80:83],a[44:47],a[172:175]
X a[20:23],v8,37888
M a[176:179],a[80:83],a[48:51],a[176:179]
X a[24:27],v8,38912
M a[180:183],a[80:83],a[52:55],a[180:183]
global_load_lds_dwordx4 v12, s[40:41] offset:3072 sc1
M a[184:187],a[80:83],a[56:59],a[184:187]
X a[28:31],v8,39936
M a[188:191],a[80:83],a[60:63],a[188:191]
v_add_u32 v12, 0x4000, v12
M a[224:227],a[84:87],a[32:35],a[224:227]
T a[80:81],v32,32768
M a[228:231],a[84:87],a[36:39],a[228:231]
T a[82:83],v32,36864
M a[232:235],a[84:87],a[40:43],a[232:235]
s_add_u32 m0, s55, 24576
M a[236:239],a[84:87],a[44:47],a[236:239]
global_load_lds_dwordx4 v11, s[50:51] offset:0
M a[240:243],a[84:87],a[48:51],a[240:243]
global_load_lds_dwordx4 v11, s[52:53] offset:1024
M a[244:247],a[84:87],a[52:55],a[244:247]
v_add_u32 v11, 229376, v11
M a[248:251],a[84:87],a[56:59],a[248:251]
M a[252:255],a[84:87],a[60:63],a[252:255]
s_waitcnt lgkmcnt(2)
M a[128:131],a[72:75],a[0:3],a[128:131]
T a[84:85],v32,33792
M a[132:135],a[72:75],a[4:7],a[132:135]
T a[86:87],v32,37888
M a[136:139],a[72:75],a[8:11],a[136:139]
M a[140:143],a[72:75],a[12:15],a[140:143]
M a[144:147],a[72:75],a[16:19],a[144:147]
M a[148:151],a[72:75],a[20:23],a[148:151]
M a[152:155],a[72:75],a[24:27],a[152:155]
M a[156:159],a[72:75],a[28:31],a[156:159]
s_waitcnt vmcnt(24) lgkmcnt(0)
s_barrier
M a[192:195],a[76:79],a[0:3],a[192:195]
T a[72:73],v9,40960
s_add_u32 m0, s54, 32768
M a[196:199],a[76:79],a[4:7],a[196:199]
T a[74:75],v9,45056
global_load_lds_dwordx4 v12, s[40:41] offset:0 sc1
M a[200:203],a[76:79],a[8:11],a[200:203]
X a[32:35],v8,49152
M a[204:207],a[76:79],a[12:15],a[204:207]
X a[36:39],v8,50176
M a[208:211],a[76:79],a[16:19],a[208:211]
X a[40:43],v8,51200
M a[212:215],a[76:79],a[20:23],a[212:215]
global_load_lds_dwordx4 v12, s[40:41] offset:1024 sc1
M a[216:219],a[76:79],a[24:27],a[216:219]
X a[44:47],v8,52224
M a[220:223],a[76:79],a[28:31],a[220:223]
global_load_lds_dwordx4 v12, s[40:41] offset:2048 sc1
M a[160:163],a[80:83],a[0:3],a[160:163]
T a[76:77],v9,41984
M a[164:167],a[80:83],a[4:7],a[164:167]
T a[78:79],v9,46080
M a[168:171],a[80:83],a[8:11],a[168:171]
X a[48:51],v8,53248
M a[172:175],a[80:83],a[12:15],a[172:175]
X a[52:55],v8,54272
M a[176:179],a[80:83],a[16:19],a[176:179]
X a[56:59],v8,55296
M a[180:183],a[80:83],a[20:23],a[180:183]
global_load_lds_dwordx4 v12, s[40:41] offset:3072 sc1
M a[184:187],a[80:83],a[24:27],a[184:187]
X a[60:63],v8,56320
M a[188:191],a[80:83],a[28:31],a[188:191]
v_add_u32 v12, 0x4000, v12
M a[224:227],a[84:87],a[0:3],a[224:227]
T a[80:81],v32,40960
M a[228:231],a[84:87],a[4:7],a[228:231]
T a[82:83],v32,45056
M a[232:235],a[84:87],a[8:11],a[232:235]
s_add_u32 m0, s55, 32768
M a[236:239],a[84:87],a[12:15],a[236:239]
global_load_lds_dwordx4 v11, s[50:51] offset:0
M a[240:243],a[84:87],a[16:19],a[240:243]
global_load_lds_dwordx4 v11, s[52:53] offset:1024
M a[244:247],a[84:87],a[20:23],a[244:247]
v_add_u32 v11, 229376, v11
M a[248:251],a[84:87],a[24:27],a[248:251]
M a[252:255],a[84:87],a[28:31],a[252:255]
s_waitcnt lgkmcnt(2)
M a[128:131],a[72:75],a[32:35],a[128:131]
T a[84:85],v32,41984
M a[132:135],a[72:75],a[36:39],a[132:135]
T a[86:87],v32,46080
M a[136:139],a[72:75],a[40:43],a[136:139]
M a[140:143],a[72:75],a[44:47],a[140:143]
M a[144:147],a[72:75],a[48:51],a[144:147]
M a[148:151],a[72:75],a[52:55],a[148:151]
M a[152:155],a[72:75],a[56:59],a[152:155]
M a[156:159],a[72:75],a[60:63],a[156:159]
s_waitcnt vmcnt(24) lgkmcnt(0)
s_barrier
M a[192:195],a[76:79],a[32:35],a[192:195]
T a[72:73],v9,0
s_add_u32 m0, s54, 49152
M a[196:199],a[76:79],a[36:39],a[196:199]
T a[74:75],v9,4096
global_load_lds_dwordx4 v12, s[40:41] offset:0 sc1
M a[200:203],a[76:79],a[40:43],a[200:203]
X a[0:3],v13,0
M a[204:207],a[76:79],a[44:47],a[204:207]
X a[4:7],v13,1024
M a[208:211],a[76:79],a[48:51],a[208:211]
X a[8:11],v13,2048
M a[212:215],a[76:79],a[52:55],a[212:215]
global_load_lds_dwordx4 v12, s[40:41] offset:1024 sc1
M a[216:219],a[76:79],a[56:59],a[216:219]
X a[12:15],v13,3072
M a[220:223],a[76:79],a[60:63],a[220:223]
global_load_lds_dwordx4 v12, s[40:41] offset:2048 sc1
M a[160:163],a[80:83],a[32:35],a[160:163]
T a[76:77],v9,1024
M a[164:167],a[80:83],a[36:39],a[164:167]
T a[78:79],v9,5120
M a[168:171],a[80:83],a[40:43],a[168:171]
X a[16:19],v13,4096
M a[172:175],a[80:83],a[44:47],a[172:175]
X a[20:23],v13,5120
M a[176:179],a[80:83],a[48:51],a[176:179]
X a[24:27],v13,6144
global_load_dword v35, v34, s[96:97] offset:0 sc1
M a[180:183],a[80:83],a[52:55],a[180:183]
global_load_lds_dwordx4 v12, s[40:41] offset:3072 sc1
M a[184:187],a[80:83],a[56:59],a[184:187]
X a[28:31],v13,7168
M a[188:191],a[80:83],a[60:63],a[188:191]
v_add_u32 v12, 0x4000, v12
M a[224:227],a[84:87],a[32:35],a[224:227]
T a[80:81],v32,0
M a[228:231],a[84:87],a[36:39],a[228:231]
T a[82:83],v32,4096
M a[232:235],a[84:87],a[40:43],a[232:235]
s_add_u32 m0, s55, 40960
M a[236:239],a[84:87],a[44:47],a[236:239]
global_load_lds_dwordx4 v11, s[50:51] offset:0
M a[240:243],a[84:87],a[48:51],a[240:243]
global_load_lds_dwordx4 v11, s[52:53] offset:1024
M a[244:247],a[84:87],a[52:55],a[244:247]
v_add_u32 v11, 229376, v11
M a[248:251],a[84:87],a[56:59],a[248:251]
M a[252:255],a[84:87],a[60:63],a[252:255]
s_waitcnt lgkmcnt(2)
M a[128:131],a[72:75],a[0:3],a[128:131]
T a[84:85],v32,1024
M a[132:135],a[72:75],a[4:7],a[132:135]
T a[86:87],v32,5120
M a[136:139],a[72:75],a[8:11],a[136:139]
M a[140:143],a[72:75],a[12:15],a[140:143]
M a[144:147],a[72:75],a[16:19],a[144:147]
M a[148:151],a[72:75],a[20:23],a[148:151]
M a[152:155],a[72:75],a[24:27],a[152:155]
M a[156:159],a[72:75],a[28:31],a[156:159]
s_waitcnt vmcnt(25) lgkmcnt(0)
s_barrier
M a[192:195],a[76:79],a[0:3],a[192:195]
T a[72:73],v9,8192
s_add_u32 m0, s54, 131072
M a[196:199],a[76:79],a[4:7],a[196:199]
T a[74:75],v9,12288
global_load_lds_dwordx4 v12, s[40:41] offset:0 sc1
M a[200:203],a[76:79],a[8:11],a[200:203]
X a[32:35],v13,16384
M a[204:207],a[76:79],a[12:15],a[204:207]
X a[36:39],v13,17408
M a[208:211],a[76:79],a[16:19],a[208:211]
X a[40:43],v13,18432
M a[212:215],a[76:79],a[20:23],a[212:215]
global_load_lds_dwordx4 v12, s[40:41] offset:1024 sc1
M a[216:219],a[76:79],a[24:27],a[216:219]
X a[44:47],v13,19456
M a[220:223],a[76:79],a[28:31],a[220:223]
global_load_lds_dwordx4 v12, s[40:41] offset:2048 sc1
M a[160:163],a[80:83],a[0:3],a[160:163]
T a[76:77],v9,9216
M a[164:167],a[80:83],a[4:7],a[164:167]
T a[78:79],v9,13312
M a[168:171],a[80:83],a[8:11],a[168:171]
X a[48:51],v13,20480
M a[172:175],a[80:83],a[12:15],a[172:175]
X a[52:55],v13,21504
M a[176:179],a[80:83],a[16:19],a[176:179]
X a[56:59],v13,22528
M a[180:183],a[80:83],a[20:23],a[180:183]
global_load_lds_dwordx4 v12, s[40:41] offset:3072 sc1
M a[184:187],a[80:83],a[24:27],a[184:187]
X a[60:63],v13,23552
M a[188:191],a[80:83],a[28:31],a[188:191]
v_add_u32 v12, 0x4000, v12
M a[224:227],a[84:87],a[0:3],a[224:227]
T a[80:81],v32,8192
M a[228:231],a[84:87],a[4:7],a[228:231]
T a[82:83],v32,12288
M a[232:235],a[84:87],a[8:11],a[232:235]
s_add_u32 m0, s55, 0
M a[236:239],a[84:87],a[12:15],a[236:239]
global_load_lds_dwordx4 v11, s[50:51] offset:0
M a[240:243],a[84:87],a[16:19],a[240:243]
global_load_lds_dwordx4 v11, s[52:53] offset:1024
M a[244:247],a[84:87],a[20:23],a[244:247]
v_add_u32 v11, 229376, v11
s_add_u32 m0, s99, 114688
M a[248:251],a[84:87],a[24:27],a[248:251]
global_load_lds_dwordx4 v18, s[82:83] offset:0
global_load_lds_dwordx4 v19, s[82:83] offset:1024
M a[252:255],a[84:87],a[28:31],a[252:255]
global_load_lds_dwordx4 v20, s[82:83] offset:2048
global_load_lds_dwordx4 v21, s[82:83] offset:3072
s_waitcnt lgkmcnt(2)
M a[128:131],a[72:75],a[32:35],a[128:131]
T a[84:85],v32,9216
M a[132:135],a[72:75],a[36:39],a[132:135]
T a[86:87],v32,13312
M a[136:139],a[72:75],a[40:43],a[136:139]
M a[140:143],a[72:75],a[44:47],a[140:143]
M a[144:147],a[72:75],a[48:51],a[144:147]
M a[148:151],a[72:75],a[52:55],a[148:151]
M a[152:155],a[72:75],a[56:59],a[152:155]
M a[156:159],a[72:75],a[60:63],a[156:159]
s_waitcnt vmcnt(29) lgkmcnt(0)
s_barrier
s_waitcnt vmcnt(13)
v_cmp_ne_u32 vcc, s98, v35
s_nop 3
s_and_b32 vcc_lo, vcc_lo, 0xffff
s_cbranch_scc0 Lok1M_%=
s_mov_b32 s65, 0x100000
Lsp1M_%=:
s_sleep 1
global_load_dword v35, v34, s[96:97] offset:0 sc1
s_waitcnt vmcnt(0)
v_cmp_ne_u32 vcc, s98, v35
s_nop 3
s_and_b32 vcc_lo, vcc_lo, 0xffff
s_cbranch_scc0 Lok1M_%=
s_sub_u32 s65, s65, 1
s_cmp_lg_u32 s65, 0
s_cbranch_scc1 Lsp1M_%=
Lok1M_%=:
M a[192:195],a[76:79],a[32:35],a[192:195]
T a[72:73],v9,16384
s_add_u32 m0, s54, 147456
M a[196:199],a[76:79],a[36:39],a[196:199]
T a[74:75],v9,20480
global_load_lds_dwordx4 v12, s[40:41] offset:0 sc1
M a[200:203],a[76:79],a[40:43],a[200:203]
X a[0:3],v8,0
M a[204:207],a[76:79],a[44:47],a[204:207]
X a[4:7],v8,1024
M a[208:211],a[76:79],a[48:51],a[208:211]
X a[8:11],v8,2048
global_load_dwordx4 v[128:131], v16, s[80:81] offset:0 sc1
M a[212:215],a[76:79],a[52:55],a[212:215]
global_load_lds_dwordx4 v12, s[40:41] offset:1024 sc1
M a[216:219],a[76:79],a[56:59],a[216:219]
X a[12:15],v8,3072
M a[220:223],a[76:79],a[60:63],a[220:223]
global_load_lds_dwordx4 v12, s[40:41] offset:2048 sc1
M a[160:163],a[80:83],a[32:35],a[160:163]
T a[76:77],v9,17408
global_load_dwordx4 v[132:135], v17, s[80:81] offset:0 sc1
M a[164:167],a[80:83],a[36:39],a[164:167]
T a[78:79],v9,21504
M a[168:171],a[80:83],a[40:43],a[168:171]
X a[16:19],v8,4096
M a[172:175],a[80:83],a[44:47],a[172:175]
X a[20:23],v8,5120
M a[176:179],a[80:83],a[48:51],a[176:179]
X a[24:27],v8,6144
global_load_dwordx4 v[136:139], v16, s[80:81] offset:1024 sc1
M a[180:183],a[80:83],a[52:55],a[180:183]
global_load_lds_dwordx4 v12, s[40:41] offset:3072 sc1
M a[184:187],a[80:83],a[56:59],a[184:187]
X a[28:31],v8,7168
M a[188:191],a[80:83],a[60:63],a[188:191]
v_add_u32 v12, 0x4000, v12
M a[224:227],a[84:87],a[32:35],a[224:227]
T a[80:81],v32,16384
global_load_dwordx4 v[140:143], v17, s[80:81] offset:1024 sc1
M a[228:231],a[84:87],a[36:39],a[228:231]
T a[82:83],v32,20480
M a[232:235],a[84:87],a[40:43],a[232:235]
s_add_u32 m0, s55, 8192
M a[236:239],a[84:87],a[44:47],a[236:239]
global_load_lds_dwordx4 v11, s[50:51] offset:0
M a[240:243],a[84:87],a[48:51],a[240:243]
global_load_lds_dwordx4 v11, s[52:53] offset:1024
M a[244:247],a[84:87],a[52:55],a[244:247]
v_add_u32 v11, 229376, v11
M a[248:251],a[84:87],a[56:59],a[248:251]
M a[252:255],a[84:87],a[60:63],a[252:255]
s_waitcnt lgkmcnt(2)
M a[128:131],a[72:75],a[0:3],a[128:131]
T a[84:85],v32,17408
M a[132:135],a[72:75],a[4:7],a[132:135]
T a[86:87],v32,21504
M a[136:139],a[72:75],a[8:11],a[136:139]
M a[140:143],a[72:75],a[12:15],a[140:143]
M a[144:147],a[72:75],a[16:19],a[144:147]
M a[148:151],a[72:75],a[20:23],a[148:151]
M a[152:155],a[72:75],a[24:27],a[152:155]
M a[156:159],a[72:75],a[28:31],a[156:159]
s_waitcnt vmcnt(33) lgkmcnt(0)
s_barrier
M a[192:195],a[76:79],a[0:3],a[192:195]
T a[72:73],v9,24576
M a[196:199],a[76:79],a[4:7],a[196:199]
T a[74:75],v9,28672
s_add_u32 s82, s82, 131072
s_addc_u32 s83, s83, 0
s_add_u32 m0, s99, 0
M a[200:203],a[76:79],a[8:11],a[200:203]
X a[32:35],v8,16384
s_add_u32 s80, s80, 2048
s_addc_u32 s81, s81, 0
M a[204:207],a[76:79],a[12:15],a[204:207]
X a[36:39],v8,17408
global_load_lds_dwordx4 v18, s[82:83] offset:0
M a[208:211],a[76:79],a[16:19],a[208:211]
X a[40:43],v8,18432
M a[212:215],a[76:79],a[20:23],a[212:215]
global_load_dwordx4 v[144:147], v16, s[80:81] offset:0 sc1
M a[216:219],a[76:79],a[24:27],a[216:219]
X a[44:47],v8,19456
M a[220:223],a[76:79],a[28:31],a[220:223]
global_load_lds_dwordx4 v19, s[82:83] offset:1024
M a[160:163],a[80:83],a[0:3],a[160:163]
T a[76:77],v9,25600
M a[164:167],a[80:83],a[4:7],a[164:167]
T a[78:79],v9,29696
global_load_dwordx4 v[148:151], v17, s[80:81] offset:0 sc1
M a[168:171],a[80:83],a[8:11],a[168:171]
X a[48:51],v8,20480
M a[172:175],a[80:83],a[12:15],a[172:175]
X a[52:55],v8,21504
global_load_lds_dwordx4 v20, s[82:83] offset:2048
M a[176:179],a[80:83],a[16:19],a[176:179]
X a[56:59],v8,22528
M a[180:183],a[80:83],a[20:23],a[180:183]
global_load_dwordx4 v[152:155], v16, s[80:81] offset:1024 sc1
M a[184:187],a[80:83],a[24:27],a[184:187]
X a[60:63],v8,23552
M a[188:191],a[80:83],a[28:31],a[188:191]
global_load_lds_dwordx4 v21, s[82:83] offset:3072
M a[224:227],a[84:87],a[0:3],a[224:227]
T a[80:81],v32,24576
M a[228:231],a[84:87],a[4:7],a[228:231]
T a[82:83],v32,28672
global_load_dwordx4 v[156:159], v17, s[80:81] offset:1024 sc1
M a[232:235],a[84:87],a[8:11],a[232:235]
M a[236:239],a[84:87],a[12:15],a[236:239]
M a[240:243],a[84:87],a[16:19],a[240:243]
M a[244:247],a[84:87],a[20:23],a[244:247]
M a[248:251],a[84:87],a[24:27],a[248:251]
M a[252:255],a[84:87],a[28:31],a[252:255]
s_waitcnt lgkmcnt(2)
M a[128:131],a[72:75],a[32:35],a[128:131]
T a[84:85],v32,25600
M a[132:135],a[72:75],a[36:39],a[132:135]
T a[86:87],v32,29696
M a[136:139],a[72:75],a[40:43],a[136:139]
M a[140:143],a[72:75],a[44:47],a[140:143]
M a[144:147],a[72:75],a[48:51],a[144:147]
M a[148:151],a[72:75],a[52:55],a[148:151]
M a[152:155],a[72:75],a[56:59],a[152:155]
M a[156:159],a[72:75],a[60:63],a[156:159]
s_waitcnt vmcnt(35) lgkmcnt(0)
s_barrier
M a[192:195],a[76:79],a[32:35],a[192:195]
T a[72:73],v9,32768
M a[196:199],a[76:79],a[36:39],a[196:199]
T a[74:75],v9,36864
s_add_u32 s82, s82, 131072
s_addc_u32 s83, s83, 0
s_add_u32 m0, s99, 16384
M a[200:203],a[76:79],a[40:43],a[200:203]
X a[0:3],v8,32768
s_add_u32 s80, s80, 2048
s_addc_u32 s81, s81, 0
M a[204:207],a[76:79],a[44:47],a[204:207]
X a[4:7],v8,33792
global_load_lds_dwordx4 v18, s[82:83] offset:0
M a[208:211],a[76:79],a[48:51],a[208:211]
X a[8:11],v8,34816
M a[212:215],a[76:79],a[52:55],a[212:215]
global_load_dwordx4 v[160:163], v16, s[80:81] offset:0 sc1
M a[216:219],a[76:79],a[56:59],a[216:219]
X a[12:15],v8,35840
M a[220:223],a[76:79],a[60:63],a[220:223]
global_load_lds_dwordx4 v19, s[82:83] offset:1024
M a[160:163],a[80:83],a[32:35],a[160:163]
T a[76:77],v9,33792
M a[164:167],a[80:83],a[36:39],a[164:167]
T a[78:79],v9,37888
global_load_dwordx4 v[164:167], v17, s[80:81] offset:0 sc1
M a[168:171],a[80:83],a[40:43],a[168:171]
X a[16:19],v8,36864
M a[172:175],a[80:83],a[44:47],a[172:175]
X a[20:23],v8,37888
global_load_lds_dwordx4 v20, s[82:83] offset:2048
M a[176:179],a[80:83],a[48:51],a[176:179]
X a[24:27],v8,38912
M a[180:183],a[80:83],a[52:55],a[180:183]
global_load_dwordx4 v[168:171], v16, s[80:81] offset:1024 sc1
M a[184:187],a[80:83],a[56:59],a[184:187]
X a[28:31],v8,39936
M a[188:191],a[80:83],a[60:63],a[188:191]
global_load_lds_dwordx4 v21, s[82:83] offset:3072
M a[224:227],a[84:87],a[32:35],a[224:227]
T a[80:81],v32,32768
M a[228:231],a[84:87],a[36:39],a[228:231]
T a[82:83],v32,36864
global_load_dwordx4 v[172:175], v17, s[80:81] offset:1024 sc1
M a[232:235],a[84:87],a[40:43],a[232:235]
M a[236:239],a[84:87],a[44:47],a[236:239]
M a[240:243],a[84:87],a[48:51],a[240:243]
M a[244:247],a[84:87],a[52:55],a[244:247]
M a[248:251],a[84:87],a[56:59],a[248:251]
M a[252:255],a[84:87],a[60:63],a[252:255]
s_waitcnt lgkmcnt(2)
M a[128:131],a[72:75],a[0:3],a[128:131]
T a[84:85],v32,33792
M a[132:135],a[72:75],a[4:7],a[132:135]
T a[86:87],v32,37888
M a[136:139],a[72:75],a[8:11],a[136:139]
M a[140:143],a[72:75],a[12:15],a[140:143]
M a[144:147],a[72:75],a[16:19],a[144:147]
M a[148:151],a[72:75],a[20:23],a[148:151]
M a[152:155],a[72:75],a[24:27],a[152:155]
M a[156:159],a[72:75],a[28:31],a[156:159]
s_waitcnt vmcnt(36) lgkmcnt(0)
s_barrier
M a[192:195],a[76:79],a[0:3],a[192:195]
T a[72:73],v9,40960
M a[196:199],a[76:79],a[4:7],a[196:199]
T a[74:75],v9,45056
s_add_u32 s82, s82, 131072
s_addc_u32 s83, s83, 0
s_add_u32 m0, s99, 81920
M a[200:203],a[76:79],a[8:11],a[200:203]
X a[32:35],v8,49152
s_add_u32 s80, s80, 2048
s_addc_u32 s81, s81, 0
M a[204:207],a[76:79],a[12:15],a[204:207]
X a[36:39],v8,50176
global_load_lds_dwordx4 v18, s[82:83] offset:0
M a[208:211],a[76:79],a[16:19],a[208:211]
X a[40:43],v8,51200
M a[212:215],a[76:79],a[20:23],a[212:215]
global_load_dwordx4 v[176:179], v16, s[80:81] offset:0 sc1
M a[216:219],a[76:79],a[24:27],a[216:219]
X a[44:47],v8,52224
M a[220:223],a[76:79],a[28:31],a[220:223]
global_load_lds_dwordx4 v19, s[82:83] offset:1024
M a[160:163],a[80:83],a[0:3],a[160:163]
T a[76:77],v9,41984
M a[164:167],a[80:83],a[4:7],a[164:167]
T a[78:79],v9,46080
global_load_dwordx4 v[180:183], v17, s[80:81] offset:0 sc1
M a[168:171],a[80:83],a[8:11],a[168:171]
X a[48:51],v8,53248
M a[172:175],a[80:83],a[12:15],a[172:175]
X a[52:55],v8,54272
global_load_lds_dwordx4 v20, s[82:83] offset:2048
M a[176:179],a[80:83],a[16:19],a[176:179]
X a[56:59],v8,55296
M a[180:183],a[80:83],a[20:23],a[180:183]
global_load_dwordx4 v[184:187], v16, s[80:81] offset:1024 sc1
M a[184:187],a[80:83],a[24:27],a[184:187]
X a[60:63],v8,56320
M a[188:191],a[80:83],a[28:31],a[188:191]
global_load_lds_dwordx4 v21, s[82:83] offset:3072
M a[224:227],a[84:87],a[0:3],a[224:227]
T a[80:81],v32,40960
M a[228:231],a[84:87],a[4:7],a[228:231]
T a[82:83],v32,45056
global_load_dwordx4 v[188:191], v17, s[80:81] offset:1024 sc1
M a[232:235],a[84:87],a[8:11],a[232:235]
M a[236:239],a[84:87],a[12:15],a[236:239]
M a[240:243],a[84:87],a[16:19],a[240:243]
M a[244:247],a[84:87],a[20:23],a[244:247]
M a[248:251],a[84:87],a[24:27],a[248:251]
M a[252:255],a[84:87],a[28:31],a[252:255]
s_waitcnt lgkmcnt(2)
M a[128:131],a[72:75],a[32:35],a[128:131]
T a[84:85],v32,41984
M a[132:135],a[72:75],a[36:39],a[132:135]
T a[86:87],v32,46080
M a[136:139],a[72:75],a[40:43],a[136:139]
M a[140:143],a[72:75],a[44:47],a[140:143]
M a[144:147],a[72:75],a[48:51],a[144:147]
M a[148:151],a[72:75],a[52:55],a[148:151]
M a[152:155],a[72:75],a[56:59],a[152:155]
M a[156:159],a[72:75],a[60:63],a[156:159]
s_waitcnt vmcnt(38) lgkmcnt(0)
s_barrier
M a[192:195],a[76:79],a[32:35],a[192:195]
T a[72:73],v9,0
M a[196:199],a[76:79],a[36:39],a[196:199]
T a[74:75],v9,4096
s_add_u32 s82, s82, 131072
s_addc_u32 s83, s83, 0
s_add_u32 m0, s99, 32768
M a[200:203],a[76:79],a[40:43],a[200:203]
X a[0:3],v13,0
s_add_u32 s80, s80, 2048
s_addc_u32 s81, s81, 0
M a[204:207],a[76:79],a[44:47],a[204:207]
X a[4:7],v13,1024
global_load_lds_dwordx4 v18, s[82:83] offset:0
M a[208:211],a[76:79],a[48:51],a[208:211]
X a[8:11],v13,2048
M a[212:215],a[76:79],a[52:55],a[212:215]
global_load_dwordx4 v[192:195], v16, s[80:81] offset:0 sc1
M a[216:219],a[76:79],a[56:59],a[216:219]
X a[12:15],v13,3072
M a[220:223],a[76:79],a[60:63],a[220:223]
global_load_lds_dwordx4 v19, s[82:83] offset:1024
M a[160:163],a[80:83],a[32:35],a[160:163]
T a[76:77],v9,1024
M a[164:167],a[80:83],a[36:39],a[164:167]
T a[78:79],v9,5120
global_load_dwordx4 v[196:199], v17, s[80:81] offset:0 sc1
M a[168:171],a[80:83],a[40:43],a[168:171]
X a[16:19],v13,4096
M a[172:175],a[80:83],a[44:47],a[172:175]
X a[20:23],v13,5120
global_load_lds_dwordx4 v20, s[82:83] offset:2048
M a[176:179],a[80:83],a[48:51],a[176:179]
X a[24:27],v13,6144
M a[180:183],a[80:83],a[52:55],a[180:183]
global_load_dwordx4 v[200:203], v16, s[80:81] offset:1024 sc1
M a[184:187],a[80:83],a[56:59],a[184:187]
X a[28:31],v13,7168
M a[188:191],a[80:83],a[60:63],a[188:191]
global_load_lds_dwordx4 v21, s[82:83] offset:3072
M a[224:227],a[84:87],a[32:35],a[224:227]
T a[80:81],v32,0
M a[228:231],a[84:87],a[36:39],a[228:231]
T a[82:83],v32,4096
global_load_dwordx4 v[204:207], v17, s[80:81] offset:1024 sc1
M a[232:235],a[84:87],a[40:43],a[232:235]
M a[236:239],a[84:87],a[44:47],a[236:239]
M a[240:243],a[84:87],a[48:51],a[240:243]
M a[244:247],a[84:87],a[52:55],a[244:247]
M a[248:251],a[84:87],a[56:59],a[248:251]
M a[252:255],a[84:87],a[60:63],a[252:255]
s_waitcnt lgkmcnt(2)
M a[128:131],a[72:75],a[0:3],a[128:131]
T a[84:85],v32,1024
M a[132:135],a[72:75],a[4:7],a[132:135]
T a[86:87],v32,5120
M a[136:139],a[72:75],a[8:11],a[136:139]
v_xor_b32 v24, 0, v22
v_xor_b32 v25, 32, v22
M a[140:143],a[72:75],a[12:15],a[140:143]
v_xor_b32 v26, 64, v22
v_xor_b32 v27, 96, v22
M a[144:147],a[72:75],a[16:19],a[144:147]
v_add_u32 v28, 0x10000, v24
v_add_u32 v29, 0x10000, v25
M a[148:151],a[72:75],a[20:23],a[148:151]
v_add_u32 v30, 0x10000, v26
v_add_u32 v31, 0x10000, v27
M a[152:155],a[72:75],a[24:27],a[152:155]
M a[156:159],a[72:75],a[28:31],a[156:159]
s_waitcnt vmcnt(32) lgkmcnt(0)
s_barrier
M a[192:195],a[76:79],a[0:3],a[192:195]
T a[72:73],v9,8192
M a[196:199],a[76:79],a[4:7],a[196:199]
T a[74:75],v9,12288
s_add_u32 s82, s82, 131072
s_addc_u32 s83, s83, 0
s_add_u32 m0, s99, 49152
M a[200:203],a[76:79],a[8:11],a[200:203]
X a[32:35],v13,16384
s_add_u32 s80, s80, 2048
s_addc_u32 s81, s81, 0
M a[204:207],a[76:79],a[12:15],a[204:207]
X a[36:39],v13,17408
global_load_lds_dwordx4 v18, s[82:83] offset:0
M a[208:211],a[76:79],a[16:19],a[208:211]
X a[40:43],v13,18432
M a[212:215],a[76:79],a[20:23],a[212:215]
global_load_dwordx4 v[208:211], v16, s[80:81] offset:0 sc1
M a[216:219],a[76:79],a[24:27],a[216:219]
X a[44:47],v13,19456
M a[220:223],a[76:79],a[28:31],a[220:223]
global_load_lds_dwordx4 v19, s[82:83] offset:1024
M a[160:163],a[80:83],a[0:3],a[160:163]
T a[76:77],v9,9216
M a[164:167],a[80:83],a[4:7],a[164:167]
T a[78:79],v9,13312
global_load_dwordx4 v[212:215], v17, s[80:81] offset:0 sc1
M a[168:171],a[80:83],a[8:11],a[168:171]
X a[48:51],v13,20480
M a[172:175],a[80:83],a[12:15],a[172:175]
X a[52:55],v13,21504
global_load_lds_dwordx4 v20, s[82:83] offset:2048
M a[176:179],a[80:83],a[16:19],a[176:179]
X a[56:59],v13,22528
global_load_dword v35, v34, s[96:97] offset:0 sc1
M a[180:183],a[80:83],a[20:23],a[180:183]
global_load_dwordx4 v[216:219], v16, s[80:81] offset:1024 sc1
M a[184:187],a[80:83],a[24:27],a[184:187]
X a[60:63],v13,23552
M a[188:191],a[80:83],a[28:31],a[188:191]
global_load_lds_dwordx4 v21, s[82:83] offset:3072
M a[224:227],a[84:87],a[0:3],a[224:227]
T a[80:81],v32,8192
M a[228:231],a[84:87],a[4:7],a[228:231]
T a[82:83],v32,12288
global_load_dwordx4 v[220:223], v17, s[80:81] offset:1024 sc1
M a[232:235],a[84:87],a[8:11],a[232:235]
M a[236:239],a[84:87],a[12:15],a[236:239]
M a[240:243],a[84:87],a[16:19],a[240:243]
M a[244:247],a[84:87],a[20:23],a[244:247]
M a[248:251],a[84:87],a[24:27],a[248:251]
M a[252:255],a[84:87],a[28:31],a[252:255]
s_waitcnt lgkmcnt(2)
M a[128:131],a[72:75],a[32:35],a[128:131]
T a[84:85],v32,9216
M a[132:135],a[72:75],a[36:39],a[132:135]
T a[86:87],v32,13312
M a[136:139],a[72:75],a[40:43],a[136:139]
M a[140:143],a[72:75],a[44:47],a[140:143]
M a[144:147],a[72:75],a[48:51],a[144:147]
M a[148:151],a[72:75],a[52:55],a[148:151]
M a[152:155],a[72:75],a[56:59],a[152:155]
M a[156:159],a[72:75],a[60:63],a[156:159]
s_waitcnt lgkmcnt(0)
s_barrier
M a[192:195],a[76:79],a[32:35],a[192:195]
T a[96:97],v28,49152
M a[196:199],a[76:79],a[36:39],a[196:199]
s_add_u32 s82, s82, 131072
s_addc_u32 s83, s83, 0
s_add_u32 m0, s99, 98304
T a[98:99],v28,57344
M a[200:203],a[76:79],a[40:43],a[200:203]
s_add_u32 s80, s80, 2048
s_addc_u32 s81, s81, 0
M a[204:207],a[76:79],a[44:47],a[204:207]
global_load_lds_dwordx4 v18, s[82:83] offset:0
T a[100:101],v29,49152
M a[208:211],a[76:79],a[48:51],a[208:211]
T a[102:103],v29,57344
M a[212:215],a[76:79],a[52:55],a[212:215]
global_load_dwordx4 v[224:227], v16, s[80:81] offset:0 sc1
M a[216:219],a[76:79],a[56:59],a[216:219]
T a[104:105],v30,49152
M a[220:223],a[76:79],a[60:63],a[220:223]
global_load_lds_dwordx4 v19, s[82:83] offset:1024
T a[106:107],v30,57344
M a[160:163],a[80:83],a[32:35],a[160:163]
M a[164:167],a[80:83],a[36:39],a[164:167]
global_load_dwordx4 v[228:231], v17, s[80:81] offset:0 sc1
T a[108:109],v31,49152
M a[168:171],a[80:83],a[40:43],a[168:171]
T a[110:111],v31,57344
M a[172:175],a[80:83],a[44:47],a[172:175]
M a[176:179],a[80:83],a[48:51],a[176:179]
T a[112:113],v28,50176
M a[180:183],a[80:83],a[52:55],a[180:183]
T a[114:115],v28,58368
M a[184:187],a[80:83],a[56:59],a[184:187]
M a[188:191],a[80:83],a[60:63],a[188:191]
T a[116:117],v29,50176
M a[224:227],a[84:87],a[32:35],a[224:227]
T a[118:119],v29,58368
M a[228:231],a[84:87],a[36:39],a[228:231]
M a[232:235],a[84:87],a[40:43],a[232:235]
T a[120:121],v30,50176
M a[236:239],a[84:87],a[44:47],a[236:239]
T a[122:123],v30,58368
M a[240:243],a[84:87],a[48:51],a[240:243]
M a[244:247],a[84:87],a[52:55],a[244:247]
T a[124:125],v31,50176
M a[248:251],a[84:87],a[56:59],a[248:251]
T a[126:127],v31,58368
M a[252:255],a[84:87],a[60:63],a[252:255]
s_waitcnt vmcnt(47)
s_waitcnt lgkmcnt(8)
M v[64:67],a[96:99],v[128:131],v[64:67]
s_add_u32 s70, s60, 147456
s_addc_u32 s71, s61, 0
v_accvgpr_read_b32 v48, a128
v_accvgpr_read_b32 v40, a132
v_accvgpr_read_b32 v49, a129
v_accvgpr_read_b32 v41, a133
M v[96:99],a[96:99],v[132:135],v[96:99]
T a[96:97],v28,51200
global_load_lds_dwordx4 v20, s[82:83] offset:2048
v_accvgpr_read_b32 v50, a130
v_accvgpr_read_b32 v42, a134
v_accvgpr_read_b32 v51, a131
v_accvgpr_read_b32 v43, a135
M v[68:71],a[100:103],v[128:131],v[68:71]
T a[98:99],v28,59392
s_mov_b32 s66, 0
s_mov_b32 s67, 0xffff
v_accvgpr_read_b32 v56, a192
v_accvgpr_read_b32 v8, a196
v_accvgpr_read_b32 v57, a193
M v[100:103],a[100:103],v[132:135],v[100:103]
T a[100:101],v29,51200
v_accvgpr_read_b32 v9, a197
v_accvgpr_read_b32 v58, a194
v_accvgpr_read_b32 v10, a198
v_accvgpr_read_b32 v59, a195
v_accvgpr_read_b32 v11, a199
M v[72:75],a[104:107],v[128:131],v[72:75]
T a[102:103],v29,59392
v_accvgpr_read_b32 v52, a160
v_accvgpr_read_b32 v44, a164
v_accvgpr_read_b32 v53, a161
v_accvgpr_read_b32 v45, a165
v_accvgpr_read_b32 v54, a162
M v[104:107],a[104:107],v[132:135],v[104:107]
T a[104:105],v30,51200
global_load_dwordx4 v[232:235], v16, s[80:81] offset:1024 sc1
v_accvgpr_read_b32 v46, a166
v_accvgpr_read_b32 v55, a163
v_accvgpr_read_b32 v47, a167
v_accvgpr_read_b32 v60, a224
M v[76:79],a[108:111],v[128:131],v[76:79]
T a[106:107],v30,59392
v_accvgpr_read_b32 v12, a228
v_accvgpr_read_b32 v61, a225
v_accvgpr_read_b32 v13, a229
v_accvgpr_read_b32 v62, a226
v_accvgpr_read_b32 v14, a230
M v[108:111],a[108:111],v[132:135],v[108:111]
T a[108:109],v31,51200
s_waitcnt lgkmcnt(7)
v_accvgpr_read_b32 v63, a227
v_accvgpr_read_b32 v15, a231
v_mul_f32 v48, v48, v56
v_mul_f32 v40, v40, v8
M v[80:83],a[112:115],v[128:131],v[80:83]
T a[110:111],v31,59392
v_mul_f32 v49, v49, v57
v_mul_f32 v41, v41, v9
v_mul_f32 v50, v50, v58
v_mul_f32 v42, v42, v10
v_mul_f32 v51, v51, v59
M v[112:115],a[112:115],v[132:135],v[112:115]
T a[112:113],v28,52224
global_load_lds_dwordx4 v21, s[82:83] offset:3072
v_mul_f32 v43, v43, v11
v_mul_f32 v52, v52, v60
v_mul_f32 v44, v44, v12
v_mul_f32 v53, v53, v61
M v[84:87],a[116:119],v[128:131],v[84:87]
T a[114:115],v28,60416
s_add_u32 s82, s82, 131072
s_addc_u32 s83, s83, 0
v_mul_f32 v45, v45, v13
v_mul_f32 v54, v54, v62
v_mul_f32 v46, v46, v14
M v[116:119],a[116:119],v[132:135],v[116:119]
T a[116:117],v29,52224
v_mul_f32 v55, v55, v63
v_mul_f32 v47, v47, v15
v_cvt_pk_bf16_f32 v48, v48, v49
v_cvt_pk_bf16_f32 v40, v40, v41
v_cvt_pk_bf16_f32 v49, v50, v51
M v[88:91],a[120:123],v[128:131],v[88:91]
T a[118:119],v29,60416
v_cvt_pk_bf16_f32 v41, v42, v43
v_cvt_pk_bf16_f32 v50, v52, v53
v_cvt_pk_bf16_f32 v42, v44, v45
v_cvt_pk_bf16_f32 v51, v54, v55
v_cvt_pk_bf16_f32 v43, v46, v47
M v[120:123],a[120:123],v[132:135],v[120:123]
T a[120:121],v30,52224
global_load_dwordx4 v[236:239], v17, s[80:81] offset:1024 sc1
s_add_u32 m0, s99, 65536
global_store_dwordx4 v16, v[48:51], s[60:61] sc1
global_store_dwordx4 v16, v[40:43], s[70:71] sc1
s_add_u32 s60, s60, 294912
s_addc_u32 s61, s61, 0
M v[92:95],a[124:127],v[128:131],v[92:95]
T a[122:123],v30,60416
s_add_u32 s80, s80, 2048
s_addc_u32 s81, s81, 0
s_add_u32 s70, s70, 294912
s_addc_u32 s71, s71, 0
v_accvgpr_read_b32 v48, a136
M v[124:127],a[124:127],v[132:135],v[124:127]
T a[124:125],v31,52224
s_waitcnt vmcnt(43) lgkmcnt(7)
s_barrier
v_accvgpr_read_b32 v40, a140
v_accvgpr_read_b32 v49, a137
v_accvgpr_read_b32 v41, a141
M v[64:67],a[96:99],v[136:139],v[64:67]
T a[126:127],v31,60416
v_accvgpr_read_b32 v50, a138
v_accvgpr_read_b32 v42, a142
v_accvgpr_read_b32 v51, a139
v_accvgpr_read_b32 v43, a143
v_accvgpr_read_b32 v56, a200
M v[96:99],a[96:99],v[140:143],v[96:99]
T a[96:97],v24,0
v_accvgpr_read_b32 v8, a204
v_accvgpr_read_b32 v57, a201
v_accvgpr_read_b32 v9, a205
v_accvgpr_read_b32 v58, a202
v_accvgpr_read_b32 v10, a206
M v[68:71],a[100:103],v[136:139],v[68:71]
T a[98:99],v24,8192
global_load_lds_dwordx4 v18, s[82:83] offset:0
v_accvgpr_read_b32 v59, a203
v_accvgpr_read_b32 v11, a207
v_accvgpr_read_b32 v52, a168
v_accvgpr_read_b32 v44, a172
M v[100:103],a[100:103],v[140:143],v[100:103]
T a[100:101],v25,0
v_accvgpr_read_b32 v53, a169
v_accvgpr_read_b32 v45, a173
v_accvgpr_read_b32 v54, a170
v_accvgpr_read_b32 v46, a174
v_accvgpr_read_b32 v55, a171
M v[72:75],a[104:107],v[136:139],v[72:75]
T a[102:103],v25,8192
v_accvgpr_read_b32 v47, a175
v_accvgpr_read_b32 v60, a232
v_accvgpr_read_b32 v12, a236
v_accvgpr_read_b32 v61, a233
v_accvgpr_read_b32 v13, a237
M v[104:107],a[104:107],v[140:143],v[104:107]
T a[104:105],v26,0
v_accvgpr_read_b32 v62, a234
v_accvgpr_read_b32 v14, a238
v_accvgpr_read_b32 v63, a235
v_accvgpr_read_b32 v15, a239
v_mul_f32 v48, v48, v56
M v[76:79],a[108:111],v[136:139],v[76:79]
T a[106:107],v26,8192
global_load_dwordx4 v[240:243], v16, s[80:81] offset:0 sc1
v_mul_f32 v40, v40, v8
v_mul_f32 v49, v49, v57
v_mul_f32 v41, v41, v9
v_mul_f32 v50, v50, v58
M v[108:111],a[108:111],v[140:143],v[108:111]
T a[108:109],v27,0
s_waitcnt lgkmcnt(7)
v_mul_f32 v42, v42, v10
v_mul_f32 v51, v51, v59
v_mul_f32 v43, v43, v11
v_mul_f32 v52, v52, v60
M v[80:83],a[112:115],v[136:139],v[80:83]
T a[110:111],v27,8192
v_mul_f32 v44, v44, v12
v_mul_f32 v53, v53, v61
v_mul_f32 v45, v45, v13
v_mul_f32 v54, v54, v62
v_mul_f32 v46, v46, v14
M v[112:115],a[112:115],v[140:143],v[112:115]
T a[112:113],v24,1024
v_mul_f32 v55, v55, v63
v_mul_f32 v47, v47, v15
v_cvt_pk_bf16_f32 v48, v48, v49
v_cvt_pk_bf16_f32 v40, v40, v41
v_cvt_pk_bf16_f32 v49, v50, v51
M v[84:87],a[116:119],v[136:139],v[84:87]
T a[114:115],v24,9216
global_load_lds_dwordx4 v19, s[82:83] offset:1024
v_cvt_pk_bf16_f32 v41, v42, v43
v_cvt_pk_bf16_f32 v50, v52, v53
v_cvt_pk_bf16_f32 v42, v44, v45
v_cvt_pk_bf16_f32 v51, v54, v55
M v[116:119],a[116:119],v[140:143],v[116:119]
T a[116:117],v25,1024
v_cvt_pk_bf16_f32 v43, v46, v47
global_store_dwordx4 v16, v[48:51], s[60:61] sc1
global_store_dwordx4 v16, v[40:43], s[70:71] sc1
s_add_u32 s60, s60, 294912
s_addc_u32 s61, s61, 0
M v[88:91],a[120:123],v[136:139],v[88:91]
T a[118:119],v25,9216
s_add_u32 s70, s70, 294912
s_addc_u32 s71, s71, 0
v_accvgpr_read_b32 v48, a144
v_accvgpr_read_b32 v40, a148
v_accvgpr_read_b32 v49, a145
M v[120:123],a[120:123],v[140:143],v[120:123]
T a[120:121],v26,1024
v_accvgpr_read_b32 v41, a149
v_accvgpr_read_b32 v50, a146
v_accvgpr_read_b32 v42, a150
v_accvgpr_read_b32 v51, a147
v_accvgpr_read_b32 v43, a151
M v[92:95],a[124:127],v[136:139],v[92:95]
T a[122:123],v26,9216
global_load_dwordx4 v[244:247], v17, s[80:81] offset:0 sc1
v_accvgpr_read_b32 v56, a208
v_accvgpr_read_b32 v8, a212
v_accvgpr_read_b32 v57, a209
v_accvgpr_read_b32 v9, a213
M v[124:127],a[124:127],v[140:143],v[124:127]
T a[124:125],v27,1024
T a[126:127],v27,9216
s_waitcnt lgkmcnt(8)
v_accvgpr_read_b32 v58, a210
v_accvgpr_read_b32 v10, a214
v_accvgpr_read_b32 v59, a211
M v[64:67],a[96:99],v[144:147],v[64:67]
v_accvgpr_read_b32 v11, a215
v_accvgpr_read_b32 v52, a176
v_accvgpr_read_b32 v44, a180
v_accvgpr_read_b32 v53, a177
v_accvgpr_read_b32 v45, a181
v_accvgpr_read_b32 v54, a178
M v[96:99],a[96:99],v[148:151],v[96:99]
T a[96:97],v24,2048
global_load_lds_dwordx4 v20, s[82:83] offset:2048
v_accvgpr_read_b32 v46, a182
v_accvgpr_read_b32 v55, a179
v_accvgpr_read_b32 v47, a183
v_accvgpr_read_b32 v60, a240
M v[68:71],a[100:103],v[144:147],v[68:71]
T a[98:99],v24,10240
v_accvgpr_read_b32 v12, a244
v_accvgpr_read_b32 v61, a241
v_accvgpr_read_b32 v13, a245
v_accvgpr_read_b32 v62, a242
v_accvgpr_read_b32 v14, a246
M v[100:103],a[100:103],v[148:151],v[100:103]
T a[100:101],v25,2048
v_accvgpr_read_b32 v63, a243
v_accvgpr_read_b32 v15, a247
v_mul_f32 v48, v48, v56
v_mul_f32 v40, v40, v8
v_mul_f32 v49, v49, v57
M v[72:75],a[104:107],v[144:147],v[72:75]
T a[102:103],v25,10240
v_mul_f32 v41, v41, v9
v_mul_f32 v50, v50, v58
v_mul_f32 v42, v42, v10
v_mul_f32 v51, v51, v59
v_mul_f32 v43, v43, v11
M v[104:107],a[104:107],v[148:151],v[104:107]
T a[104:105],v26,2048
global_load_dwordx4 v[248:251], v16, s[80:81] offset:1024 sc1
v_mul_f32 v52, v52, v60
v_mul_f32 v44, v44, v12
v_mul_f32 v53, v53, v61
v_mul_f32 v45, v45, v13
M v[76:79],a[108:111],v[144:147],v[76:79]
T a[106:107],v26,10240
v_mul_f32 v54, v54, v62
v_mul_f32 v46, v46, v14
v_mul_f32 v55, v55, v63
v_mul_f32 v47, v47, v15
v_cvt_pk_bf16_f32 v48, v48, v49
M v[108:111],a[108:111],v[148:151],v[108:111]
T a[108:109],v27,2048
s_waitcnt lgkmcnt(7)
v_cvt_pk_bf16_f32 v40, v40, v41
v_cvt_pk_bf16_f32 v49, v50, v51
v_cvt_pk_bf16_f32 v41, v42, v43
v_cvt_pk_bf16_f32 v50, v52, v53
M v[80:83],a[112:115],v[144:147],v[80:83]
T a[110:111],v27,10240
v_cvt_pk_bf16_f32 v42, v44, v45
v_cvt_pk_bf16_f32 v51, v54, v55
v_cvt_pk_bf16_f32 v43, v46, v47
global_store_dwordx4 v16, v[48:51], s[60:61] sc1
global_store_dwordx4 v16, v[40:43], s[70:71] sc1
M v[112:115],a[112:115],v[148:151],v[112:115]
T a[112:113],v24,3072
global_load_lds_dwordx4 v21, s[82:83] offset:3072
s_add_u32 s60, s60, 294912
s_addc_u32 s61, s61, 0
s_add_u32 s70, s70, 294912
s_addc_u32 s71, s71, 0
M v[84:87],a[116:119],v[144:147],v[84:87]
T a[114:115],v24,11264
s_add_u32 s82, s82, 131072
s_addc_u32 s83, s83, 0
v_accvgpr_read_b32 v48, a152
v_accvgpr_read_b32 v40, a156
v_accvgpr_read_b32 v49, a153
M v[116:119],a[116:119],v[148:151],v[116:119]
T a[116:117],v25,3072
s_mov_b64 s[82:83], s[84:85]
v_accvgpr_read_b32 v41, a157
v_accvgpr_read_b32 v50, a154
v_accvgpr_read_b32 v42, a158
v_accvgpr_read_b32 v51, a155
M v[88:91],a[120:123],v[144:147],v[88:91]
T a[118:119],v25,11264
v_accvgpr_read_b32 v43, a159
v_accvgpr_read_b32 v56, a216
v_accvgpr_read_b32 v8, a220
v_accvgpr_read_b32 v57, a217
v_accvgpr_read_b32 v9, a221
M v[120:123],a[120:123],v[148:151],v[120:123]
T a[120:121],v26,3072
global_load_dwordx4 v[252:255], v17, s[80:81] offset:1024 sc1
s_add_u32 m0, s99, 114688
v_accvgpr_read_b32 v58, a218
v_accvgpr_read_b32 v10, a222
v_accvgpr_read_b32 v59, a219
M v[92:95],a[124:127],v[144:147],v[92:95]
T a[122:123],v26,11264
s_add_u32 s80, s80, 2048
s_addc_u32 s81, s81, 0
v_accvgpr_read_b32 v11, a223
v_accvgpr_read_b32 v52, a184
v_accvgpr_read_b32 v44, a188
M v[124:127],a[124:127],v[148:151],v[124:127]
T a[124:125],v27,3072
s_waitcnt vmcnt(47) lgkmcnt(7)
s_barrier
s_waitcnt vmcnt(25)
v_cmp_ne_u32 vcc, s98, v35
s_nop 3
s_and_b32 vcc_lo, vcc_lo, 0xffff0000
s_cbranch_scc0 Lokq1_%=
s_mov_b32 s65, 0x100000
Lspq1_%=:
s_sleep 1
global_load_dword v35, v34, s[96:97] offset:0 sc1
s_waitcnt vmcnt(0)
v_cmp_ne_u32 vcc, s98, v35
s_nop 3
s_and_b32 vcc_lo, vcc_lo, 0xffff0000
s_cbranch_scc0 Lokq1_%=
s_sub_u32 s65, s65, 1
s_cmp_lg_u32 s65, 0
s_cbranch_scc1 Lspq1_%=
Lokq1_%=:
s_mov_b64 s[80:81], s[86:87]
M v[64:67],a[96:99],v[152:155],v[64:67]
T a[126:127],v27,11264
v_accvgpr_read_b32 v53, a185
v_accvgpr_read_b32 v45, a189
v_accvgpr_read_b32 v54, a186
v_accvgpr_read_b32 v46, a190
v_accvgpr_read_b32 v55, a187
M v[96:99],a[96:99],v[156:159],v[96:99]
T a[96:97],v24,16384
v_accvgpr_read_b32 v47, a191
v_accvgpr_read_b32 v60, a248
v_accvgpr_read_b32 v12, a252
v_accvgpr_read_b32 v61, a249
v_accvgpr_read_b32 v13, a253
M v[68:71],a[100:103],v[152:155],v[68:71]
T a[98:99],v24,24576
global_load_lds_dwordx4 v18, s[82:83] offset:0
v_accvgpr_read_b32 v62, a250
v_accvgpr_read_b32 v14, a254
v_accvgpr_read_b32 v63, a251
v_accvgpr_read_b32 v15, a255
M v[100:103],a[100:103],v[156:159],v[100:103]
T a[100:101],v25,16384
v_mul_f32 v48, v48, v56
v_mul_f32 v40, v40, v8
v_mul_f32 v49, v49, v57
v_mul_f32 v41, v41, v9
v_mul_f32 v50, v50, v58
M v[72:75],a[104:107],v[152:155],v[72:75]
T a[102:103],v25,24576
v_mul_f32 v42, v42, v10
v_mul_f32 v51, v51, v59
v_mul_f32 v43, v43, v11
v_mul_f32 v52, v52, v60
v_mul_f32 v44, v44, v12
M v[104:107],a[104:107],v[156:159],v[104:107]
T a[104:105],v26,16384
v_mul_f32 v53, v53, v61
v_mul_f32 v45, v45, v13
v_mul_f32 v54, v54, v62
v_mul_f32 v46, v46, v14
v_mul_f32 v55, v55, v63
M v[76:79],a[108:111],v[152:155],v[76:79]
T a[106:107],v26,24576
global_load_dwordx4 v[128:131], v16, s[80:81] offset:0 sc1
v_mul_f32 v47, v47, v15
v_cvt_pk_bf16_f32 v48, v48, v49
v_cvt_pk_bf16_f32 v40, v40, v41
v_cvt_pk_bf16_f32 v49, v50, v51
M v[108:111],a[108:111],v[156:159],v[108:111]
T a[108:109],v27,16384
s_waitcnt lgkmcnt(7)
v_cvt_pk_bf16_f32 v41, v42, v43
v_cvt_pk_bf16_f32 v50, v52, v53
v_cvt_pk_bf16_f32 v42, v44, v45
v_cvt_pk_bf16_f32 v51, v54, v55
M v[80:83],a[112:115],v[152:155],v[80:83]
T a[110:111],v27,24576
v_cvt_pk_bf16_f32 v43, v46, v47
global_store_dwordx4 v16, v[48:51], s[60:61] sc1
global_store_dwordx4 v16, v[40:43], s[70:71] sc1
M v[112:115],a[112:115],v[156:159],v[112:115]
T a[112:113],v24,17408
M v[84:87],a[116:119],v[152:155],v[84:87]
T a[114:115],v24,25600
global_load_lds_dwordx4 v19, s[82:83] offset:1024
M v[116:119],a[116:119],v[156:159],v[116:119]
T a[116:117],v25,17408
M v[88:91],a[120:123],v[152:155],v[88:91]
T a[118:119],v25,25600
M v[120:123],a[120:123],v[156:159],v[120:123]
T a[120:121],v26,17408
M v[92:95],a[124:127],v[152:155],v[92:95]
T a[122:123],v26,25600
global_load_dwordx4 v[132:135], v17, s[80:81] offset:0 sc1
M v[124:127],a[124:127],v[156:159],v[124:127]
T a[124:125],v27,17408
T a[126:127],v27,25600
s_waitcnt lgkmcnt(8)
M v[64:67],a[96:99],v[160:163],v[64:67]
M v[96:99],a[96:99],v[164:167],v[96:99]
T a[96:97],v24,18432
global_load_lds_dwordx4 v20, s[82:83] offset:2048
M v[68:71],a[100:103],v[160:163],v[68:71]
T a[98:99],v24,26624
M v[100:103],a[100:103],v[164:167],v[100:103]
T a[100:101],v25,18432
M v[72:75],a[104:107],v[160:163],v[72:75]
T a[102:103],v25,26624
M v[104:107],a[104:107],v[164:167],v[104:107]
T a[104:105],v26,18432
global_load_dwordx4 v[136:139], v16, s[80:81] offset:1024 sc1
M v[76:79],a[108:111],v[160:163],v[76:79]
T a[106:107],v26,26624
M v[108:111],a[108:111],v[164:167],v[108:111]
T a[108:109],v27,18432
s_waitcnt lgkmcnt(7)
M v[80:83],a[112:115],v[160:163],v[80:83]
T a[110:111],v27,26624
M v[112:115],a[112:115],v[164:167],v[112:115]
T a[112:113],v24,19456
global_load_lds_dwordx4 v21, s[82:83] offset:3072
M v[84:87],a[116:119],v[160:163],v[84:87]
T a[114:115],v24,27648
s_add_u32 s82, s82, 131072
s_addc_u32 s83, s83, 0
M v[116:119],a[116:119],v[164:167],v[116:119]
T a[116:117],v25,19456
M v[88:91],a[120:123],v[160:163],v[88:91]
T a[118:119],v25,27648
M v[120:123],a[120:123],v[164:167],v[120:123]
T a[120:121],v26,19456
global_load_dwordx4 v[140:143], v17, s[80:81] offset:1024 sc1
s_add_u32 m0, s99, 0
M v[92:95],a[124:127],v[160:163],v[92:95]
T a[122:123],v26,27648
s_add_u32 s80, s80, 2048
s_addc_u32 s81, s81, 0
M v[124:127],a[124:127],v[164:167],v[124:127]
T a[124:125],v27,19456
s_waitcnt vmcnt(49) lgkmcnt(7)
s_barrier
M v[64:67],a[96:99],v[168:171],v[64:67]
T a[126:127],v27,27648
M v[96:99],a[96:99],v[172:175],v[96:99]
T a[96:97],v28,16384
M v[68:71],a[100:103],v[168:171],v[68:71]
T a[98:99],v28,24576
global_load_lds_dwordx4 v18, s[82:83] offset:0
M v[100:103],a[100:103],v[172:175],v[100:103]
T a[100:101],v29,16384
M v[72:75],a[104:107],v[168:171],v[72:75]
T a[102:103],v29,24576
M v[104:107],a[104:107],v[172:175],v[104:107]
T a[104:105],v30,16384
M v[76:79],a[108:111],v[168:171],v[76:79]
T a[106:107],v30,24576
global_load_dwordx4 v[144:147], v16, s[80:81] offset:0 sc1
M v[108:111],a[108:111],v[172:175],v[108:111]
T a[108:109],v31,16384
s_waitcnt lgkmcnt(7)
M v[80:83],a[112:115],v[168:171],v[80:83]
T a[110:111],v31,24576
M v[112:115],a[112:115],v[172:175],v[112:115]
T a[112:113],v28,17408
M v[84:87],a[116:119],v[168:171],v[84:87]
T a[114:115],v28,25600
global_load_lds_dwordx4 v19, s[82:83] offset:1024
M v[116:119],a[116:119],v[172:175],v[116:119]
T a[116:117],v29,17408
M v[88:91],a[120:123],v[168:171],v[88:91]
T a[118:119],v29,25600
M v[120:123],a[120:123],v[172:175],v[120:123]
T a[120:121],v30,17408
M v[92:95],a[124:127],v[168:171],v[92:95]
T a[122:123],v30,25600
global_load_dwordx4 v[148:151], v17, s[80:81] offset:0 sc1
M v[124:127],a[124:127],v[172:175],v[124:127]
T a[124:125],v31,17408
T a[126:127],v31,25600
s_waitcnt lgkmcnt(8)
M v[64:67],a[96:99],v[176:179],v[64:67]
M v[96:99],a[96:99],v[180:183],v[96:99]
T a[96:97],v28,18432
global_load_lds_dwordx4 v20, s[82:83] offset:2048
M v[68:71],a[100:103],v[176:179],v[68:71]
T a[98:99],v28,26624
M v[100:103],a[100:103],v[180:183],v[100:103]
T a[100:101],v29,18432
M v[72:75],a[104:107],v[176:179],v[72:75]
T a[102:103],v29,26624
M v[104:107],a[104:107],v[180:183],v[104:107]
T a[104:105],v30,18432
global_load_dwordx4 v[152:155], v16, s[80:81] offset:1024 sc1
M v[76:79],a[108:111],v[176:179],v[76:79]
T a[106:107],v30,26624
M v[108:111],a[108:111],v[180:183],v[108:111]
T a[108:109],v31,18432
s_waitcnt lgkmcnt(7)
M v[80:83],a[112:115],v[176:179],v[80:83]
T a[110:111],v31,26624
M v[112:115],a[112:115],v[180:183],v[112:115]
T a[112:113],v28,19456
global_load_lds_dwordx4 v21, s[82:83] offset:3072
M v[84:87],a[116:119],v[176:179],v[84:87]
T a[114:115],v28,27648
s_add_u32 s82, s82, 131072
s_addc_u32 s83, s83, 0
M v[116:119],a[116:119],v[180:183],v[116:119]
T a[116:117],v29,19456
M v[88:91],a[120:123],v[176:179],v[88:91]
T a[118:119],v29,27648
M v[120:123],a[120:123],v[180:183],v[120:123]
T a[120:121],v30,19456
global_load_dwordx4 v[156:159], v17, s[80:81] offset:1024 sc1
s_add_u32 m0, s99, 16384
M v[92:95],a[124:127],v[176:179],v[92:95]
T a[122:123],v30,27648
s_add_u32 s80, s80, 2048
s_addc_u32 s81, s81, 0
M v[124:127],a[124:127],v[180:183],v[124:127]
T a[124:125],v31,19456
s_waitcnt vmcnt(49) lgkmcnt(7)
s_barrier
M v[64:67],a[96:99],v[184:187],v[64:67]
T a[126:127],v31,27648
M v[96:99],a[96:99],v[188:191],v[96:99]
T a[96:97],v24,32768
M v[68:71],a[100:103],v[184:187],v[68:71]
T a[98:99],v24,40960
global_load_lds_dwordx4 v18, s[82:83] offset:0
M v[100:103],a[100:103],v[188:191],v[100:103]
T a[100:101],v25,32768
M v[72:75],a[104:107],v[184:187],v[72:75]
T a[102:103],v25,40960
M v[104:107],a[104:107],v[188:191],v[104:107]
T a[104:105],v26,32768
M v[76:79],a[108:111],v[184:187],v[76:79]
T a[106:107],v26,40960
global_load_dwordx4 v[160:163], v16, s[80:81] offset:0 sc1
M v[108:111],a[108:111],v[188:191],v[108:111]
T a[108:109],v27,32768
s_waitcnt lgkmcnt(7)
M v[80:83],a[112:115],v[184:187],v[80:83]
T a[110:111],v27,40960
M v[112:115],a[112:115],v[188:191],v[112:115]
T a[112:113],v24,33792
M v[84:87],a[116:119],v[184:187],v[84:87]
T a[114:115],v24,41984
global_load_lds_dwordx4 v19, s[82:83] offset:1024
M v[116:119],a[116:119],v[188:191],v[116:119]
T a[116:117],v25,33792
M v[88:91],a[120:123],v[184:187],v[88:91]
T a[118:119],v25,41984
M v[120:123],a[120:123],v[188:191],v[120:123]
T a[120:121],v26,33792
M v[92:95],a[124:127],v[184:187],v[92:95]
T a[122:123],v26,41984
global_load_dwordx4 v[164:167], v17, s[80:81] offset:0 sc1
M v[124:127],a[124:127],v[188:191],v[124:127]
T a[124:125],v27,33792
T a[126:127],v27,41984
s_waitcnt lgkmcnt(8)
M v[64:67],a[96:99],v[192:195],v[64:67]
M v[96:99],a[96:99],v[196:199],v[96:99]
T a[96:97],v24,34816
global_load_lds_dwordx4 v20, s[82:83] offset:2048
M v[68:71],a[100:103],v[192:195],v[68:71]
T a[98:99],v24,43008
M v[100:103],a[100:103],v[196:199],v[100:103]
T a[100:101],v25,34816
M v[72:75],a[104:107],v[192:195],v[72:75]
T a[102:103],v25,43008
M v[104:107],a[104:107],v[196:199],v[104:107]
T a[104:105],v26,34816
global_load_dwordx4 v[168:171], v16, s[80:81] offset:1024 sc1
M v[76:79],a[108:111],v[192:195],v[76:79]
T a[106:107],v26,43008
M v[108:111],a[108:111],v[196:199],v[108:111]
T a[108:109],v27,34816
s_waitcnt lgkmcnt(7)
M v[80:83],a[112:115],v[192:195],v[80:83]
T a[110:111],v27,43008
M v[112:115],a[112:115],v[196:199],v[112:115]
T a[112:113],v24,35840
global_load_lds_dwordx4 v21, s[82:83] offset:3072
M v[84:87],a[116:119],v[192:195],v[84:87]
T a[114:115],v24,44032
s_add_u32 s82, s82, 131072
s_addc_u32 s83, s83, 0
M v[116:119],a[116:119],v[196:199],v[116:119]
T a[116:117],v25,35840
M v[88:91],a[120:123],v[192:195],v[88:91]
T a[118:119],v25,44032
M v[120:123],a[120:123],v[196:199],v[120:123]
T a[120:121],v26,35840
global_load_dwordx4 v[172:175], v17, s[80:81] offset:1024 sc1
s_add_u32 m0, s99, 81920
M v[92:95],a[124:127],v[192:195],v[92:95]
T a[122:123],v26,44032
s_add_u32 s80, s80, 2048
s_addc_u32 s81, s81, 0
M v[124:127],a[124:127],v[196:199],v[124:127]
T a[124:125],v27,35840
s_waitcnt vmcnt(48) lgkmcnt(7)
s_barrier
M v[64:67],a[96:99],v[200:203],v[64:67]
T a[126:127],v27,44032
M v[96:99],a[96:99],v[204:207],v[96:99]
T a[96:97],v24,49152
M v[68:71],a[100:103],v[200:203],v[68:71]
T a[98:99],v24,57344
global_load_lds_dwordx4 v18, s[82:83] offset:0
M v[100:103],a[100:103],v[204:207],v[100:103]
T a[100:101],v25,49152
M v[72:75],a[104:107],v[200:203],v[72:75]
T a[102:103],v25,57344
M v[104:107],a[104:107],v[204:207],v[104:107]
T a[104:105],v26,49152
M v[76:79],a[108:111],v[200:203],v[76:79]
T a[106:107],v26,57344
global_load_dwordx4 v[176:179], v16, s[80:81] offset:0 sc1
M v[108:111],a[108:111],v[204:207],v[108:111]
T a[108:109],v27,49152
s_waitcnt lgkmcnt(7)
M v[80:83],a[112:115],v[200:203],v[80:83]
T a[110:111],v27,57344
M v[112:115],a[112:115],v[204:207],v[112:115]
T a[112:113],v24,50176
M v[84:87],a[116:119],v[200:203],v[84:87]
T a[114:115],v24,58368
global_load_lds_dwordx4 v19, s[82:83] offset:1024
M v[116:119],a[116:119],v[204:207],v[116:119]
T a[116:117],v25,50176
M v[88:91],a[120:123],v[200:203],v[88:91]
T a[118:119],v25,58368
M v[120:123],a[120:123],v[204:207],v[120:123]
T a[120:121],v26,50176
M v[92:95],a[124:127],v[200:203],v[92:95]
T a[122:123],v26,58368
global_load_dwordx4 v[180:183], v17, s[80:81] offset:0 sc1
M v[124:127],a[124:127],v[204:207],v[124:127]
T a[124:125],v27,50176
T a[126:127],v27,58368
s_waitcnt lgkmcnt(8)
M v[64:67],a[96:99],v[208:211],v[64:67]
M v[96:99],a[96:99],v[212:215],v[96:99]
T a[96:97],v24,51200
global_load_lds_dwordx4 v20, s[82:83] offset:2048
M v[68:71],a[100:103],v[208:211],v[68:71]
T a[98:99],v24,59392
M v[100:103],a[100:103],v[212:215],v[100:103]
T a[100:101],v25,51200
M v[72:75],a[104:107],v[208:211],v[72:75]
T a[102:103],v25,59392
M v[104:107],a[104:107],v[212:215],v[104:107]
T a[104:105],v26,51200
global_load_dwordx4 v[184:187], v16, s[80:81] offset:1024 sc1
M v[76:79],a[108:111],v[208:211],v[76:79]
T a[106:107],v26,59392
M v[108:111],a[108:111],v[212:215],v[108:111]
T a[108:109],v27,51200
s_waitcnt lgkmcnt(7)
M v[80:83],a[112:115],v[208:211],v[80:83]
T a[110:111],v27,59392
M v[112:115],a[112:115],v[212:215],v[112:115]
T a[112:113],v24,52224
global_load_lds_dwordx4 v21, s[82:83] offset:3072
M v[84:87],a[116:119],v[208:211],v[84:87]
T a[114:115],v24,60416
s_add_u32 s82, s82, 131072
s_addc_u32 s83, s83, 0
M v[116:119],a[116:119],v[212:215],v[116:119]
T a[116:117],v25,52224
M v[88:91],a[120:123],v[208:211],v[88:91]
T a[118:119],v25,60416
M v[120:123],a[120:123],v[212:215],v[120:123]
T a[120:121],v26,52224
global_load_dwordx4 v[188:191], v17, s[80:81] offset:1024 sc1
s_add_u32 m0, s99, 32768
M v[92:95],a[124:127],v[208:211],v[92:95]
T a[122:123],v26,60416
s_add_u32 s80, s80, 2048
s_addc_u32 s81, s81, 0
M v[124:127],a[124:127],v[212:215],v[124:127]
T a[124:125],v27,52224
s_waitcnt vmcnt(30) lgkmcnt(7)
s_barrier
s_cmp_lg_u32 s99, 0
s_cbranch_scc1 Lnf2M_%=
s_mov_b64 exec, 1
v_mov_b32 v37, s98
global_store_dword v36, v37, s[96:97] offset:1024 sc1
s_mov_b64 exec, -1
Lnf2M_%=:
s_nop 0
M v[64:67],a[96:99],v[216:219],v[64:67]
T a[126:127],v27,60416
M v[96:99],a[96:99],v[220:223],v[96:99]
T a[96:97],v28,32768
M v[68:71],a[100:103],v[216:219],v[68:71]
T a[98:99],v28,40960
global_load_lds_dwordx4 v18, s[82:83] offset:0
M v[100:103],a[100:103],v[220:223],v[100:103]
T a[100:101],v29,32768
M v[72:75],a[104:107],v[216:219],v[72:75]
T a[102:103],v29,40960
M v[104:107],a[104:107],v[220:223],v[104:107]
T a[104:105],v30,32768
M v[76:79],a[108:111],v[216:219],v[76:79]
T a[106:107],v30,40960
global_load_dwordx4 v[192:195], v16, s[80:81] offset:0 sc1
M v[108:111],a[108:111],v[220:223],v[108:111]
T a[108:109],v31,32768
s_waitcnt lgkmcnt(7)
M v[80:83],a[112:115],v[216:219],v[80:83]
T a[110:111],v31,40960
M v[112:115],a[112:115],v[220:223],v[112:115]
T a[112:113],v28,33792
M v[84:87],a[116:119],v[216:219],v[84:87]
T a[114:115],v28,41984
global_load_lds_dwordx4 v19, s[82:83] offset:1024
M v[116:119],a[116:119],v[220:223],v[116:119]
T a[116:117],v29,33792
M v[88:91],a[120:123],v[216:219],v[88:91]
T a[118:119],v29,41984
M v[120:123],a[120:123],v[220:223],v[120:123]
T a[120:121],v30,33792
M v[92:95],a[124:127],v[216:219],v[92:95]
T a[122:123],v30,41984
global_load_dwordx4 v[196:199], v17, s[80:81] offset:0 sc1
M v[124:127],a[124:127],v[220:223],v[124:127]
T a[124:125],v31,33792
T a[126:127],v31,41984
s_waitcnt lgkmcnt(8)
M v[64:67],a[96:99],v[224:227],v[64:67]
M v[96:99],a[96:99],v[228:231],v[96:99]
T a[96:97],v28,34816
global_load_lds_dwordx4 v20, s[82:83] offset:2048
M v[68:71],a[100:103],v[224:227],v[68:71]
T a[98:99],v28,43008
M v[100:103],a[100:103],v[228:231],v[100:103]
T a[100:101],v29,34816
M v[72:75],a[104:107],v[224:227],v[72:75]
T a[102:103],v29,43008
M v[104:107],a[104:107],v[228:231],v[104:107]
T a[104:105],v30,34816
global_load_dwordx4 v[200:203], v16, s[80:81] offset:1024 sc1
M v[76:79],a[108:111],v[224:227],v[76:79]
T a[106:107],v30,43008
M v[108:111],a[108:111],v[228:231],v[108:111]
T a[108:109],v31,34816
s_waitcnt lgkmcnt(7)
M v[80:83],a[112:115],v[224:227],v[80:83]
T a[110:111],v31,43008
M v[112:115],a[112:115],v[228:231],v[112:115]
T a[112:113],v28,35840
global_load_lds_dwordx4 v21, s[82:83] offset:3072
M v[84:87],a[116:119],v[224:227],v[84:87]
T a[114:115],v28,44032
s_add_u32 s82, s82, 131072
s_addc_u32 s83, s83, 0
M v[116:119],a[116:119],v[228:231],v[116:119]
T a[116:117],v29,35840
M v[88:91],a[120:123],v[224:227],v[88:91]
T a[118:119],v29,44032
M v[120:123],a[120:123],v[228:231],v[120:123]
T a[120:121],v30,35840
global_load_dwordx4 v[204:207], v17, s[80:81] offset:1024 sc1
s_add_u32 m0, s99, 49152
M v[92:95],a[124:127],v[224:227],v[92:95]
T a[122:123],v30,44032
s_add_u32 s80, s80, 2048
s_addc_u32 s81, s81, 0
M v[124:127],a[124:127],v[228:231],v[124:127]
T a[124:125],v31,35840
s_waitcnt vmcnt(42) lgkmcnt(7)
s_barrier
M v[64:67],a[96:99],v[232:235],v[64:67]
T a[126:127],v31,44032
M v[96:99],a[96:99],v[236:239],v[96:99]
T a[96:97],v28,0
M v[68:71],a[100:103],v[232:235],v[68:71]
T a[98:99],v28,8192
global_load_lds_dwordx4 v18, s[82:83] offset:0
M v[100:103],a[100:103],v[236:239],v[100:103]
T a[100:101],v29,0
M v[72:75],a[104:107],v[232:235],v[72:75]
T a[102:103],v29,8192
M v[104:107],a[104:107],v[236:239],v[104:107]
T a[104:105],v30,0
M v[76:79],a[108:111],v[232:235],v[76:79]
T a[106:107],v30,8192
global_load_dwordx4 v[208:211], v16, s[80:81] offset:0 sc1
M v[108:111],a[108:111],v[236:239],v[108:111]
T a[108:109],v31,0
s_waitcnt lgkmcnt(7)
M v[80:83],a[112:115],v[232:235],v[80:83]
T a[110:111],v31,8192
M v[112:115],a[112:115],v[236:239],v[112:115]
T a[112:113],v28,1024
M v[84:87],a[116:119],v[232:235],v[84:87]
T a[114:115],v28,9216
global_load_lds_dwordx4 v19, s[82:83] offset:1024
M v[116:119],a[116:119],v[236:239],v[116:119]
T a[116:117],v29,1024
M v[88:91],a[120:123],v[232:235],v[88:91]
T a[118:119],v29,9216
M v[120:123],a[120:123],v[236:239],v[120:123]
T a[120:121],v30,1024
M v[92:95],a[124:127],v[232:235],v[92:95]
T a[122:123],v30,9216
global_load_dwordx4 v[212:215], v17, s[80:81] offset:0 sc1
M v[124:127],a[124:127],v[236:239],v[124:127]
T a[124:125],v31,1024
T a[126:127],v31,9216
s_waitcnt lgkmcnt(8)
M v[64:67],a[96:99],v[240:243],v[64:67]
M v[96:99],a[96:99],v[244:247],v[96:99]
T a[96:97],v28,2048
global_load_lds_dwordx4 v20, s[82:83] offset:2048
M v[68:71],a[100:103],v[240:243],v[68:71]
T a[98:99],v28,10240
M v[100:103],a[100:103],v[244:247],v[100:103]
T a[100:101],v29,2048
M v[72:75],a[104:107],v[240:243],v[72:75]
T a[102:103],v29,10240
M v[104:107],a[104:107],v[244:247],v[104:107]
T a[104:105],v30,2048
global_load_dwordx4 v[216:219], v16, s[80:81] offset:1024 sc1
M v[76:79],a[108:111],v[240:243],v[76:79]
T a[106:107],v30,10240
M v[108:111],a[108:111],v[244:247],v[108:111]
T a[108:109],v31,2048
s_waitcnt lgkmcnt(7)
M v[80:83],a[112:115],v[240:243],v[80:83]
T a[110:111],v31,10240
M v[112:115],a[112:115],v[244:247],v[112:115]
T a[112:113],v28,3072
global_load_lds_dwordx4 v21, s[82:83] offset:3072
M v[84:87],a[116:119],v[240:243],v[84:87]
T a[114:115],v28,11264
s_add_u32 s82, s82, 131072
s_addc_u32 s83, s83, 0
M v[116:119],a[116:119],v[244:247],v[116:119]
T a[116:117],v29,3072
M v[88:91],a[120:123],v[240:243],v[88:91]
T a[118:119],v29,11264
M v[120:123],a[120:123],v[244:247],v[120:123]
T a[120:121],v30,3072
global_load_dwordx4 v[220:223], v17, s[80:81] offset:1024 sc1
s_add_u32 m0, s99, 98304
M v[92:95],a[124:127],v[240:243],v[92:95]
T a[122:123],v30,11264
s_add_u32 s80, s80, 2048
s_addc_u32 s81, s81, 0
M v[124:127],a[124:127],v[244:247],v[124:127]
T a[124:125],v31,3072
s_waitcnt vmcnt(40) lgkmcnt(7)
s_barrier
M v[64:67],a[96:99],v[248:251],v[64:67]
T a[126:127],v31,11264
global_load_dword v35, v34, s[96:97] offset:1024 sc1
M v[96:99],a[96:99],v[252:255],v[96:99]
T a[96:97],v28,49152
M v[68:71],a[100:103],v[248:251],v[68:71]
T a[98:99],v28,57344
global_load_lds_dwordx4 v18, s[82:83] offset:0
M v[100:103],a[100:103],v[252:255],v[100:103]
T a[100:101],v29,49152
M v[72:75],a[104:107],v[248:251],v[72:75]
T a[102:103],v29,57344
M v[104:107],a[104:107],v[252:255],v[104:107]
T a[104:105],v30,49152
M v[76:79],a[108:111],v[248:251],v[76:79]
T a[106:107],v30,57344
global_load_dwordx4 v[224:227], v16, s[80:81] offset:0 sc1
M v[108:111],a[108:111],v[252:255],v[108:111]
T a[108:109],v31,49152
s_waitcnt lgkmcnt(7)
M v[80:83],a[112:115],v[248:251],v[80:83]
T a[110:111],v31,57344
M v[112:115],a[112:115],v[252:255],v[112:115]
T a[112:113],v28,50176
M v[84:87],a[116:119],v[248:251],v[84:87]
T a[114:115],v28,58368
global_load_lds_dwordx4 v19, s[82:83] offset:1024
M v[116:119],a[116:119],v[252:255],v[116:119]
T a[116:117],v29,50176
M v[88:91],a[120:123],v[248:251],v[88:91]
T a[118:119],v29,58368
M v[120:123],a[120:123],v[252:255],v[120:123]
T a[120:121],v30,50176
M v[92:95],a[124:127],v[248:251],v[92:95]
T a[122:123],v30,58368
global_load_dwordx4 v[228:231], v17, s[80:81] offset:0 sc1
M v[124:127],a[124:127],v[252:255],v[124:127]
T a[124:125],v31,50176
T a[126:127],v31,58368
s_mov_b32 s64, 2
Lg2M_%=:
s_waitcnt lgkmcnt(8)
M v[64:67],a[96:99],v[128:131],v[64:67]
M v[96:99],a[96:99],v[132:135],v[96:99]
T a[96:97],v28,51200
global_load_lds_dwordx4 v20, s[82:83] offset:2048
M v[68:71],a[100:103],v[128:131],v[68:71]
T a[98:99],v28,59392
M v[100:103],a[100:103],v[132:135],v[100:103]
T a[100:101],v29,51200
M v[72:75],a[104:107],v[128:131],v[72:75]
T a[102:103],v29,59392
M v[104:107],a[104:107],v[132:135],v[104:107]
T a[104:105],v30,51200
global_load_dwordx4 v[232:235], v16, s[80:81] offset:1024 sc1
M v[76:79],a[108:111],v[128:131],v[76:79]
T a[106:107],v30,59392
M v[108:111],a[108:111],v[132:135],v[108:111]
T a[108:109],v31,51200
s_waitcnt lgkmcnt(7)
M v[80:83],a[112:115],v[128:131],v[80:83]
T a[110:111],v31,59392
M v[112:115],a[112:115],v[132:135],v[112:115]
T a[112:113],v28,52224
global_load_lds_dwordx4 v21, s[82:83] offset:3072
M v[84:87],a[116:119],v[128:131],v[84:87]
T a[114:115],v28,60416
s_add_u32 s82, s82, 131072
s_addc_u32 s83, s83, 0
M v[116:119],a[116:119],v[132:135],v[116:119]
T a[116:117],v29,52224
M v[88:91],a[120:123],v[128:131],v[88:91]
T a[118:119],v29,60416
M v[120:123],a[120:123],v[132:135],v[120:123]
T a[120:121],v30,52224
global_load_dwordx4 v[236:239], v17, s[80:81] offset:1024 sc1
s_add_u32 m0, s99, 65536
M v[92:95],a[124:127],v[128:131],v[92:95]
T a[122:123],v30,60416
s_add_u32 s80, s80, 2048
s_addc_u32 s81, s81, 0
M v[124:127],a[124:127],v[132:135],v[124:127]
T a[124:125],v31,52224
s_waitcnt vmcnt(41) lgkmcnt(7)
s_barrier
M v[64:67],a[96:99],v[136:139],v[64:67]
T a[126:127],v31,60416
M v[96:99],a[96:99],v[140:143],v[96:99]
T a[96:97],v24,0
M v[68:71],a[100:103],v[136:139],v[68:71]
T a[98:99],v24,8192
global_load_lds_dwordx4 v18, s[82:83] offset:0
M v[100:103],a[100:103],v[140:143],v[100:103]
T a[100:101],v25,0
M v[72:75],a[104:107],v[136:139],v[72:75]
T a[102:103],v25,8192
M v[104:107],a[104:107],v[140:143],v[104:107]
T a[104:105],v26,0
M v[76:79],a[108:111],v[136:139],v[76:79]
T a[106:107],v26,8192
global_load_dwordx4 v[240:243], v16, s[80:81] offset:0 sc1
M v[108:111],a[108:111],v[140:143],v[108:111]
T a[108:109],v27,0
s_waitcnt lgkmcnt(7)
M v[80:83],a[112:115],v[136:139],v[80:83]
T a[110:111],v27,8192
M v[112:115],a[112:115],v[140:143],v[112:115]
T a[112:113],v24,1024
M v[84:87],a[116:119],v[136:139],v[84:87]
T a[114:115],v24,9216
global_load_lds_dwordx4 v19, s[82:83] offset:1024
M v[116:119],a[116:119],v[140:143],v[116:119]
T a[116:117],v25,1024
M v[88:91],a[120:123],v[136:139],v[88:91]
T a[118:119],v25,9216
M v[120:123],a[120:123],v[140:143],v[120:123]
T a[120:121],v26,1024
M v[92:95],a[124:127],v[136:139],v[92:95]
T a[122:123],v26,9216
global_load_dwordx4 v[244:247], v17, s[80:81] offset:0 sc1
M v[124:127],a[124:127],v[140:143],v[124:127]
T a[124:125],v27,1024
T a[126:127],v27,9216
s_waitcnt lgkmcnt(8)
M v[64:67],a[96:99],v[144:147],v[64:67]
M v[96:99],a[96:99],v[148:151],v[96:99]
T a[96:97],v24,2048
global_load_lds_dwordx4 v20, s[82:83] offset:2048
M v[68:71],a[100:103],v[144:147],v[68:71]
T a[98:99],v24,10240
M v[100:103],a[100:103],v[148:151],v[100:103]
T a[100:101],v25,2048
M v[72:75],a[104:107],v[144:147],v[72:75]
T a[102:103],v25,10240
M v[104:107],a[104:107],v[148:151],v[104:107]
T a[104:105],v26,2048
global_load_dwordx4 v[248:251], v16, s[80:81] offset:1024 sc1
M v[76:79],a[108:111],v[144:147],v[76:79]
T a[106:107],v26,10240
M v[108:111],a[108:111],v[148:151],v[108:111]
T a[108:109],v27,2048
s_waitcnt lgkmcnt(7)
M v[80:83],a[112:115],v[144:147],v[80:83]
T a[110:111],v27,10240
M v[112:115],a[112:115],v[148:151],v[112:115]
T a[112:113],v24,3072
global_load_lds_dwordx4 v21, s[82:83] offset:3072
M v[84:87],a[116:119],v[144:147],v[84:87]
T a[114:115],v24,11264
s_add_u32 s82, s82, 131072
s_addc_u32 s83, s83, 0
M v[116:119],a[116:119],v[148:151],v[116:119]
T a[116:117],v25,3072
s_cmp_eq_u32 s64, 1
s_cselect_b64 s[82:83], s[88:89], s[82:83]
M v[88:91],a[120:123],v[144:147],v[88:91]
T a[118:119],v25,11264
M v[120:123],a[120:123],v[148:151],v[120:123]
T a[120:121],v26,3072
global_load_dwordx4 v[252:255], v17, s[80:81] offset:1024 sc1
s_add_u32 m0, s99, 114688
M v[92:95],a[124:127],v[144:147],v[92:95]
T a[122:123],v26,11264
s_add_u32 s80, s80, 2048
s_addc_u32 s81, s81, 0
M v[124:127],a[124:127],v[148:151],v[124:127]
T a[124:125],v27,3072
s_waitcnt vmcnt(41) lgkmcnt(7)
s_barrier
s_waitcnt vmcnt(16)
v_cmp_ne_u32 vcc, s98, v35
s_nop 3
s_and_b32 vcc_lo, vcc_lo, s66
s_cbranch_scc0 LoklpM_%=
s_mov_b32 s65, 0x100000
LsplpM_%=:
s_sleep 1
global_load_dword v35, v34, s[96:97] offset:1024 sc1
s_waitcnt vmcnt(0)
v_cmp_ne_u32 vcc, s98, v35
s_nop 3
s_and_b32 vcc_lo, vcc_lo, s66
s_cbranch_scc0 LoklpM_%=
s_sub_u32 s65, s65, 1
s_cmp_lg_u32 s65, 0
s_cbranch_scc1 LsplpM_%=
LoklpM_%=:
M v[64:67],a[96:99],v[152:155],v[64:67]
T a[126:127],v27,11264
s_cmp_eq_u32 s64, 1
s_cselect_b64 s[80:81], s[90:91], s[80:81]
M v[96:99],a[96:99],v[156:159],v[96:99]
T a[96:97],v24,16384
M v[68:71],a[100:103],v[152:155],v[68:71]
T a[98:99],v24,24576
global_load_lds_dwordx4 v18, s[82:83] offset:0
M v[100:103],a[100:103],v[156:159],v[100:103]
T a[100:101],v25,16384
M v[72:75],a[104:107],v[152:155],v[72:75]
T a[102:103],v25,24576
s_lshr_b64 s[66:67], s[66:67], 32
M v[104:107],a[104:107],v[156:159],v[104:107]
T a[104:105],v26,16384
M v[76:79],a[108:111],v[152:155],v[76:79]
T a[106:107],v26,24576
global_load_dwordx4 v[128:131], v16, s[80:81] offset:0 sc1
M v[108:111],a[108:111],v[156:159],v[108:111]
T a[108:109],v27,16384
s_waitcnt lgkmcnt(7)
M v[80:83],a[112:115],v[152:155],v[80:83]
T a[110:111],v27,24576
M v[112:115],a[112:115],v[156:159],v[112:115]
T a[112:113],v24,17408
M v[84:87],a[116:119],v[152:155],v[84:87]
T a[114:115],v24,25600
global_load_lds_dwordx4 v19, s[82:83] offset:1024
M v[116:119],a[116:119],v[156:159],v[116:119]
T a[116:117],v25,17408
M v[88:91],a[120:123],v[152:155],v[88:91]
T a[118:119],v25,25600
M v[120:123],a[120:123],v[156:159],v[120:123]
T a[120:121],v26,17408
M v[92:95],a[124:127],v[152:155],v[92:95]
T a[122:123],v26,25600
global_load_dwordx4 v[132:135], v17, s[80:81] offset:0 sc1
M v[124:127],a[124:127],v[156:159],v[124:127]
T a[124:125],v27,17408
T a[126:127],v27,25600
s_waitcnt lgkmcnt(8)
M v[64:67],a[96:99],v[160:163],v[64:67]
M v[96:99],a[96:99],v[164:167],v[96:99]
T a[96:97],v24,18432
global_load_lds_dwordx4 v20, s[82:83] offset:2048
M v[68:71],a[100:103],v[160:163],v[68:71]
T a[98:99],v24,26624
M v[100:103],a[100:103],v[164:167],v[100:103]
T a[100:101],v25,18432
M v[72:75],a[104:107],v[160:163],v[72:75]
T a[102:103],v25,26624
M v[104:107],a[104:107],v[164:167],v[104:107]
T a[104:105],v26,18432
global_load_dwordx4 v[136:139], v16, s[80:81] offset:1024 sc1
M v[76:79],a[108:111],v[160:163],v[76:79]
T a[106:107],v26,26624
M v[108:111],a[108:111],v[164:167],v[108:111]
T a[108:109],v27,18432
s_waitcnt lgkmcnt(7)
M v[80:83],a[112:115],v[160:163],v[80:83]
T a[110:111],v27,26624
M v[112:115],a[112:115],v[164:167],v[112:115]
T a[112:113],v24,19456
global_load_lds_dwordx4 v21, s[82:83] offset:3072
M v[84:87],a[116:119],v[160:163],v[84:87]
T a[114:115],v24,27648
s_add_u32 s82, s82, 131072
s_addc_u32 s83, s83, 0
M v[116:119],a[116:119],v[164:167],v[116:119]
T a[116:117],v25,19456
M v[88:91],a[120:123],v[160:163],v[88:91]
T a[118:119],v25,27648
M v[120:123],a[120:123],v[164:167],v[120:123]
T a[120:121],v26,19456
global_load_dwordx4 v[140:143], v17, s[80:81] offset:1024 sc1
s_add_u32 m0, s99, 0
M v[92:95],a[124:127],v[160:163],v[92:95]
T a[122:123],v26,27648
s_add_u32 s80, s80, 2048
s_addc_u32 s81, s81, 0
M v[124:127],a[124:127],v[164:167],v[124:127]
T a[124:125],v27,19456
s_waitcnt vmcnt(41) lgkmcnt(7)
s_barrier
M v[64:67],a[96:99],v[168:171],v[64:67]
T a[126:127],v27,27648
M v[96:99],a[96:99],v[172:175],v[96:99]
T a[96:97],v28,16384
M v[68:71],a[100:103],v[168:171],v[68:71]
T a[98:99],v28,24576
global_load_lds_dwordx4 v18, s[82:83] offset:0
M v[100:103],a[100:103],v[172:175],v[100:103]
T a[100:101],v29,16384
M v[72:75],a[104:107],v[168:171],v[72:75]
T a[102:103],v29,24576
M v[104:107],a[104:107],v[172:175],v[104:107]
T a[104:105],v30,16384
M v[76:79],a[108:111],v[168:171],v[76:79]
T a[106:107],v30,24576
global_load_dwordx4 v[144:147], v16, s[80:81] offset:0 sc1
M v[108:111],a[108:111],v[172:175],v[108:111]
T a[108:109],v31,16384
s_waitcnt lgkmcnt(7)
M v[80:83],a[112:115],v[168:171],v[80:83]
T a[110:111],v31,24576
M v[112:115],a[112:115],v[172:175],v[112:115]
T a[112:113],v28,17408
M v[84:87],a[116:119],v[168:171],v[84:87]
T a[114:115],v28,25600
global_load_lds_dwordx4 v19, s[82:83] offset:1024
M v[116:119],a[116:119],v[172:175],v[116:119]
T a[116:117],v29,17408
M v[88:91],a[120:123],v[168:171],v[88:91]
T a[118:119],v29,25600
M v[120:123],a[120:123],v[172:175],v[120:123]
T a[120:121],v30,17408
M v[92:95],a[124:127],v[168:171],v[92:95]
T a[122:123],v30,25600
global_load_dwordx4 v[148:151], v17, s[80:81] offset:0 sc1
M v[124:127],a[124:127],v[172:175],v[124:127]
T a[124:125],v31,17408
T a[126:127],v31,25600
s_waitcnt lgkmcnt(8)
M v[64:67],a[96:99],v[176:179],v[64:67]
M v[96:99],a[96:99],v[180:183],v[96:99]
T a[96:97],v28,18432
global_load_lds_dwordx4 v20, s[82:83] offset:2048
M v[68:71],a[100:103],v[176:179],v[68:71]
T a[98:99],v28,26624
M v[100:103],a[100:103],v[180:183],v[100:103]
T a[100:101],v29,18432
M v[72:75],a[104:107],v[176:179],v[72:75]
T a[102:103],v29,26624
M v[104:107],a[104:107],v[180:183],v[104:107]
T a[104:105],v30,18432
global_load_dwordx4 v[152:155], v16, s[80:81] offset:1024 sc1
M v[76:79],a[108:111],v[176:179],v[76:79]
T a[106:107],v30,26624
M v[108:111],a[108:111],v[180:183],v[108:111]
T a[108:109],v31,18432
s_waitcnt lgkmcnt(7)
M v[80:83],a[112:115],v[176:179],v[80:83]
T a[110:111],v31,26624
M v[112:115],a[112:115],v[180:183],v[112:115]
T a[112:113],v28,19456
global_load_lds_dwordx4 v21, s[82:83] offset:3072
M v[84:87],a[116:119],v[176:179],v[84:87]
T a[114:115],v28,27648
s_add_u32 s82, s82, 131072
s_addc_u32 s83, s83, 0
M v[116:119],a[116:119],v[180:183],v[116:119]
T a[116:117],v29,19456
M v[88:91],a[120:123],v[176:179],v[88:91]
T a[118:119],v29,27648
M v[120:123],a[120:123],v[180:183],v[120:123]
T a[120:121],v30,19456
global_load_dwordx4 v[156:159], v17, s[80:81] offset:1024 sc1
s_add_u32 m0, s99, 16384
M v[92:95],a[124:127],v[176:179],v[92:95]
T a[122:123],v30,27648
s_add_u32 s80, s80, 2048
s_addc_u32 s81, s81, 0
M v[124:127],a[124:127],v[180:183],v[124:127]
T a[124:125],v31,19456
s_waitcnt vmcnt(41) lgkmcnt(7)
s_barrier
M v[64:67],a[96:99],v[184:187],v[64:67]
T a[126:127],v31,27648
M v[96:99],a[96:99],v[188:191],v[96:99]
T a[96:97],v24,32768
M v[68:71],a[100:103],v[184:187],v[68:71]
T a[98:99],v24,40960
global_load_lds_dwordx4 v18, s[82:83] offset:0
M v[100:103],a[100:103],v[188:191],v[100:103]
T a[100:101],v25,32768
M v[72:75],a[104:107],v[184:187],v[72:75]
T a[102:103],v25,40960
M v[104:107],a[104:107],v[188:191],v[104:107]
T a[104:105],v26,32768
M v[76:79],a[108:111],v[184:187],v[76:79]
T a[106:107],v26,40960
global_load_dwordx4 v[160:163], v16, s[80:81] offset:0 sc1
M v[108:111],a[108:111],v[188:191],v[108:111]
T a[108:109],v27,32768
s_waitcnt lgkmcnt(7)
M v[80:83],a[112:115],v[184:187],v[80:83]
T a[110:111],v27,40960
M v[112:115],a[112:115],v[188:191],v[112:115]
T a[112:113],v24,33792
M v[84:87],a[116:119],v[184:187],v[84:87]
T a[114:115],v24,41984
global_load_lds_dwordx4 v19, s[82:83] offset:1024
M v[116:119],a[116:119],v[188:191],v[116:119]
T a[116:117],v25,33792
M v[88:91],a[120:123],v[184:187],v[88:91]
T a[118:119],v25,41984
M v[120:123],a[120:123],v[188:191],v[120:123]
T a[120:121],v26,33792
M v[92:95],a[124:127],v[184:187],v[92:95]
T a[122:123],v26,41984
global_load_dwordx4 v[164:167], v17, s[80:81] offset:0 sc1
M v[124:127],a[124:127],v[188:191],v[124:127]
T a[124:125],v27,33792
T a[126:127],v27,41984
s_waitcnt lgkmcnt(8)
M v[64:67],a[96:99],v[192:195],v[64:67]
M v[96:99],a[96:99],v[196:199],v[96:99]
T a[96:97],v24,34816
global_load_lds_dwordx4 v20, s[82:83] offset:2048
M v[68:71],a[100:103],v[192:195],v[68:71]
T a[98:99],v24,43008
M v[100:103],a[100:103],v[196:199],v[100:103]
T a[100:101],v25,34816
M v[72:75],a[104:107],v[192:195],v[72:75]
T a[102:103],v25,43008
M v[104:107],a[104:107],v[196:199],v[104:107]
T a[104:105],v26,34816
global_load_dwordx4 v[168:171], v16, s[80:81] offset:1024 sc1
M v[76:79],a[108:111],v[192:195],v[76:79]
T a[106:107],v26,43008
M v[108:111],a[108:111],v[196:199],v[108:111]
T a[108:109],v27,34816
s_waitcnt lgkmcnt(7)
M v[80:83],a[112:115],v[192:195],v[80:83]
T a[110:111],v27,43008
M v[112:115],a[112:115],v[196:199],v[112:115]
T a[112:113],v24,35840
global_load_lds_dwordx4 v21, s[82:83] offset:3072
M v[84:87],a[116:119],v[192:195],v[84:87]
T a[114:115],v24,44032
s_add_u32 s82, s82, 131072
s_addc_u32 s83, s83, 0
M v[116:119],a[116:119],v[196:199],v[116:119]
T a[116:117],v25,35840
M v[88:91],a[120:123],v[192:195],v[88:91]
T a[118:119],v25,44032
M v[120:123],a[120:123],v[196:199],v[120:123]
T a[120:121],v26,35840
global_load_dwordx4 v[172:175], v17, s[80:81] offset:1024 sc1
s_add_u32 m0, s99, 81920
M v[92:95],a[124:127],v[192:195],v[92:95]
T a[122:123],v26,44032
s_add_u32 s80, s80, 2048
s_addc_u32 s81, s81, 0
M v[124:127],a[124:127],v[196:199],v[124:127]
T a[124:125],v27,35840
s_waitcnt vmcnt(41) lgkmcnt(7)
s_barrier
M v[64:67],a[96:99],v[200:203],v[64:67]
T a[126:127],v27,44032
M v[96:99],a[96:99],v[204:207],v[96:99]
T a[96:97],v24,49152
M v[68:71],a[100:103],v[200:203],v[68:71]
T a[98:99],v24,57344
global_load_lds_dwordx4 v18, s[82:83] offset:0
M v[100:103],a[100:103],v[204:207],v[100:103]
T a[100:101],v25,49152
M v[72:75],a[104:107],v[200:203],v[72:75]
T a[102:103],v25,57344
M v[104:107],a[104:107],v[204:207],v[104:107]
T a[104:105],v26,49152
M v[76:79],a[108:111],v[200:203],v[76:79]
T a[106:107],v26,57344
global_load_dwordx4 v[176:179], v16, s[80:81] offset:0 sc1
M v[108:111],a[108:111],v[204:207],v[108:111]
T a[108:109],v27,49152
s_waitcnt lgkmcnt(7)
M v[80:83],a[112:115],v[200:203],v[80:83]
T a[110:111],v27,57344
M v[112:115],a[112:115],v[204:207],v[112:115]
T a[112:113],v24,50176
M v[84:87],a[116:119],v[200:203],v[84:87]
T a[114:115],v24,58368
global_load_lds_dwordx4 v19, s[82:83] offset:1024
M v[116:119],a[116:119],v[204:207],v[116:119]
T a[116:117],v25,50176
M v[88:91],a[120:123],v[200:203],v[88:91]
T a[118:119],v25,58368
M v[120:123],a[120:123],v[204:207],v[120:123]
T a[120:121],v26,50176
M v[92:95],a[124:127],v[200:203],v[92:95]
T a[122:123],v26,58368
global_load_dwordx4 v[180:183], v17, s[80:81] offset:0 sc1
M v[124:127],a[124:127],v[204:207],v[124:127]
T a[124:125],v27,50176
T a[126:127],v27,58368
s_waitcnt lgkmcnt(8)
M v[64:67],a[96:99],v[208:211],v[64:67]
M v[96:99],a[96:99],v[212:215],v[96:99]
T a[96:97],v24,51200
global_load_lds_dwordx4 v20, s[82:83] offset:2048
M v[68:71],a[100:103],v[208:211],v[68:71]
T a[98:99],v24,59392
M v[100:103],a[100:103],v[212:215],v[100:103]
T a[100:101],v25,51200
M v[72:75],a[104:107],v[208:211],v[72:75]
T a[102:103],v25,59392
M v[104:107],a[104:107],v[212:215],v[104:107]
T a[104:105],v26,51200
global_load_dwordx4 v[184:187], v16, s[80:81] offset:1024 sc1
M v[76:79],a[108:111],v[208:211],v[76:79]
T a[106:107],v26,59392
M v[108:111],a[108:111],v[212:215],v[108:111]
T a[108:109],v27,51200
s_waitcnt lgkmcnt(7)
M v[80:83],a[112:115],v[208:211],v[80:83]
T a[110:111],v27,59392
M v[112:115],a[112:115],v[212:215],v[112:115]
T a[112:113],v24,52224
global_load_lds_dwordx4 v21, s[82:83] offset:3072
M v[84:87],a[116:119],v[208:211],v[84:87]
T a[114:115],v24,60416
s_add_u32 s82, s82, 131072
s_addc_u32 s83, s83, 0
M v[116:119],a[116:119],v[212:215],v[116:119]
T a[116:117],v25,52224
M v[88:91],a[120:123],v[208:211],v[88:91]
T a[118:119],v25,60416
M v[120:123],a[120:123],v[212:215],v[120:123]
T a[120:121],v26,52224
global_load_dwordx4 v[188:191], v17, s[80:81] offset:1024 sc1
s_add_u32 m0, s99, 32768
M v[92:95],a[124:127],v[208:211],v[92:95]
T a[122:123],v26,60416
s_add_u32 s80, s80, 2048
s_addc_u32 s81, s81, 0
M v[124:127],a[124:127],v[212:215],v[124:127]
T a[124:125],v27,52224
s_waitcnt vmcnt(40) lgkmcnt(7)
s_barrier
M v[64:67],a[96:99],v[216:219],v[64:67]
T a[126:127],v27,60416
M v[96:99],a[96:99],v[220:223],v[96:99]
T a[96:97],v28,32768
M v[68:71],a[100:103],v[216:219],v[68:71]
T a[98:99],v28,40960
global_load_lds_dwordx4 v18, s[82:83] offset:0
M v[100:103],a[100:103],v[220:223],v[100:103]
T a[100:101],v29,32768
M v[72:75],a[104:107],v[216:219],v[72:75]
T a[102:103],v29,40960
M v[104:107],a[104:107],v[220:223],v[104:107]
T a[104:105],v30,32768
M v[76:79],a[108:111],v[216:219],v[76:79]
T a[106:107],v30,40960
global_load_dwordx4 v[192:195], v16, s[80:81] offset:0 sc1
M v[108:111],a[108:111],v[220:223],v[108:111]
T a[108:109],v31,32768
s_waitcnt lgkmcnt(7)
M v[80:83],a[112:115],v[216:219],v[80:83]
T a[110:111],v31,40960
M v[112:115],a[112:115],v[220:223],v[112:115]
T a[112:113],v28,33792
M v[84:87],a[116:119],v[216:219],v[84:87]
T a[114:115],v28,41984
global_load_lds_dwordx4 v19, s[82:83] offset:1024
M v[116:119],a[116:119],v[220:223],v[116:119]
T a[116:117],v29,33792
M v[88:91],a[120:123],v[216:219],v[88:91]
T a[118:119],v29,41984
M v[120:123],a[120:123],v[220:223],v[120:123]
T a[120:121],v30,33792
M v[92:95],a[124:127],v[216:219],v[92:95]
T a[122:123],v30,41984
global_load_dwordx4 v[196:199], v17, s[80:81] offset:0 sc1
M v[124:127],a[124:127],v[220:223],v[124:127]
T a[124:125],v31,33792
T a[126:127],v31,41984
s_waitcnt lgkmcnt(8)
M v[64:67],a[96:99],v[224:227],v[64:67]
M v[96:99],a[96:99],v[228:231],v[96:99]
T a[96:97],v28,34816
global_load_lds_dwordx4 v20, s[82:83] offset:2048
M v[68:71],a[100:103],v[224:227],v[68:71]
T a[98:99],v28,43008
M v[100:103],a[100:103],v[228:231],v[100:103]
T a[100:101],v29,34816
M v[72:75],a[104:107],v[224:227],v[72:75]
T a[102:103],v29,43008
M v[104:107],a[104:107],v[228:231],v[104:107]
T a[104:105],v30,34816
global_load_dwordx4 v[200:203], v16, s[80:81] offset:1024 sc1
M v[76:79],a[108:111],v[224:227],v[76:79]
T a[106:107],v30,43008
M v[108:111],a[108:111],v[228:231],v[108:111]
T a[108:109],v31,34816
s_waitcnt lgkmcnt(7)
M v[80:83],a[112:115],v[224:227],v[80:83]
T a[110:111],v31,43008
M v[112:115],a[112:115],v[228:231],v[112:115]
T a[112:113],v28,35840
global_load_lds_dwordx4 v21, s[82:83] offset:3072
M v[84:87],a[116:119],v[224:227],v[84:87]
T a[114:115],v28,44032
s_add_u32 s82, s82, 131072
s_addc_u32 s83, s83, 0
M v[116:119],a[116:119],v[228:231],v[116:119]
T a[116:117],v29,35840
M v[88:91],a[120:123],v[224:227],v[88:91]
T a[118:119],v29,44032
M v[120:123],a[120:123],v[228:231],v[120:123]
T a[120:121],v30,35840
global_load_dwordx4 v[204:207], v17, s[80:81] offset:1024 sc1
s_add_u32 m0, s99, 49152
M v[92:95],a[124:127],v[224:227],v[92:95]
T a[122:123],v30,44032
s_add_u32 s80, s80, 2048
s_addc_u32 s81, s81, 0
M v[124:127],a[124:127],v[228:231],v[124:127]
T a[124:125],v31,35840
s_waitcnt vmcnt(40) lgkmcnt(7)
s_barrier
M v[64:67],a[96:99],v[232:235],v[64:67]
T a[126:127],v31,44032
M v[96:99],a[96:99],v[236:239],v[96:99]
T a[96:97],v28,0
M v[68:71],a[100:103],v[232:235],v[68:71]
T a[98:99],v28,8192
global_load_lds_dwordx4 v18, s[82:83] offset:0
M v[100:103],a[100:103],v[236:239],v[100:103]
T a[100:101],v29,0
M v[72:75],a[104:107],v[232:235],v[72:75]
T a[102:103],v29,8192
M v[104:107],a[104:107],v[236:239],v[104:107]
T a[104:105],v30,0
M v[76:79],a[108:111],v[232:235],v[76:79]
T a[106:107],v30,8192
global_load_dwordx4 v[208:211], v16, s[80:81] offset:0 sc1
M v[108:111],a[108:111],v[236:239],v[108:111]
T a[108:109],v31,0
s_waitcnt lgkmcnt(7)
M v[80:83],a[112:115],v[232:235],v[80:83]
T a[110:111],v31,8192
M v[112:115],a[112:115],v[236:239],v[112:115]
T a[112:113],v28,1024
M v[84:87],a[116:119],v[232:235],v[84:87]
T a[114:115],v28,9216
global_load_lds_dwordx4 v19, s[82:83] offset:1024
M v[116:119],a[116:119],v[236:239],v[116:119]
T a[116:117],v29,1024
M v[88:91],a[120:123],v[232:235],v[88:91]
T a[118:119],v29,9216
M v[120:123],a[120:123],v[236:239],v[120:123]
T a[120:121],v30,1024
M v[92:95],a[124:127],v[232:235],v[92:95]
T a[122:123],v30,9216
global_load_dwordx4 v[212:215], v17, s[80:81] offset:0 sc1
M v[124:127],a[124:127],v[236:239],v[124:127]
T a[124:125],v31,1024
T a[126:127],v31,9216
s_waitcnt lgkmcnt(8)
M v[64:67],a[96:99],v[240:243],v[64:67]
M v[96:99],a[96:99],v[244:247],v[96:99]
T a[96:97],v28,2048
global_load_lds_dwordx4 v20, s[82:83] offset:2048
M v[68:71],a[100:103],v[240:243],v[68:71]
T a[98:99],v28,10240
M v[100:103],a[100:103],v[244:247],v[100:103]
T a[100:101],v29,2048
M v[72:75],a[104:107],v[240:243],v[72:75]
T a[102:103],v29,10240
M v[104:107],a[104:107],v[244:247],v[104:107]
T a[104:105],v30,2048
global_load_dwordx4 v[216:219], v16, s[80:81] offset:1024 sc1
M v[76:79],a[108:111],v[240:243],v[76:79]
T a[106:107],v30,10240
M v[108:111],a[108:111],v[244:247],v[108:111]
T a[108:109],v31,2048
s_waitcnt lgkmcnt(7)
M v[80:83],a[112:115],v[240:243],v[80:83]
T a[110:111],v31,10240
M v[112:115],a[112:115],v[244:247],v[112:115]
T a[112:113],v28,3072
global_load_lds_dwordx4 v21, s[82:83] offset:3072
M v[84:87],a[116:119],v[240:243],v[84:87]
T a[114:115],v28,11264
s_add_u32 s82, s82, 131072
s_addc_u32 s83, s83, 0
M v[116:119],a[116:119],v[244:247],v[116:119]
T a[116:117],v29,3072
M v[88:91],a[120:123],v[240:243],v[88:91]
T a[118:119],v29,11264
M v[120:123],a[120:123],v[244:247],v[120:123]
T a[120:121],v30,3072
global_load_dwordx4 v[220:223], v17, s[80:81] offset:1024 sc1
s_add_u32 m0, s99, 98304
M v[92:95],a[124:127],v[240:243],v[92:95]
T a[122:123],v30,11264
s_add_u32 s80, s80, 2048
s_addc_u32 s81, s81, 0
M v[124:127],a[124:127],v[244:247],v[124:127]
T a[124:125],v31,3072
s_waitcnt vmcnt(40) lgkmcnt(7)
s_barrier
M v[64:67],a[96:99],v[248:251],v[64:67]
T a[126:127],v31,11264
global_load_dword v35, v34, s[96:97] offset:1024 sc1
M v[96:99],a[96:99],v[252:255],v[96:99]
T a[96:97],v28,49152
M v[68:71],a[100:103],v[248:251],v[68:71]
T a[98:99],v28,57344
global_load_lds_dwordx4 v18, s[82:83] offset:0
M v[100:103],a[100:103],v[252:255],v[100:103]
T a[100:101],v29,49152
M v[72:75],a[104:107],v[248:251],v[72:75]
T a[102:103],v29,57344
M v[104:107],a[104:107],v[252:255],v[104:107]
T a[104:105],v30,49152
M v[76:79],a[108:111],v[248:251],v[76:79]
T a[106:107],v30,57344
global_load_dwordx4 v[224:227], v16, s[80:81] offset:0 sc1
M v[108:111],a[108:111],v[252:255],v[108:111]
T a[108:109],v31,49152
s_waitcnt lgkmcnt(7)
M v[80:83],a[112:115],v[248:251],v[80:83]
T a[110:111],v31,57344
M v[112:115],a[112:115],v[252:255],v[112:115]
T a[112:113],v28,50176
M v[84:87],a[116:119],v[248:251],v[84:87]
T a[114:115],v28,58368
global_load_lds_dwordx4 v19, s[82:83] offset:1024
M v[116:119],a[116:119],v[252:255],v[116:119]
T a[116:117],v29,50176
M v[88:91],a[120:123],v[248:251],v[88:91]
T a[118:119],v29,58368
s_sub_u32 s64, s64, 1
M v[120:123],a[120:123],v[252:255],v[120:123]
T a[120:121],v30,50176
M v[92:95],a[124:127],v[248:251],v[92:95]
T a[122:123],v30,58368
global_load_dwordx4 v[228:231], v17, s[80:81] offset:0 sc1
s_cmp_lg_u32 s64, 0
M v[124:127],a[124:127],v[252:255],v[124:127]
T a[124:125],v31,50176
T a[126:127],v31,58368
s_cbranch_scc1 Lg2M_%=
s_waitcnt lgkmcnt(8)
M v[64:67],a[96:99],v[128:131],v[64:67]
M v[96:99],a[96:99],v[132:135],v[96:99]
T a[96:97],v28,51200
global_load_lds_dwordx4 v20, s[82:83] offset:2048
M v[68:71],a[100:103],v[128:131],v[68:71]
T a[98:99],v28,59392
M v[100:103],a[100:103],v[132:135],v[100:103]
T a[100:101],v29,51200
M v[72:75],a[104:107],v[128:131],v[72:75]
T a[102:103],v29,59392
M v[104:107],a[104:107],v[132:135],v[104:107]
T a[104:105],v30,51200
global_load_dwordx4 v[232:235], v16, s[80:81] offset:1024 sc1
M v[76:79],a[108:111],v[128:131],v[76:79]
T a[106:107],v30,59392
M v[108:111],a[108:111],v[132:135],v[108:111]
T a[108:109],v31,51200
s_waitcnt lgkmcnt(7)
M v[80:83],a[112:115],v[128:131],v[80:83]
T a[110:111],v31,59392
M v[112:115],a[112:115],v[132:135],v[112:115]
T a[112:113],v28,52224
global_load_lds_dwordx4 v21, s[82:83] offset:3072
M v[84:87],a[116:119],v[128:131],v[84:87]
T a[114:115],v28,60416
s_add_u32 s82, s82, 131072
s_addc_u32 s83, s83, 0
M v[116:119],a[116:119],v[132:135],v[116:119]
T a[116:117],v29,52224
M v[88:91],a[120:123],v[128:131],v[88:91]
T a[118:119],v29,60416
M v[120:123],a[120:123],v[132:135],v[120:123]
T a[120:121],v30,52224
global_load_dwordx4 v[236:239], v17, s[80:81] offset:1024 sc1
s_add_u32 m0, s99, 65536
M v[92:95],a[124:127],v[128:131],v[92:95]
T a[122:123],v30,60416
s_add_u32 s80, s80, 2048
s_addc_u32 s81, s81, 0
M v[124:127],a[124:127],v[132:135],v[124:127]
T a[124:125],v31,52224
s_waitcnt vmcnt(41) lgkmcnt(7)
s_barrier
M v[64:67],a[96:99],v[136:139],v[64:67]
T a[126:127],v31,60416
M v[96:99],a[96:99],v[140:143],v[96:99]
T a[96:97],v24,0
M v[68:71],a[100:103],v[136:139],v[68:71]
T a[98:99],v24,8192
global_load_lds_dwordx4 v18, s[82:83] offset:0
M v[100:103],a[100:103],v[140:143],v[100:103]
T a[100:101],v25,0
M v[72:75],a[104:107],v[136:139],v[72:75]
T a[102:103],v25,8192
M v[104:107],a[104:107],v[140:143],v[104:107]
T a[104:105],v26,0
M v[76:79],a[108:111],v[136:139],v[76:79]
T a[106:107],v26,8192
global_load_dwordx4 v[240:243], v16, s[80:81] offset:0 sc1
M v[108:111],a[108:111],v[140:143],v[108:111]
T a[108:109],v27,0
s_waitcnt lgkmcnt(7)
M v[80:83],a[112:115],v[136:139],v[80:83]
T a[110:111],v27,8192
M v[112:115],a[112:115],v[140:143],v[112:115]
T a[112:113],v24,1024
M v[84:87],a[116:119],v[136:139],v[84:87]
T a[114:115],v24,9216
global_load_lds_dwordx4 v19, s[82:83] offset:1024
M v[116:119],a[116:119],v[140:143],v[116:119]
T a[116:117],v25,1024
M v[88:91],a[120:123],v[136:139],v[88:91]
T a[118:119],v25,9216
M v[120:123],a[120:123],v[140:143],v[120:123]
T a[120:121],v26,1024
M v[92:95],a[124:127],v[136:139],v[92:95]
T a[122:123],v26,9216
global_load_dwordx4 v[244:247], v17, s[80:81] offset:0 sc1
M v[124:127],a[124:127],v[140:143],v[124:127]
T a[124:125],v27,1024
T a[126:127],v27,9216
s_waitcnt lgkmcnt(8)
M v[64:67],a[96:99],v[144:147],v[64:67]
M v[96:99],a[96:99],v[148:151],v[96:99]
T a[96:97],v24,2048
global_load_lds_dwordx4 v20, s[82:83] offset:2048
M v[68:71],a[100:103],v[144:147],v[68:71]
T a[98:99],v24,10240
M v[100:103],a[100:103],v[148:151],v[100:103]
T a[100:101],v25,2048
M v[72:75],a[104:107],v[144:147],v[72:75]
T a[102:103],v25,10240
M v[104:107],a[104:107],v[148:151],v[104:107]
T a[104:105],v26,2048
global_load_dwordx4 v[248:251], v16, s[80:81] offset:1024 sc1
M v[76:79],a[108:111],v[144:147],v[76:79]
T a[106:107],v26,10240
M v[108:111],a[108:111],v[148:151],v[108:111]
T a[108:109],v27,2048
s_waitcnt lgkmcnt(7)
M v[80:83],a[112:115],v[144:147],v[80:83]
T a[110:111],v27,10240
M v[112:115],a[112:115],v[148:151],v[112:115]
T a[112:113],v24,3072
global_load_lds_dwordx4 v21, s[82:83] offset:3072
M v[84:87],a[116:119],v[144:147],v[84:87]
T a[114:115],v24,11264
M v[116:119],a[116:119],v[148:151],v[116:119]
T a[116:117],v25,3072
M v[88:91],a[120:123],v[144:147],v[88:91]
T a[118:119],v25,11264
M v[120:123],a[120:123],v[148:151],v[120:123]
T a[120:121],v26,3072
global_load_dwordx4 v[252:255], v17, s[80:81] offset:1024 sc1
M v[92:95],a[124:127],v[144:147],v[92:95]
T a[122:123],v26,11264
M v[124:127],a[124:127],v[148:151],v[124:127]
T a[124:125],v27,3072
s_waitcnt vmcnt(41) lgkmcnt(7)
s_barrier
M v[64:67],a[96:99],v[152:155],v[64:67]
T a[126:127],v27,11264
M v[96:99],a[96:99],v[156:159],v[96:99]
T a[96:97],v24,16384
M v[68:71],a[100:103],v[152:155],v[68:71]
T a[98:99],v24,24576
M v[100:103],a[100:103],v[156:159],v[100:103]
T a[100:101],v25,16384
M v[72:75],a[104:107],v[152:155],v[72:75]
T a[102:103],v25,24576
M v[104:107],a[104:107],v[156:159],v[104:107]
T a[104:105],v26,16384
M v[76:79],a[108:111],v[152:155],v[76:79]
T a[106:107],v26,24576
M v[108:111],a[108:111],v[156:159],v[108:111]
T a[108:109],v27,16384
s_waitcnt lgkmcnt(7)
M v[80:83],a[112:115],v[152:155],v[80:83]
T a[110:111],v27,24576
M v[112:115],a[112:115],v[156:159],v[112:115]
T a[112:113],v24,17408
M v[84:87],a[116:119],v[152:155],v[84:87]
T a[114:115],v24,25600
M v[116:119],a[116:119],v[156:159],v[116:119]
T a[116:117],v25,17408
M v[88:91],a[120:123],v[152:155],v[88:91]
T a[118:119],v25,25600
M v[120:123],a[120:123],v[156:159],v[120:123]
T a[120:121],v26,17408
M v[92:95],a[124:127],v[152:155],v[92:95]
T a[122:123],v26,25600
M v[124:127],a[124:127],v[156:159],v[124:127]
T a[124:125],v27,17408
T a[126:127],v27,25600
s_waitcnt lgkmcnt(8)
M v[64:67],a[96:99],v[160:163],v[64:67]
M v[96:99],a[96:99],v[164:167],v[96:99]
T a[96:97],v24,18432
M v[68:71],a[100:103],v[160:163],v[68:71]
T a[98:99],v24,26624
M v[100:103],a[100:103],v[164:167],v[100:103]
T a[100:101],v25,18432
M v[72:75],a[104:107],v[160:163],v[72:75]
T a[102:103],v25,26624
M v[104:107],a[104:107],v[164:167],v[104:107]
T a[104:105],v26,18432
M v[76:79],a[108:111],v[160:163],v[76:79]
T a[106:107],v26,26624
M v[108:111],a[108:111],v[164:167],v[108:111]
T a[108:109],v27,18432
s_waitcnt lgkmcnt(7)
M v[80:83],a[112:115],v[160:163],v[80:83]
T a[110:111],v27,26624
M v[112:115],a[112:115],v[164:167],v[112:115]
T a[112:113],v24,19456
M v[84:87],a[116:119],v[160:163],v[84:87]
T a[114:115],v24,27648
M v[116:119],a[116:119],v[164:167],v[116:119]
T a[116:117],v25,19456
M v[88:91],a[120:123],v[160:163],v[88:91]
T a[118:119],v25,27648
M v[120:123],a[120:123],v[164:167],v[120:123]
T a[120:121],v26,19456
M v[92:95],a[124:127],v[160:163],v[92:95]
T a[122:123],v26,27648
M v[124:127],a[124:127],v[164:167],v[124:127]
T a[124:125],v27,19456
s_waitcnt vmcnt(33) lgkmcnt(7)
s_barrier
M v[64:67],a[96:99],v[168:171],v[64:67]
T a[126:127],v27,27648
M v[96:99],a[96:99],v[172:175],v[96:99]
T a[96:97],v28,16384
M v[68:71],a[100:103],v[168:171],v[68:71]
T a[98:99],v28,24576
M v[100:103],a[100:103],v[172:175],v[100:103]
T a[100:101],v29,16384
M v[72:75],a[104:107],v[168:171],v[72:75]
T a[102:103],v29,24576
M v[104:107],a[104:107],v[172:175],v[104:107]
T a[104:105],v30,16384
M v[76:79],a[108:111],v[168:171],v[76:79]
T a[106:107],v30,24576
M v[108:111],a[108:111],v[172:175],v[108:111]
T a[108:109],v31,16384
s_waitcnt lgkmcnt(7)
M v[80:83],a[112:115],v[168:171],v[80:83]
T a[110:111],v31,24576
M v[112:115],a[112:115],v[172:175],v[112:115]
T a[112:113],v28,17408
M v[84:87],a[116:119],v[168:171],v[84:87]
T a[114:115],v28,25600
M v[116:119],a[116:119],v[172:175],v[116:119]
T a[116:117],v29,17408
M v[88:91],a[120:123],v[168:171],v[88:91]
T a[118:119],v29,25600
M v[120:123],a[120:123],v[172:175],v[120:123]
T a[120:121],v30,17408
M v[92:95],a[124:127],v[168:171],v[92:95]
T a[122:123],v30,25600
M v[124:127],a[124:127],v[172:175],v[124:127]
T a[124:125],v31,17408
T a[126:127],v31,25600
s_waitcnt lgkmcnt(8)
M v[64:67],a[96:99],v[176:179],v[64:67]
M v[96:99],a[96:99],v[180:183],v[96:99]
T a[96:97],v28,18432
M v[68:71],a[100:103],v[176:179],v[68:71]
T a[98:99],v28,26624
M v[100:103],a[100:103],v[180:183],v[100:103]
T a[100:101],v29,18432
M v[72:75],a[104:107],v[176:179],v[72:75]
T a[102:103],v29,26624
M v[104:107],a[104:107],v[180:183],v[104:107]
T a[104:105],v30,18432
M v[76:79],a[108:111],v[176:179],v[76:79]
T a[106:107],v30,26624
M v[108:111],a[108:111],v[180:183],v[108:111]
T a[108:109],v31,18432
s_waitcnt lgkmcnt(7)
M v[80:83],a[112:115],v[176:179],v[80:83]
T a[110:111],v31,26624
M v[112:115],a[112:115],v[180:183],v[112:115]
T a[112:113],v28,19456
M v[84:87],a[116:119],v[176:179],v[84:87]
T a[114:115],v28,27648
M v[116:119],a[116:119],v[180:183],v[116:119]
T a[116:117],v29,19456
M v[88:91],a[120:123],v[176:179],v[88:91]
T a[118:119],v29,27648
M v[120:123],a[120:123],v[180:183],v[120:123]
T a[120:121],v30,19456
M v[92:95],a[124:127],v[176:179],v[92:95]
T a[122:123],v30,27648
M v[124:127],a[124:127],v[180:183],v[124:127]
T a[124:125],v31,19456
s_waitcnt vmcnt(25) lgkmcnt(7)
s_barrier
M v[64:67],a[96:99],v[184:187],v[64:67]
T a[126:127],v31,27648
M v[96:99],a[96:99],v[188:191],v[96:99]
T a[96:97],v24,32768
M v[68:71],a[100:103],v[184:187],v[68:71]
T a[98:99],v24,40960
M v[100:103],a[100:103],v[188:191],v[100:103]
T a[100:101],v25,32768
M v[72:75],a[104:107],v[184:187],v[72:75]
T a[102:103],v25,40960
M v[104:107],a[104:107],v[188:191],v[104:107]
T a[104:105],v26,32768
M v[76:79],a[108:111],v[184:187],v[76:79]
T a[106:107],v26,40960
M v[108:111],a[108:111],v[188:191],v[108:111]
T a[108:109],v27,32768
s_waitcnt lgkmcnt(7)
M v[80:83],a[112:115],v[184:187],v[80:83]
T a[110:111],v27,40960
M v[112:115],a[112:115],v[188:191],v[112:115]
T a[112:113],v24,33792
M v[84:87],a[116:119],v[184:187],v[84:87]
T a[114:115],v24,41984
M v[116:119],a[116:119],v[188:191],v[116:119]
T a[116:117],v25,33792
M v[88:91],a[120:123],v[184:187],v[88:91]
T a[118:119],v25,41984
M v[120:123],a[120:123],v[188:191],v[120:123]
T a[120:121],v26,33792
M v[92:95],a[124:127],v[184:187],v[92:95]
T a[122:123],v26,41984
M v[124:127],a[124:127],v[188:191],v[124:127]
T a[124:125],v27,33792
T a[126:127],v27,41984
s_waitcnt lgkmcnt(8)
M v[64:67],a[96:99],v[192:195],v[64:67]
M v[96:99],a[96:99],v[196:199],v[96:99]
T a[96:97],v24,34816
M v[68:71],a[100:103],v[192:195],v[68:71]
T a[98:99],v24,43008
M v[100:103],a[100:103],v[196:199],v[100:103]
T a[100:101],v25,34816
M v[72:75],a[104:107],v[192:195],v[72:75]
T a[102:103],v25,43008
M v[104:107],a[104:107],v[196:199],v[104:107]
T a[104:105],v26,34816
M v[76:79],a[108:111],v[192:195],v[76:79]
T a[106:107],v26,43008
M v[108:111],a[108:111],v[196:199],v[108:111]
T a[108:109],v27,34816
s_waitcnt lgkmcnt(7)
M v[80:83],a[112:115],v[192:195],v[80:83]
T a[110:111],v27,43008
M v[112:115],a[112:115],v[196:199],v[112:115]
T a[112:113],v24,35840
M v[84:87],a[116:119],v[192:195],v[84:87]
T a[114:115],v24,44032
M v[116:119],a[116:119],v[196:199],v[116:119]
T a[116:117],v25,35840
M v[88:91],a[120:123],v[192:195],v[88:91]
T a[118:119],v25,44032
M v[120:123],a[120:123],v[196:199],v[120:123]
T a[120:121],v26,35840
M v[92:95],a[124:127],v[192:195],v[92:95]
T a[122:123],v26,44032
M v[124:127],a[124:127],v[196:199],v[124:127]
T a[124:125],v27,35840
s_waitcnt vmcnt(17) lgkmcnt(7)
s_barrier
M v[64:67],a[96:99],v[200:203],v[64:67]
T a[126:127],v27,44032
M v[96:99],a[96:99],v[204:207],v[96:99]
T a[96:97],v24,49152
M v[68:71],a[100:103],v[200:203],v[68:71]
T a[98:99],v24,57344
M v[100:103],a[100:103],v[204:207],v[100:103]
T a[100:101],v25,49152
M v[72:75],a[104:107],v[200:203],v[72:75]
T a[102:103],v25,57344
M v[104:107],a[104:107],v[204:207],v[104:107]
T a[104:105],v26,49152
M v[76:79],a[108:111],v[200:203],v[76:79]
T a[106:107],v26,57344
M v[108:111],a[108:111],v[204:207],v[108:111]
T a[108:109],v27,49152
s_waitcnt lgkmcnt(7)
M v[80:83],a[112:115],v[200:203],v[80:83]
T a[110:111],v27,57344
M v[112:115],a[112:115],v[204:207],v[112:115]
T a[112:113],v24,50176
M v[84:87],a[116:119],v[200:203],v[84:87]
T a[114:115],v24,58368
M v[116:119],a[116:119],v[204:207],v[116:119]
T a[116:117],v25,50176
M v[88:91],a[120:123],v[200:203],v[88:91]
T a[118:119],v25,58368
M v[120:123],a[120:123],v[204:207],v[120:123]
T a[120:121],v26,50176
M v[92:95],a[124:127],v[200:203],v[92:95]
T a[122:123],v26,58368
M v[124:127],a[124:127],v[204:207],v[124:127]
T a[124:125],v27,50176
T a[126:127],v27,58368
s_waitcnt lgkmcnt(8)
M v[64:67],a[96:99],v[208:211],v[64:67]
M v[96:99],a[96:99],v[212:215],v[96:99]
T a[96:97],v24,51200
M v[68:71],a[100:103],v[208:211],v[68:71]
T a[98:99],v24,59392
M v[100:103],a[100:103],v[212:215],v[100:103]
T a[100:101],v25,51200
M v[72:75],a[104:107],v[208:211],v[72:75]
T a[102:103],v25,59392
M v[104:107],a[104:107],v[212:215],v[104:107]
T a[104:105],v26,51200
M v[76:79],a[108:111],v[208:211],v[76:79]
T a[106:107],v26,59392
M v[108:111],a[108:111],v[212:215],v[108:111]
T a[108:109],v27,51200
s_waitcnt lgkmcnt(7)
M v[80:83],a[112:115],v[208:211],v[80:83]
T a[110:111],v27,59392
M v[112:115],a[112:115],v[212:215],v[112:115]
T a[112:113],v24,52224
M v[84:87],a[116:119],v[208:211],v[84:87]
T a[114:115],v24,60416
M v[116:119],a[116:119],v[212:215],v[116:119]
T a[116:117],v25,52224
M v[88:91],a[120:123],v[208:211],v[88:91]
T a[118:119],v25,60416
M v[120:123],a[120:123],v[212:215],v[120:123]
T a[120:121],v26,52224
M v[92:95],a[124:127],v[208:211],v[92:95]
T a[122:123],v26,60416
M v[124:127],a[124:127],v[212:215],v[124:127]
T a[124:125],v27,52224
s_waitcnt vmcnt(8) lgkmcnt(7)
s_barrier
M v[64:67],a[96:99],v[216:219],v[64:67]
T a[126:127],v27,60416
M v[96:99],a[96:99],v[220:223],v[96:99]
T a[96:97],v28,32768
M v[68:71],a[100:103],v[216:219],v[68:71]
T a[98:99],v28,40960
M v[100:103],a[100:103],v[220:223],v[100:103]
T a[100:101],v29,32768
M v[72:75],a[104:107],v[216:219],v[72:75]
T a[102:103],v29,40960
M v[104:107],a[104:107],v[220:223],v[104:107]
T a[104:105],v30,32768
M v[76:79],a[108:111],v[216:219],v[76:79]
T a[106:107],v30,40960
M v[108:111],a[108:111],v[220:223],v[108:111]
T a[108:109],v31,32768
s_waitcnt lgkmcnt(7)
M v[80:83],a[112:115],v[216:219],v[80:83]
T a[110:111],v31,40960
M v[112:115],a[112:115],v[220:223],v[112:115]
T a[112:113],v28,33792
M v[84:87],a[116:119],v[216:219],v[84:87]
T a[114:115],v28,41984
M v[116:119],a[116:119],v[220:223],v[116:119]
T a[116:117],v29,33792
M v[88:91],a[120:123],v[216:219],v[88:91]
T a[118:119],v29,41984
M v[120:123],a[120:123],v[220:223],v[120:123]
T a[120:121],v30,33792
M v[92:95],a[124:127],v[216:219],v[92:95]
T a[122:123],v30,41984
M v[124:127],a[124:127],v[220:223],v[124:127]
T a[124:125],v31,33792
T a[126:127],v31,41984
s_waitcnt lgkmcnt(8)
M v[64:67],a[96:99],v[224:227],v[64:67]
M v[96:99],a[96:99],v[228:231],v[96:99]
T a[96:97],v28,34816
M v[68:71],a[100:103],v[224:227],v[68:71]
T a[98:99],v28,43008
M v[100:103],a[100:103],v[228:231],v[100:103]
T a[100:101],v29,34816
M v[72:75],a[104:107],v[224:227],v[72:75]
T a[102:103],v29,43008
M v[104:107],a[104:107],v[228:231],v[104:107]
T a[104:105],v30,34816
M v[76:79],a[108:111],v[224:227],v[76:79]
T a[106:107],v30,43008
M v[108:111],a[108:111],v[228:231],v[108:111]
T a[108:109],v31,34816
s_waitcnt lgkmcnt(7)
M v[80:83],a[112:115],v[224:227],v[80:83]
T a[110:111],v31,43008
M v[112:115],a[112:115],v[228:231],v[112:115]
T a[112:113],v28,35840
M v[84:87],a[116:119],v[224:227],v[84:87]
T a[114:115],v28,44032
M v[116:119],a[116:119],v[228:231],v[116:119]
T a[116:117],v29,35840
M v[88:91],a[120:123],v[224:227],v[88:91]
T a[118:119],v29,44032
M v[120:123],a[120:123],v[228:231],v[120:123]
T a[120:121],v30,35840
M v[92:95],a[124:127],v[224:227],v[92:95]
T a[122:123],v30,44032
M v[124:127],a[124:127],v[228:231],v[124:127]
T a[124:125],v31,35840
s_waitcnt vmcnt(0) lgkmcnt(7)
s_barrier
M v[64:67],a[96:99],v[232:235],v[64:67]
T a[126:127],v31,44032
M v[96:99],a[96:99],v[236:239],v[96:99]
T a[96:97],v28,0
M v[68:71],a[100:103],v[232:235],v[68:71]
T a[98:99],v28,8192
M v[100:103],a[100:103],v[236:239],v[100:103]
T a[100:101],v29,0
M v[72:75],a[104:107],v[232:235],v[72:75]
T a[102:103],v29,8192
M v[104:107],a[104:107],v[236:239],v[104:107]
T a[104:105],v30,0
M v[76:79],a[108:111],v[232:235],v[76:79]
T a[106:107],v30,8192
M v[108:111],a[108:111],v[236:239],v[108:111]
T a[108:109],v31,0
s_waitcnt lgkmcnt(7)
M v[80:83],a[112:115],v[232:235],v[80:83]
T a[110:111],v31,8192
M v[112:115],a[112:115],v[236:239],v[112:115]
T a[112:113],v28,1024
M v[84:87],a[116:119],v[232:235],v[84:87]
T a[114:115],v28,9216
M v[116:119],a[116:119],v[236:239],v[116:119]
T a[116:117],v29,1024
M v[88:91],a[120:123],v[232:235],v[88:91]
T a[118:119],v29,9216
M v[120:123],a[120:123],v[236:239],v[120:123]
T a[120:121],v30,1024
M v[92:95],a[124:127],v[232:235],v[92:95]
T a[122:123],v30,9216
M v[124:127],a[124:127],v[236:239],v[124:127]
T a[124:125],v31,1024
T a[126:127],v31,9216
s_waitcnt lgkmcnt(8)
M v[64:67],a[96:99],v[240:243],v[64:67]
M v[96:99],a[96:99],v[244:247],v[96:99]
T a[96:97],v28,2048
M v[68:71],a[100:103],v[240:243],v[68:71]
T a[98:99],v28,10240
M v[100:103],a[100:103],v[244:247],v[100:103]
T a[100:101],v29,2048
M v[72:75],a[104:107],v[240:243],v[72:75]
T a[102:103],v29,10240
M v[104:107],a[104:107],v[244:247],v[104:107]
T a[104:105],v30,2048
M v[76:79],a[108:111],v[240:243],v[76:79]
T a[106:107],v30,10240
M v[108:111],a[108:111],v[244:247],v[108:111]
T a[108:109],v31,2048
s_waitcnt lgkmcnt(7)
M v[80:83],a[112:115],v[240:243],v[80:83]
T a[110:111],v31,10240
M v[112:115],a[112:115],v[244:247],v[112:115]
T a[112:113],v28,3072
M v[84:87],a[116:119],v[240:243],v[84:87]
T a[114:115],v28,11264
M v[116:119],a[116:119],v[244:247],v[116:119]
T a[116:117],v29,3072
M v[88:91],a[120:123],v[240:243],v[88:91]
T a[118:119],v29,11264
M v[120:123],a[120:123],v[244:247],v[120:123]
T a[120:121],v30,3072
M v[92:95],a[124:127],v[240:243],v[92:95]
T a[122:123],v30,11264
M v[124:127],a[124:127],v[244:247],v[124:127]
T a[124:125],v31,3072
s_waitcnt lgkmcnt(7)
M v[64:67],a[96:99],v[248:251],v[64:67]
T a[126:127],v31,11264
M v[96:99],a[96:99],v[252:255],v[96:99]
M v[68:71],a[100:103],v[248:251],v[68:71]
M v[100:103],a[100:103],v[252:255],v[100:103]
M v[72:75],a[104:107],v[248:251],v[72:75]
M v[104:107],a[104:107],v[252:255],v[104:107]
M v[76:79],a[108:111],v[248:251],v[76:79]
M v[108:111],a[108:111],v[252:255],v[108:111]
s_waitcnt lgkmcnt(0)
M v[80:83],a[112:115],v[248:251],v[80:83]
M v[112:115],a[112:115],v[252:255],v[112:115]
M v[84:87],a[116:119],v[248:251],v[84:87]
M v[116:119],a[116:119],v[252:255],v[116:119]
M v[88:91],a[120:123],v[248:251],v[88:91]
M v[120:123],a[120:123],v[252:255],v[120:123]
M v[92:95],a[124:127],v[248:251],v[92:95]
M v[124:127],a[124:127],v[252:255],v[124:127]
s_nop 7
global_store_dwordx4 v16, v[64:67], s[94:95] offset:0 sc1
global_store_dwordx4 v16, v[68:71], s[94:95] offset:1024 sc1
global_store_dwordx4 v16, v[72:75], s[94:95] offset:2048 sc1
global_store_dwordx4 v16, v[76:79], s[94:95] offset:3072 sc1
s_add_u32 s94, s94, 4096
s_addc_u32 s95, s95, 0
global_store_dwordx4 v16, v[80:83], s[94:95] offset:0 sc1
global_store_dwordx4 v16, v[84:87], s[94:95] offset:1024 sc1
global_store_dwordx4 v16, v[88:91], s[94:95] offset:2048 sc1
global_store_dwordx4 v16, v[92:95], s[94:95] offset:3072 sc1
s_add_u32 s94, s94, 4096
s_addc_u32 s95, s95, 0
global_store_dwordx4 v16, v[96:99], s[94:95] offset:0 sc1
global_store_dwordx4 v16, v[100:103], s[94:95] offset:1024 sc1
global_store_dwordx4 v16, v[104:107], s[94:95] offset:2048 sc1
global_store_dwordx4 v16, v[108:111], s[94:95] offset:3072 sc1
s_add_u32 s94, s94, 4096
s_addc_u32 s95, s95, 0
global_store_dwordx4 v16, v[112:115], s[94:95] offset:0 sc1
global_store_dwordx4 v16, v[116:119], s[94:95] offset:1024 sc1
global_store_dwordx4 v16, v[120:123], s[94:95] offset:2048 sc1
global_store_dwordx4 v16, v[124:127], s[94:95] offset:3072 sc1
s_waitcnt vmcnt(0)
s_barrier
v_mov_b32 v37, s98
s_cmp_lg_u32 s99, 0
s_cbranch_scc1 Lg2nf_%=
s_mov_b64 exec, 1
global_store_dword v38, v37, s[76:77] sc1
s_mov_b64 exec, -1
Lg2nf_%=:
s_cmp_lg_u32 s27, 1
s_cbranch_scc1 Lgud_%=
s_cmp_lg_u32 s99, 0
s_cbranch_scc1 Lgud_%=
s_mov_b32 s65, 0x100000
Lgul_%=:
global_load_dwordx4 v[24:27], v16, s[28:29] sc1
global_load_dwordx4 v[28:31], v16, s[28:29] offset:1024 sc1
s_waitcnt vmcnt(0)
v_xor_b32 v24, s98, v24
v_xor_b32 v25, s98, v25
v_xor_b32 v26, s98, v26
v_xor_b32 v27, s98, v27
v_xor_b32 v28, s98, v28
v_xor_b32 v29, s98, v29
v_xor_b32 v30, s98, v30
v_xor_b32 v31, s98, v31
v_or3_b32 v24, v24, v25, v26
v_or3_b32 v27, v27, v28, v29
v_or3_b32 v24, v24, v30, v31
v_or_b32 v24, v24, v27
v_cmp_ne_u32 vcc, 0, v24
s_cbranch_vccz Lguk_%=
s_sub_u32 s65, s65, 1
s_cmp_lg_u32 s65, 0
s_cbranch_scc1 Lgul_%=
s_branch Lgud_%=
Lguk_%=:
v_mov_b32 v24, 0
v_mov_b32 v25, s98
s_mov_b64 exec, 1
global_store_dword v24, v25, s[24:25] sc1
s_mov_b64 exec, -1
Lgud_%=:
Lend_%=:
Lcode1_%=:
.purgem M
.purgem T
.purgem X
)ASM"
    : "+{v28}"(GV), "+{s[16:31]}"(P8), "+{s[8:15]}"(P7), "+{s[40:55]}"(P1), "+{s[56:63]}"(P2), "+{s[76:79]}"(P6), "+{s[80:95]}"(P3), "+{s[96:99]}"(P4), "+{v[36:39]}"(PFV), "+{v[6:7]}"(ST), "+{v[8:23]}"(PV)
    :
    : "memory","scc","vcc","v3","v4","v5","s64","s65","s66","s67","s68","s69","s70","s71","s72","s73","s74","s75","v24","v25","v26","v27","v29","v30","v31","v32","v33","v34","v35","v40","v41","v42","v43","v44","v45","v46","v47","v48","v49","v50","v51","v52","v53","v54","v55","v56","v57","v58","v59","v60","v61","v62","v63","v64","v65","v66","v67","v68","v69","v70","v71","v72","v73","v74","v75","v76","v77","v78","v79","v80","v81","v82","v83","v84","v85","v86","v87","v88","v89","v90","v91","v92","v93","v94","v95","v96","v97","v98","v99","v100","v101","v102","v103","v104","v105","v106","v107","v108","v109","v110","v111","v112","v113","v114","v115","v116","v117","v118","v119","v120","v121","v122","v123","v124","v125","v126","v127","v128","v129","v130","v131","v132","v133","v134","v135","v136","v137","v138","v139","v140","v141","v142","v143","v144","v145","v146","v147","v148","v149","v150","v151","v152","v153","v154","v155","v156","v157","v158","v159","v160","v161","v162","v163","v164","v165","v166","v167","v168","v169","v170","v171","v172","v173","v174","v175","v176","v177","v178","v179","v180","v181","v182","v183","v184","v185","v186","v187","v188","v189","v190","v191","v192","v193","v194","v195","v196","v197","v198","v199","v200","v201","v202","v203","v204","v205","v206","v207","v208","v209","v210","v211","v212","v213","v214","v215","v216","v217","v218","v219","v220","v221","v222","v223","v224","v225","v226","v227","v228","v229","v230","v231","v232","v233","v234","v235","v236","v237","v238","v239","v240","v241","v242","v243","v244","v245","v246","v247","v248","v249","v250","v251","v252","v253","v254","v255","a0","a1","a2","a3","a4","a5","a6","a7","a8","a9","a10","a11","a12","a13","a14","a15","a16","a17","a18","a19","a20","a21","a22","a23","a24","a25","a26","a27","a28","a29","a30","a31","a32","a33","a34","a35","a36","a37","a38","a39","a40","a41","a42","a43","a44","a45","a46","a47","a48","a49","a50","a51","a52","a53","a54","a55","a56","a57","a58","a59","a60","a61","a62","a63","a64","a65","a66","a67","a68","a69","a70","a71","a72","a73","a74","a75","a76","a77","a78","a79","a80","a81","a82","a83","a84","a85","a86","a87","a88","a89","a90","a91","a92","a93","a94","a95","a96","a97","a98","a99","a100","a101","a102","a103","a104","a105","a106","a107","a108","a109","a110","a111","a112","a113","a114","a115","a116","a117","a118","a119","a120","a121","a122","a123","a124","a125","a126","a127","a128","a129","a130","a131","a132","a133","a134","a135","a136","a137","a138","a139","a140","a141","a142","a143","a144","a145","a146","a147","a148","a149","a150","a151","a152","a153","a154","a155","a156","a157","a158","a159","a160","a161","a162","a163","a164","a165","a166","a167","a168","a169","a170","a171","a172","a173","a174","a175","a176","a177","a178","a179","a180","a181","a182","a183","a184","a185","a186","a187","a188","a189","a190","a191","a192","a193","a194","a195","a196","a197","a198","a199","a200","a201","a202","a203","a204","a205","a206","a207","a208","a209","a210","a211","a212","a213","a214","a215","a216","a217","a218","a219","a220","a221","a222","a223","a224","a225","a226","a227","a228","a229","a230","a231","a232","a233","a234","a235","a236","a237","a238","a239","a240","a241","a242","a243","a244","a245","a246","a247","a248","a249","a250","a251","a252","a253","a254","a255");

}

extern "C" void kernel_launch(void* const* d_in, const int* in_sizes, int n_in, void* d_out, int out_size, void* d_ws, size_t ws_size, hipStream_t stream){
  (void)hipFuncSetAttribute((const void*)fused_kernel,hipFuncAttributeMaxDynamicSharedMemorySize,163840);
  fused_kernel<<<256,256,163840,stream>>>((const char*)d_in[0],(char*)d_ws,(char*)d_out,(const char*)d_in[1],(const char*)d_in[4],(const char*)d_in[5],(const char*)d_in[2],(const char*)d_in[6]);
}
```
